```python
import torch

def _softmax(x, axis=-1):
    return torch.softmax(x, dim=axis)
def _gelu(x):
    return torch.nn.functional.gelu(x, approximate="tanh")
def _silu(x):
    return torch.nn.functional.silu(x)
def _sigmoid(x):
    return torch.sigmoid(x)
def _layernorm(x, axis=-1, eps=1e-5):
    m = x.mean(dim=axis, keepdim=True)
    v = x.var(dim=axis, keepdim=True, unbiased=False)
    return (x - m) / torch.sqrt(v + eps)
def _scatter_add(tgt, idx, src, axis=0):
    return tgt.clone().index_add_(axis, idx, src)
def _segment_sum(src, seg, n):
    out = torch.zeros((n,) + src.shape[1:], dtype=src.dtype)
    return out.index_add_(0, seg, src)
def _pad(x, pw):
    flat = tuple(v for pair in reversed(pw) for v in pair)
    return torch.nn.functional.pad(x, flat)

kernel_name = 'sequential_preLN__ssm_conv_h3__dense_activation_silu__prompt'

def setup_inputs():

    _w = (lambda a: (lambda t: t.to(torch.bfloat16) if t.is_floating_point() else t)(torch.as_tensor(a)))
    def _A_setup():
        B, S, D = 1, 2048, 512
        ks = 4
        return {
            "q": _w((lambda *s: torch.randn(*s, dtype=torch.bfloat16) * 0.05)(B, S, D)),
            "k": _w((lambda *s: torch.randn(*s, dtype=torch.bfloat16) * 0.2)(B, S, D)),
            "v": _w((lambda *s: torch.randn(*s, dtype=torch.bfloat16) * 0.2)(B, S, D)),
            "shift_k": _w((lambda *s: torch.randn(*s, dtype=torch.bfloat16) * 0.2)(D, ks) * 0.5),
            "ssm_k": _w((lambda *s: torch.randn(*s, dtype=torch.bfloat16) * 0.8)(D, S) * (1.0 / S ** 0.5)),
            "ssm_D": _w((lambda *s: torch.randn(*s, dtype=torch.bfloat16) * 0.2)(D)),
        }
    _A_in = _A_setup()

    def _B_setup():
        B, S, D, F = 1, 2048, 512, 1376
        return {
            "x": _w((lambda *s: torch.randn(*s, dtype=torch.bfloat16) * 0.05)(B, S, D)),
            "w1": _w((lambda *s: torch.randn(*s, dtype=torch.bfloat16) * 0.8)(D, F) * (D ** -0.5)),
            "w2": _w((lambda *s: torch.randn(*s, dtype=torch.bfloat16) * 0.1)(F, D) * (F ** -0.5)),
        }
    _B_in = _B_setup()

    return {
        "A_q": _A_in["q"],
        "A_k": _A_in["k"],
        "A_v": _A_in["v"],
        "A_shift_k": _A_in["shift_k"],
        "A_ssm_k": _A_in["ssm_k"],
        "A_ssm_D": _A_in["ssm_D"],
        "B_x": _B_in["x"],
        "B_w1": _B_in["w1"],
        "B_w2": _B_in["w2"],
    }

def reference(A_q, A_k, A_v, A_shift_k, A_ssm_k, A_ssm_D, B_x, B_w1, B_w2):
    def _A_ref(q, k, v, shift_k, ssm_k, ssm_D):
        B, S, D = q.shape
        ks = shift_k.shape[-1]
        k_pad = _pad(k, ((0, 0), (ks - 1, 0), (0, 0)))
        k_shift = torch.zeros_like(k)
        for t in range(ks):
            k_shift = k_shift + k_pad[:, t:t + S, :] * shift_k[:, ks - 1 - t][None, None, :]
        kv = k_shift * v
        L = 2 * S
        kv_pad = _pad(kv, ((0, 0), (0, S), (0, 0)))
        kf_pad = _pad(ssm_k, ((0, 0), (0, S)))
        kv_f = (lambda x, axes=(-2,-1): torch.fft.rfft2(x.float(), dim=axes))(kv_pad[:, None, :, :], axes=(1, 2))
        kf_f = (lambda x, axes=(-2,-1): torch.fft.rfft2(x.float(), dim=axes))(kf_pad[:, None, :], axes=(1, 2))
        kf_f = (lambda x, *d: x.permute(*d))(kf_f, 1, 2, 0)
        y_f = kv_f * kf_f[None, :, :, :]
        y = (lambda x, s, axes=(-2,-1): torch.fft.irfft2(x, s=s, dim=axes).to(torch.bfloat16))(y_f, (1, L), axes=(1, 2))[:, 0, :S, :]
        y = y + kv * ssm_D[None, None, :]
        return q * y

    def _B_ref(x, w1, w2):
        h = torch.einsum("bsd,df->bsf", x, w1)
        h = _silu(h)
        out = torch.einsum("bsf,fd->bsd", h, w2)
        return out

    x = A_q
    h = x + _A_ref(_layernorm(x, axis=-1), A_k, A_v, A_shift_k, A_ssm_k, A_ssm_D)
    h2 = h + _B_ref(_layernorm(h, axis=-1), B_w1, B_w2)
    return h2
```

```cpp
#include <hip/hip_runtime.h>
#include <hip/hip_bf16.h>
#include <stdint.h>

typedef unsigned short u16;
typedef short bf16x8 __attribute__((ext_vector_type(8)));
typedef short s16x4 __attribute__((ext_vector_type(4)));
typedef float f32x4 __attribute__((ext_vector_type(4)));
typedef float f32x2 __attribute__((ext_vector_type(2)));
typedef unsigned u32x4 __attribute__((ext_vector_type(4)));
typedef unsigned u32x2 __attribute__((ext_vector_type(2)));
typedef __bf16 bf16x2_t __attribute__((ext_vector_type(2)));
typedef __bf16 bf16x8_t __attribute__((ext_vector_type(8)));
typedef short v4i16_t __attribute__((ext_vector_type(4)));
#define LAS __attribute__((address_space(3)))

#define S_LEN 2048
#define D_DIM 512
#define F_DIM 1376

#define WAIT_V(n) asm volatile("s_waitcnt vmcnt(%0)" ::"n"(n) : "memory")
#define WAIT_L0() asm volatile("s_waitcnt lgkmcnt(0)" ::: "memory")
#define RAW_BAR() do { asm volatile("" ::: "memory"); __builtin_amdgcn_s_barrier(); asm volatile("" ::: "memory"); } while (0)

__device__ __forceinline__ float bf_lo(unsigned x) { return __uint_as_float(x << 16); }
__device__ __forceinline__ float bf_hi(unsigned x) { return __uint_as_float(x & 0xffff0000u); }
__device__ __forceinline__ unsigned pk_bf16(float lo, float hi) {
  bf16x2_t v; v.x = (__bf16)lo; v.y = (__bf16)hi;
  return __builtin_bit_cast(unsigned, v);
}
__device__ __forceinline__ float rbf(float x) { return __uint_as_float(((unsigned)__builtin_bit_cast(u16, (__bf16)x)) << 16); }
__device__ __forceinline__ s16x4 tr_read(const LAS char* p) {
  return __builtin_bit_cast(s16x4, __builtin_amdgcn_ds_read_tr16_b64_v4i16((LAS v4i16_t*)p));
}
__device__ __forceinline__ bf16x8_t cat8(s16x4 a, s16x4 b) {
  bf16x8 r = __builtin_shufflevector(a, b, 0, 1, 2, 3, 4, 5, 6, 7);
  return __builtin_bit_cast(bf16x8_t, r);
}
__device__ __forceinline__ f32x4 mfma16(bf16x8_t a, bf16x8_t b, f32x4 c) {
  return __builtin_amdgcn_mfma_f32_16x16x32_bf16(a, b, c, 0, 0, 0);
}
__device__ __forceinline__ f32x4 sel4(bool c, f32x4 a, f32x4 b) {
  f32x4 r; r[0] = c ? a[0] : b[0]; r[1] = c ? a[1] : b[1]; r[2] = c ? a[2] : b[2]; r[3] = c ? a[3] : b[3]; return r;
}
__device__ __forceinline__ void glds16(const void* g, LAS char* l) {
  __builtin_amdgcn_global_load_lds((const __attribute__((address_space(1))) unsigned*)g, (LAS unsigned*)l, 16, 0, 0);
}

__global__ __launch_bounds__(256) void k0_prep(const u16* __restrict__ q, const u16* __restrict__ k, const u16* __restrict__ v,
                                               const u16* __restrict__ shk, const u16* __restrict__ w1,
                                               u16* __restrict__ kvT, u16* __restrict__ qT, f32x2* __restrict__ statq,
                                               float* __restrict__ cs1) {
  __shared__ __attribute__((aligned(16))) char sm[2 * 8192 + 64 * 33 * 4];
  const int b = blockIdx.x, t = threadIdx.x;
  const int cg = b & 7, rg = b >> 3;
  const int cc = t & 7, rp = t >> 3;
  const int d0 = cg * 64 + cc * 8;
  const int r0 = rg * 64 + rp * 2;
  u32x4 kk[5], vv[2], qq[2], ww[4];
#pragma unroll
  for (int i = 0; i < 5; ++i) {
    int r = r0 - 3 + i;
    int rc = r < 0 ? 0 : r;
    kk[i] = *(const u32x4*)(k + (size_t)rc * D_DIM + d0);
    if (r < 0) kk[i] = (u32x4){0, 0, 0, 0};
  }
#pragma unroll
  for (int i = 0; i < 2; ++i) {
    vv[i] = *(const u32x4*)(v + (size_t)(r0 + i) * D_DIM + d0);
    qq[i] = *(const u32x4*)(q + (size_t)(r0 + i) * D_DIM + d0);
  }
#pragma unroll
  for (int i = 0; i < 4; ++i) ww[i] = *(const u32x4*)(shk + (size_t)d0 * 4 + i * 8);
  const int srow = b * 8 + (t >> 5), sl = t & 31;
  u32x4 sq0 = *(const u32x4*)(q + (size_t)srow * D_DIM + sl * 8);
  u32x4 sq1 = *(const u32x4*)(q + (size_t)srow * D_DIM + 256 + sl * 8);
  u32x4 cw[8];
  const bool do_cs = b < F_DIM / 32;
  if (do_cs) {
#pragma unroll
    for (int i = 0; i < 8; ++i) cw[i] = *(const u32x4*)(w1 + (size_t)((t >> 2) + 64 * i) * F_DIM + b * 32 + (t & 3) * 8);
  }
  unsigned kvp[8], qp[8];
#pragma unroll
  for (int e = 0; e < 8; ++e) {
    unsigned wa = ww[e >> 1][(e & 1) * 2], wb = ww[e >> 1][(e & 1) * 2 + 1];
    float w0 = bf_lo(wa), w1_ = bf_hi(wa), w2 = bf_lo(wb), w3 = bf_hi(wb);
    float kx[5];
#pragma unroll
    for (int i = 0; i < 5; ++i) kx[i] = (e & 1) ? bf_hi(kk[i][e >> 1]) : bf_lo(kk[i][e >> 1]);
    float v0 = (e & 1) ? bf_hi(vv[0][e >> 1]) : bf_lo(vv[0][e >> 1]);
    float v1 = (e & 1) ? bf_hi(vv[1][e >> 1]) : bf_lo(vv[1][e >> 1]);
    float s0 = kx[0] * w3 + kx[1] * w2 + kx[2] * w1_ + kx[3] * w0;
    float s1 = kx[1] * w3 + kx[2] * w2 + kx[3] * w1_ + kx[4] * w0;
    kvp[e] = pk_bf16(rbf(s0) * v0, rbf(s1) * v1);
    unsigned qa = qq[0][e >> 1], qb = qq[1][e >> 1];
    qp[e] = (e & 1) ? ((qa >> 16) | (qb & 0xffff0000u)) : ((qa & 0xffffu) | (qb << 16));
  }
#pragma unroll
  for (int e = 0; e < 8; ++e) {
    int ch = cc * 8 + e;
    int off = ch * 128 + (((rp >> 2) ^ cc) << 4) + (rp & 3) * 4;
    *(unsigned*)(sm + off) = kvp[e];
    *(unsigned*)(sm + 8192 + off) = qp[e];
  }
  {
    float x[16];
#pragma unroll
    for (int i = 0; i < 4; ++i) { x[2 * i] = bf_lo(sq0[i]); x[2 * i + 1] = bf_hi(sq0[i]); x[8 + 2 * i] = bf_lo(sq1[i]); x[9 + 2 * i] = bf_hi(sq1[i]); }
    float s = 0;
#pragma unroll
    for (int i = 0; i < 16; ++i) s += x[i];
#pragma unroll
    for (int m = 16; m >= 1; m >>= 1) s += __shfl_xor(s, m);
    float mean = s * (1.0f / D_DIM);
    float vs = 0;
#pragma unroll
    for (int i = 0; i < 16; ++i) { float dd = x[i] - mean; vs += dd * dd; }
#pragma unroll
    for (int m = 16; m >= 1; m >>= 1) vs += __shfl_xor(vs, m);
    float var = rbf(vs * (1.0f / D_DIM));
    float den = rbf(sqrtf(rbf(var + 1e-5f)));
    if (sl == 0) statq[srow] = (f32x2){rbf(mean), 1.0f / den};
  }
  float* csm = (float*)(sm + 16384);
  if (do_cs) {
    float a[8];
#pragma unroll
    for (int e = 0; e < 8; ++e) a[e] = 0;
#pragma unroll
    for (int i = 0; i < 8; ++i)
#pragma unroll
      for (int j = 0; j < 4; ++j) { a[2 * j] += bf_lo(cw[i][j]); a[2 * j + 1] += bf_hi(cw[i][j]); }
#pragma unroll
    for (int e = 0; e < 8; ++e) csm[(t >> 2) * 33 + (t & 3) * 8 + e] = a[e];
  }
  __syncthreads();
  if (do_cs && t < 32) {
    float s = 0;
    for (int i = 0; i < 64; ++i) s += csm[i * 33 + t];
    cs1[b * 32 + t] = s;
  }
#pragma unroll
  for (int i = 0; i < 2; ++i) {
    int idx = t + 256 * i, ch = idx >> 3, c = idx & 7;
    int off = ch * 128 + ((c ^ (ch >> 3)) << 4);
    u32x4 a = *(const u32x4*)(sm + off), bq = *(const u32x4*)(sm + 8192 + off);
    size_t g = (size_t)(cg * 64 + ch) * S_LEN + rg * 64 + c * 8;
    *(u32x4*)(kvT + g) = a;
    *(u32x4*)(qT + g) = bq;
  }
}

constexpr int X_BYTES = 4160;
constexpr int H_ROW = 1152;
constexpr int H_BYTES = 4 * H_ROW;
constexpr int K1_X = 0, K1_H = 2 * X_BYTES, K1_QD = K1_H + 2 * H_BYTES;
constexpr int K1_CY = K1_QD + 16384;
constexpr int CY_BYTES = (512 + 1) * 16;
constexpr int K1_LDS = K1_CY + 4 * CY_BYTES;

template <int TS>
__device__ __forceinline__ void conv_main(const LAS char* xa, const LAS char* hb, f32x4 (&acc)[4][4]) {
  constexpr int T[4] = {TS == 0 ? 0 : 1, TS == 0 ? 3 : 2, TS == 0 ? 4 : 5, TS == 0 ? 7 : 6};
  constexpr int NA = T[3] + 1;
  bf16x8_t A[8];
#pragma unroll
  for (int a = 0; a < NA; ++a) A[a] = cat8(tr_read(xa + 512 * a), tr_read(xa + 512 * a + 32));
#pragma unroll
  for (int bb = 0; bb < NA; ++bb) {
    bf16x8_t B[4];
#pragma unroll
    for (int p = 0; p < 4; ++p) {
      const LAS char* ad = hb + p * H_ROW + 896 - 128 * bb;
      s16x4 lo = *(const LAS s16x4*)ad, hi = *(const LAS s16x4*)(ad + 8);
      B[p] = cat8(lo, hi);
    }
#pragma unroll
    for (int p = 0; p < 4; ++p)
#pragma unroll
      for (int j = 0; j < 4; ++j)
        if (T[j] >= bb) acc[j][p] = mfma16(A[T[j] - bb], B[p], acc[j][p]);
  }
}

__global__ __launch_bounds__(512) void k1_conv(const u16* __restrict__ kvT, const u16* __restrict__ qT, const f32x2* __restrict__ statq,
                                               const u16* __restrict__ ssmk, const u16* __restrict__ ssmD, u16* __restrict__ hT) {
  extern __shared__ __attribute__((aligned(16))) char smem_[];
  LAS char* sm = (LAS char*)smem_;
  const int b = blockIdx.x, t = threadIdx.x, lane = t & 63;
  const int w = __builtin_amdgcn_readfirstlane(t >> 6);
  const int dbase = (b & 7) * 64 + (b >> 3) * 2;
  const int c2 = w >> 2, ts = (w >> 1) & 1, par = w & 1;
  const int d = dbase + c2;
  const int n = lane & 15, g = lane >> 4;
  glds16(kvT + (size_t)d * S_LEN + (w & 3) * 512 + lane * 8, sm + K1_X + c2 * X_BYTES + 32 + (w & 3) * 1024);
  const int hc = t >> 8, hw = t & 255;
  u32x4 hv = *(const u32x4*)(ssmk + (size_t)(dbase + hc) * S_LEN + hw * 8);
  const int tf0 = ts == 0 ? (par == 0 ? 0 : 4) : (par == 0 ? 1 : 5);
  const int tf1 = ts == 0 ? (par == 0 ? 3 : 7) : (par == 0 ? 2 : 6);
  u32x2 qv[2]; f32x4 st[2][2];
  {
    int s0 = tf0 * 256 + n * 16 + g * 4, s1 = tf1 * 256 + n * 16 + g * 4;
    qv[0] = *(const u32x2*)(qT + (size_t)d * S_LEN + s0);
    qv[1] = *(const u32x2*)(qT + (size_t)d * S_LEN + s1);
    st[0][0] = *(const f32x4*)(statq + s0); st[0][1] = *(const f32x4*)(statq + s0 + 2);
    st[1][0] = *(const f32x4*)(statq + s1); st[1][1] = *(const f32x4*)(statq + s1 + 2);
  }
  const float Dv = bf_lo(ssmD[d]);
  if (t < 64) {
    *(LAS u32x4*)(sm + K1_H + (t >> 3) * H_ROW + 1024 + (t & 7) * 16) = (u32x4){0, 0, 0, 0};
  } else if (t < 68) {
    int i = t - 64;
    *(LAS u32x4*)(sm + K1_X + (i >> 1) * X_BYTES + (i & 1) * 16) = (u32x4){0, 0, 0, 0};
  } else if (t < 72) {
    *(LAS u32x4*)(sm + K1_CY + (t - 68) * CY_BYTES) = (u32x4){0, 0, 0, 0};
  }
  {
    LAS char* hb = sm + K1_H + hc * H_BYTES + 1020 - 4 * hw;
    *(LAS unsigned*)(hb + 0 * H_ROW) = __builtin_amdgcn_perm(hv[1], hv[3], 0x07060302u);
    *(LAS unsigned*)(hb + 1 * H_ROW) = __builtin_amdgcn_perm(hv[1], hv[3], 0x05040100u);
    *(LAS unsigned*)(hb + 2 * H_ROW) = __builtin_amdgcn_perm(hv[0], hv[2], 0x07060302u);
    *(LAS unsigned*)(hb + 3 * H_ROW) = __builtin_amdgcn_perm(hv[0], hv[2], 0x05040100u);
  }
  WAIT_V(0);
  __syncthreads();
  f32x4 acc[4][4];
#pragma unroll
  for (int j = 0; j < 4; ++j)
#pragma unroll
    for (int p = 0; p < 4; ++p) acc[j][p] = (f32x4){0, 0, 0, 0};
  {
    const int q_ = (lane & 15) >> 2, pp = lane & 3;
    const LAS char* xa = sm + K1_X + c2 * X_BYTES + 8 + 64 * g + 8 * q_ + 8 * pp + 256 * par;
    const LAS char* hb = sm + K1_H + c2 * H_BYTES + 1016 - 8 * n + 16 * g + 64 * par - 896;
    if (ts == 0) conv_main<0>(xa, hb, acc); else conv_main<1>(xa, hb, acc);
  }
  f32x4 zq[4], zc[4];
#pragma unroll
  for (int j = 0; j < 4; ++j) {
    zq[j][0] = acc[j][3][0];
    zq[j][1] = acc[j][3][1] + acc[j][2][0];
    zq[j][2] = acc[j][3][2] + acc[j][2][1] + acc[j][1][0];
    zq[j][3] = (acc[j][3][3] + acc[j][2][2]) + (acc[j][1][1] + acc[j][0][0]);
    zc[j][0] = acc[j][2][3] + acc[j][1][2] + acc[j][0][1];
    zc[j][1] = acc[j][1][3] + acc[j][0][2];
    zc[j][2] = acc[j][0][3];
    zc[j][3] = 0.f;
  }
  const int tl0 = ts == 0 ? 0 : 1, tl1 = ts == 0 ? 3 : 2, tl2 = ts == 0 ? 4 : 5, tl3 = ts == 0 ? 7 : 6;
  {
    LAS char* cy = sm + K1_CY + (c2 * 2 + par) * CY_BYTES + 16 + lane * 16;
    *(LAS f32x4*)(cy + tl0 * 1024) = zc[0];
    *(LAS f32x4*)(cy + tl1 * 1024) = zc[1];
    *(LAS f32x4*)(cy + tl2 * 1024) = zc[2];
    *(LAS f32x4*)(cy + tl3 * 1024) = zc[3];
    LAS char* qd = sm + K1_QD + w * 2048 + lane * 16;
    *(LAS f32x4*)(qd) = sel4(par, zq[0], zq[2]);
    *(LAS f32x4*)(qd + 1024) = sel4(par, zq[1], zq[3]);
  }
  __syncthreads();
  {
    const int prev = g > 0 ? lane - 16 : (n > 0 ? 47 + n : -1);
    const LAS char* cy0 = sm + K1_CY + (c2 * 2) * CY_BYTES + 16 + prev * 16;
    const LAS char* qd = sm + K1_QD + (w ^ 1) * 2048 + lane * 16;
#pragma unroll
    for (int i = 0; i < 2; ++i) {
      const int tf = i ? tf1 : tf0;
      f32x4 own = sel4(par, zq[2 + i], zq[i]);
      f32x4 pq = *(const LAS f32x4*)(qd + i * 1024);
      f32x4 ca = *(const LAS f32x4*)(cy0 + tf * 1024);
      f32x4 cb = *(const LAS f32x4*)(cy0 + CY_BYTES + tf * 1024);
      f32x4 y = (own + pq) + (ca + cb);
      const int s0 = tf * 256 + n * 16 + g * 4;
      u32x2 xv = *(const LAS u32x2*)(sm + K1_X + c2 * X_BYTES + 32 + s0 * 2);
      float xx[4] = {bf_lo(xv[0]), bf_hi(xv[0]), bf_lo(xv[1]), bf_hi(xv[1])};
      float qx[4] = {bf_lo(qv[i][0]), bf_hi(qv[i][0]), bf_lo(qv[i][1]), bf_hi(qv[i][1])};
      float mu[4] = {st[i][0][0], st[i][0][2], st[i][1][0], st[i][1][2]};
      float ri[4] = {st[i][0][1], st[i][0][3], st[i][1][1], st[i][1][3]};
      float o[4];
#pragma unroll
      for (int e = 0; e < 4; ++e) {
        float yy = y[e] + xx[e] * Dv;
        float ln = (qx[e] - mu[e]) * ri[e];
        o[e] = qx[e] + ln * yy;
      }
      u32x2 ov = {pk_bf16(o[0], o[1]), pk_bf16(o[2], o[3])};
      *(u32x2*)(hT + (size_t)d * S_LEN + s0) = ov;
    }
  }
}

constexpr int G1_SLOT = 16384 + 12288, G1_R = 5;
constexpr int G1_LDS = G1_SLOT * G1_R + 4096;

__global__ __launch_bounds__(512) void k3_gemm1(const u16* __restrict__ hT, const u16* __restrict__ w1, const float* __restrict__ cs1,
                                                u16* __restrict__ h1T) {
  extern __shared__ __attribute__((aligned(16))) char smem_[];
  LAS char* sm = (LAS char*)smem_;
  const int b = blockIdx.x, t = threadIdx.x, lane = t & 63;
  const int w = __builtin_amdgcn_readfirstlane(t >> 6);
  const int j = b >> 3, panel = (b & 7) * 2 + (j >= 15 ? 1 : 0), ct = j >= 15 ? j - 15 : j;
  const int m0 = panel * 128, n0 = ct * 96;
  const int g = lane >> 4, fr = lane & 15, q_ = fr >> 2, pp = lane & 3;
  f32x4 acc[4][3];
  float st[4];
  if (w >= 4) {
    const int lw = w - 4;
    const char* src[7];
#pragma unroll
    for (int u = 0; u < 4; ++u) {
      int r = (lw * 4 + u) * 4 + (lane >> 4), cp = lane & 15;
      int c = cp ^ (2 * (r & 7));
      src[u] = (const char*)hT + (size_t)r * (S_LEN * 2) + m0 * 2 + c * 16;
    }
#pragma unroll
    for (int u = 0; u < 3; ++u) {
      int byte = (lw * 3 + u) * 1024 + lane * 16;
      int r = byte / 192, cp = (byte % 192) >> 4;
      int c = cp ^ (2 * ((r >> 2) & 1));
      int col = n0 + c * 8;
      if (col > F_DIM - 8) col = F_DIM - 8;
      src[4 + u] = (const char*)w1 + (size_t)r * (F_DIM * 2) + col * 2;
    }
    auto issue = [&](int ch) {
      LAS char* base = sm + (ch % G1_R) * G1_SLOT;
#pragma unroll
      for (int u = 0; u < 4; ++u) glds16(src[u] + (size_t)ch * (64 * S_LEN * 2), base + (lw * 4 + u) * 1024);
#pragma unroll
      for (int u = 0; u < 3; ++u) glds16(src[4 + u] + (size_t)ch * (64 * F_DIM * 2), base + 16384 + (lw * 3 + u) * 1024);
    };
#pragma unroll
    for (int c = 0; c < G1_R - 1; ++c) issue(c);
#pragma unroll
    for (int c = 0; c < 8; ++c) {
      int ahead = (c + G1_R - 1 < 8 ? c + G1_R - 1 : 8) - c - 1;
      if (ahead >= 3) WAIT_V(21); else if (ahead == 2) WAIT_V(14); else if (ahead == 1) WAIT_V(7); else WAIT_V(0);
      RAW_BAR();
      if (c + G1_R - 1 < 8) issue(c + G1_R - 1);
    }
    RAW_BAR();
  } else {
    const int wm = w >> 1, wn = w & 1;
    int aoff[4], boff[3];
    const int rl = 4 * g + q_;
#pragma unroll
    for (int i = 0; i < 4; ++i) aoff[i] = rl * 256 + (((wm * 4 + i) ^ (rl & 7)) << 5) + 8 * pp;
#pragma unroll
    for (int i = 0; i < 3; ++i) boff[i] = 16384 + rl * 192 + (((wn * 3 + i) ^ ((rl >> 2) & 1)) << 5) + 8 * pp;
#pragma unroll
    for (int i = 0; i < 4; ++i) {
      st[i] = 0.f;
#pragma unroll
      for (int jn = 0; jn < 3; ++jn) acc[i][jn] = (f32x4){0, 0, 0, 0};
    }
    const bf16x2_t ones = {(__bf16)1.0f, (__bf16)1.0f};
#pragma unroll
    for (int c = 0; c < 8; ++c) {
      RAW_BAR();
      const LAS char* base = sm + (c % G1_R) * G1_SLOT;
#pragma unroll
      for (int ks = 0; ks < 2; ++ks) {
        bf16x8_t A[4], B[3];
#pragma unroll
        for (int i = 0; i < 4; ++i) A[i] = cat8(tr_read(base + aoff[i] + ks * 32 * 256), tr_read(base + aoff[i] + (ks * 32 + 16) * 256));
#pragma unroll
        for (int i = 0; i < 3; ++i) B[i] = cat8(tr_read(base + boff[i] + ks * 32 * 192), tr_read(base + boff[i] + (ks * 32 + 16) * 192));
#pragma unroll
        for (int i = 0; i < 4; ++i) {
#pragma unroll
          for (int jn = 0; jn < 3; ++jn) acc[i][jn] = mfma16(A[i], B[jn], acc[i][jn]);
#pragma unroll
          for (int e = 0; e < 4; ++e) {
            bf16x2_t x = {A[i][2 * e], A[i][2 * e + 1]};
            st[i] = __builtin_amdgcn_fdot2_f32_bf16(x, wn ? x : ones, st[i], false);
          }
        }
      }
      WAIT_L0();
    }
    RAW_BAR();
  }
  LAS float* sstat = (LAS float*)(sm);
  LAS char* sacc = sm + 2048;
  if (w < 4) {
    const int wm = w >> 1, wn = w & 1;
#pragma unroll
    for (int i = 0; i < 4; ++i) {
      float s = st[i];
      s += __shfl_xor(s, 16);
      s += __shfl_xor(s, 32);
      if (g == 0) sstat[wn * 128 + wm * 64 + i * 16 + fr] = s;
    }
#pragma unroll
    for (int i = 2; i < 4; ++i)
#pragma unroll
      for (int jn = 0; jn < 3; ++jn) *(LAS f32x4*)(sacc + w * 6144 + ((i - 2) * 3 + jn) * 1024 + lane * 16) = acc[i][jn];
  }
  __syncthreads();
  {
    const int cw = w & 3, wm = cw >> 1, wn = cw & 1, hi = w >> 2;
    f32x4 a2[2][3];
    if (hi) {
#pragma unroll
      for (int i = 0; i < 2; ++i)
#pragma unroll
        for (int jn = 0; jn < 3; ++jn) a2[i][jn] = *(const LAS f32x4*)(sacc + cw * 6144 + (i * 3 + jn) * 1024 + lane * 16);
    } else {
#pragma unroll
      for (int i = 0; i < 2; ++i)
#pragma unroll
        for (int jn = 0; jn < 3; ++jn) a2[i][jn] = acc[i][jn];
    }
    float csv[3];
#pragma unroll
    for (int jn = 0; jn < 3; ++jn) { int nn = n0 + (wn * 3 + jn) * 16 + fr; csv[jn] = cs1[nn < F_DIM ? nn : F_DIM - 1]; }
#pragma unroll
    for (int i = 0; i < 2; ++i) {
      const int ml = wm * 64 + (hi * 2 + i) * 16 + g * 4;
      f32x4 s1 = *(const LAS f32x4*)(sstat + ml), s2 = *(const LAS f32x4*)(sstat + 128 + ml);
      float mu[4], rs[4];
#pragma unroll
      for (int e = 0; e < 4; ++e) {
        float mean = s1[e] * (1.0f / D_DIM);
        float var = fmaxf(s2[e] * (1.0f / D_DIM) - mean * mean, 0.f);
        float den = rbf(sqrtf(rbf(rbf(var) + 1e-5f)));
        mu[e] = rbf(mean); rs[e] = 1.0f / den;
      }
#pragma unroll
      for (int jn = 0; jn < 3; ++jn) {
        const int nn = n0 + (wn * 3 + jn) * 16 + fr;
        float o[4];
#pragma unroll
        for (int e = 0; e < 4; ++e) {
          float x = (a2[i][jn][e] - mu[e] * csv[jn]) * rs[e];
          o[e] = x * __builtin_amdgcn_rcpf(1.0f + __builtin_amdgcn_exp2f(-1.44269504f * x));
        }
        u32x2 ov = {pk_bf16(o[0], o[1]), pk_bf16(o[2], o[3])};
        if (nn < F_DIM) *(u32x2*)(h1T + (size_t)nn * S_LEN + m0 + ml) = ov;
      }
    }
  }
}

constexpr int G2_SLOT = 16384, G2_R = 8, G2_NCH = 22;
constexpr int G2_LDS = G2_SLOT * G2_R + 8192;

__global__ __launch_bounds__(512) void k4_gemm2(const u16* __restrict__ h1T, const u16* __restrict__ w2, const u16* __restrict__ hT,
                                                u16* __restrict__ out) {
  extern __shared__ __attribute__((aligned(16))) char smem_[];
  LAS char* sm = (LAS char*)smem_;
  const int b = blockIdx.x, t = threadIdx.x, lane = t & 63;
  const int w = __builtin_amdgcn_readfirstlane(t >> 6);
  const int j = b >> 3, mt = (b & 7) * 4 + (j >> 3), dt = j & 7;
  const int m0 = mt * 64, d0 = dt * 64;
  const int g = lane >> 4, fr = lane & 15, q_ = fr >> 2, pp = lane & 3;
  LAS char* sres = sm + G2_SLOT * G2_R;
  f32x4 acc[4][2];
  if (w >= 4) {
    const int lw = w - 4;
    const char* src[4];
#pragma unroll
    for (int u = 0; u < 2; ++u) {
      int r = (lw * 2 + u) * 8 + (lane >> 3), cp = lane & 7;
      int c = cp ^ (2 * ((r >> 1) & 3));
      src[u] = (const char*)w2 + (size_t)r * (D_DIM * 2) + d0 * 2 + c * 16;
      src[2 + u] = (const char*)h1T + (size_t)r * (S_LEN * 2) + m0 * 2 + c * 16;
    }
#pragma unroll
    for (int u = 0; u < 2; ++u) {
      int r = (lw * 2 + u) * 8 + (lane >> 3), cp = lane & 7;
      int c = cp ^ (2 * ((r >> 1) & 3));
      glds16((const char*)hT + (size_t)(d0 + r) * (S_LEN * 2) + m0 * 2 + c * 16, sres + (lw * 2 + u) * 1024);
    }
    auto issue = [&](int ch) {
      LAS char* base = sm + (ch % G2_R) * G2_SLOT;
      if (ch == G2_NCH - 1 && lw >= 2) {
#pragma unroll
        for (int u = 0; u < 2; ++u) {
          glds16(src[u], base + (lw * 2 + u) * 1024);
          glds16(src[2 + u], base + 8192 + (lw * 2 + u) * 1024);
        }
      } else {
#pragma unroll
        for (int u = 0; u < 2; ++u) {
          glds16(src[u] + (size_t)ch * (64 * D_DIM * 2), base + (lw * 2 + u) * 1024);
          glds16(src[2 + u] + (size_t)ch * (64 * S_LEN * 2), base + 8192 + (lw * 2 + u) * 1024);
        }
      }
    };
#pragma unroll
    for (int c = 0; c < G2_R - 1; ++c) issue(c);
#pragma unroll
    for (int c = 0; c < G2_NCH; ++c) {
      int ahead = (c + G2_R - 1 < G2_NCH ? c + G2_R - 1 : G2_NCH) - c - 1;
      switch (ahead) { case 6: WAIT_V(24); break; case 5: WAIT_V(20); break; case 4: WAIT_V(16); break; case 3: WAIT_V(12); break;
                       case 2: WAIT_V(8); break; case 1: WAIT_V(4); break; default: WAIT_V(0); }
      RAW_BAR();
      if (c + G2_R - 1 < G2_NCH) issue(c + G2_R - 1);
    }
    RAW_BAR();
  } else {
    const int kh = w >> 1, mh = w & 1;
    int aoff[4], boff[2];
    const int rl = 4 * g + q_;
    const int f = (rl >> 1) & 3;
#pragma unroll
    for (int i = 0; i < 4; ++i) aoff[i] = (kh * 32 + rl) * 128 + ((i ^ f) << 5) + 8 * pp;
#pragma unroll
    for (int i = 0; i < 2; ++i) boff[i] = 8192 + (kh * 32 + rl) * 128 + (((mh * 2 + i) ^ f) << 5) + 8 * pp;
#pragma unroll
    for (int i = 0; i < 4; ++i) { acc[i][0] = (f32x4){0, 0, 0, 0}; acc[i][1] = (f32x4){0, 0, 0, 0}; }
#pragma unroll
    for (int c = 0; c < G2_NCH; ++c) {
      RAW_BAR();
      if (c == G2_NCH - 1 && kh == 1) continue;
      const LAS char* base = sm + (c % G2_R) * G2_SLOT;
      bf16x8_t A[4], B[2];
#pragma unroll
      for (int i = 0; i < 4; ++i) A[i] = cat8(tr_read(base + aoff[i]), tr_read(base + aoff[i] + 16 * 128));
#pragma unroll
      for (int i = 0; i < 2; ++i) B[i] = cat8(tr_read(base + boff[i]), tr_read(base + boff[i] + 16 * 128));
#pragma unroll
      for (int i = 0; i < 4; ++i)
#pragma unroll
        for (int jm = 0; jm < 2; ++jm) acc[i][jm] = mfma16(A[i], B[jm], acc[i][jm]);
      WAIT_L0();
    }
    RAW_BAR();
  }
  LAS char* sx = sm;
  if (w < 4) {
    const int kh = w >> 1;
#pragma unroll
    for (int i = 0; i < 2; ++i)
#pragma unroll
      for (int jm = 0; jm < 2; ++jm) *(LAS f32x4*)(sx + w * 4096 + (i * 2 + jm) * 1024 + lane * 16) = sel4(kh, acc[i][jm], acc[2 + i][jm]);
  }
  __syncthreads();
  if (w < 4) {
    const int kh = w >> 1, mh = w & 1;
#pragma unroll
    for (int i = 0; i < 2; ++i)
#pragma unroll
      for (int jm = 0; jm < 2; ++jm) {
        f32x4 o = *(const LAS f32x4*)(sx + (w ^ 2) * 4096 + (i * 2 + jm) * 1024 + lane * 16);
        f32x4 a = sel4(kh, acc[2 + i][jm], acc[i][jm]);
        a += o;
        const int ds = kh * 2 + i;
        const int ms = mh * 2 + jm;
        const int r = ds * 16 + 4 * g + q_;
        s16x4 rv = tr_read(sres + r * 128 + ((ms ^ ((r >> 1) & 3)) << 5) + 8 * pp);
        u32x2 ru = __builtin_bit_cast(u32x2, rv);
        float o0 = bf_lo(ru[0]) + rbf(a[0]), o1 = bf_hi(ru[0]) + rbf(a[1]), o2 = bf_lo(ru[1]) + rbf(a[2]), o3 = bf_hi(ru[1]) + rbf(a[3]);
        u32x2 ov = {pk_bf16(o0, o1), pk_bf16(o2, o3)};
        *(u32x2*)(out + (size_t)(m0 + ms * 16 + fr) * D_DIM + d0 + ds * 16 + g * 4) = ov;
      }
  }
}

extern "C" void kernel_launch(void* const* d_in, const int* in_sizes, int n_in, void* d_out, int out_size, void* d_ws, size_t ws_size,
                              hipStream_t stream) {
  const u16* q = (const u16*)d_in[0];
  const u16* k = (const u16*)d_in[1];
  const u16* v = (const u16*)d_in[2];
  const u16* shk = (const u16*)d_in[3];
  const u16* ssmk = (const u16*)d_in[4];
  const u16* ssmD = (const u16*)d_in[5];
  const u16* w1 = (const u16*)d_in[7];
  const u16* w2 = (const u16*)d_in[8];
  char* ws = (char*)d_ws;
  u16* kvT = (u16*)(ws);
  u16* qT = (u16*)(ws + (2 << 20));
  u16* hT = (u16*)(ws + (4 << 20));
  u16* h1T = (u16*)(ws + (6 << 20));
  f32x2* statq = (f32x2*)(ws + (12 << 20));
  float* cs1 = (float*)(ws + (12 << 20) + 65536);
  (void)hipFuncSetAttribute((const void*)k1_conv, hipFuncAttributeMaxDynamicSharedMemorySize, K1_LDS);
  (void)hipFuncSetAttribute((const void*)k3_gemm1, hipFuncAttributeMaxDynamicSharedMemorySize, G1_LDS);
  (void)hipFuncSetAttribute((const void*)k4_gemm2, hipFuncAttributeMaxDynamicSharedMemorySize, G2_LDS);
  k0_prep<<<256, 256, 0, stream>>>(q, k, v, shk, w1, kvT, qT, statq, cs1);
  k1_conv<<<256, 512, K1_LDS, stream>>>(kvT, qT, statq, ssmk, ssmD, hT);
  k3_gemm1<<<240, 512, G1_LDS, stream>>>(hT, w1, cs1, h1T);
  k4_gemm2<<<256, 512, G2_LDS, stream>>>(h1T, w2, hT, (u16*)d_out);
}
```

```cpp
#include <hip/hip_runtime.h>
#include <hip/hip_bf16.h>
#include <stdint.h>
#include <type_traits>
#include <utility>

typedef unsigned short u16;
typedef short bf16x8 __attribute__((ext_vector_type(8)));
typedef short s16x4 __attribute__((ext_vector_type(4)));
typedef float f32x4 __attribute__((ext_vector_type(4)));
typedef float f32x2 __attribute__((ext_vector_type(2)));
typedef unsigned u32x4 __attribute__((ext_vector_type(4)));
typedef unsigned u32x2 __attribute__((ext_vector_type(2)));
typedef __bf16 bf16x2_t __attribute__((ext_vector_type(2)));
typedef __bf16 bf16x8_t __attribute__((ext_vector_type(8)));
typedef short v4i16_t __attribute__((ext_vector_type(4)));
#define LAS __attribute__((address_space(3)))
#define GAS __attribute__((address_space(1)))
typedef GAS unsigned gu32;
#define RLX_AGENT __ATOMIC_RELAXED, __HIP_MEMORY_SCOPE_AGENT

constexpr int S_LEN = 2048, D_DIM = 512, F_DIM = 1376;
constexpr int AUX_SC1 = 16;

constexpr unsigned WS_KVT = 0, WS_QT = 2u << 20, WS_HT = 4u << 20, WS_H1T = 6u << 20, WS_STQ = 12u << 20;
constexpr unsigned WS_FL1 = WS_STQ + 65536, WS_FL2 = WS_FL1 + 32768, WS_FL3 = WS_FL2 + 32768, WS_EPOCH = WS_FL3 + 32768;
constexpr unsigned WS_STAMP = WS_EPOCH + 4096;
constexpr unsigned WS_BYTES = 16u << 20;
constexpr int FLAG_STRIDE = 16;
constexpr unsigned SPIN_CAP = 1u << 18;

#define WAIT_V(n) asm volatile("s_waitcnt vmcnt(%0)" ::"n"(n) : "memory")
#define WAIT_L0() asm volatile("s_waitcnt lgkmcnt(0)" ::: "memory")
#define RAW_BAR() do { asm volatile("" ::: "memory"); __builtin_amdgcn_s_barrier(); asm volatile("" ::: "memory"); } while (0)
#define LBAR() do { WAIT_L0(); RAW_BAR(); } while (0)
#define SCHED() __builtin_amdgcn_sched_barrier(0)
template <int N> __device__ __forceinline__ void interleave_mfma_ds() {
#pragma unroll
  for (int i = 0; i < N; ++i) { __builtin_amdgcn_sched_group_barrier(0x8, 1, 0); __builtin_amdgcn_sched_group_barrier(0x100, 1, 0); }
}

#define STAMP(i)
#define CSTAMP(i)

__device__ __forceinline__ float bf_lo(unsigned x) { return __uint_as_float(x << 16); }
__device__ __forceinline__ float bf_hi(unsigned x) { return __uint_as_float(x & 0xffff0000u); }
__device__ __forceinline__ unsigned pk_bf16(float lo, float hi) {
  bf16x2_t v; v.x = (__bf16)lo; v.y = (__bf16)hi;
  return __builtin_bit_cast(unsigned, v);
}
__device__ __forceinline__ float rbf(float x) { return __uint_as_float(((unsigned)__builtin_bit_cast(u16, (__bf16)x)) << 16); }
__device__ __forceinline__ s16x4 tr_read(const LAS char* p) {
  return __builtin_bit_cast(s16x4, __builtin_amdgcn_ds_read_tr16_b64_v4i16((LAS v4i16_t*)p));
}
__device__ __forceinline__ bf16x8_t cat8(s16x4 a, s16x4 b) {
  bf16x8 r = __builtin_shufflevector(a, b, 0, 1, 2, 3, 4, 5, 6, 7);
  return __builtin_bit_cast(bf16x8_t, r);
}
__device__ __forceinline__ bf16x8_t tr_frag(const LAS char* p, int second) { return cat8(tr_read(p), tr_read(p + second)); }
__device__ __forceinline__ f32x4 mfma16(bf16x8_t a, bf16x8_t b, f32x4 c) {
  return __builtin_amdgcn_mfma_f32_16x16x32_bf16(a, b, c, 0, 0, 0);
}
template <int AUX>
__device__ __forceinline__ void dma16(const void* sbase, unsigned voff, unsigned lds) {
  if constexpr (AUX == AUX_SC1)
    asm volatile("s_mov_b32 m0, %2\n\ts_nop 0\n\tglobal_load_lds_dwordx4 %0, %1 sc1" ::"v"(voff), "s"(sbase), "s"(lds) : "memory");
  else
    asm volatile("s_mov_b32 m0, %2\n\ts_nop 0\n\tglobal_load_lds_dwordx4 %0, %1" ::"v"(voff), "s"(sbase), "s"(lds) : "memory");
}
__device__ __forceinline__ u32x4 ld128_asm(const void* sbase, unsigned voff) {
  u32x4 r; asm volatile("global_load_dwordx4 %0, %1, %2" : "=v"(r) : "v"(voff), "s"(sbase) : "memory"); return r;
}
__device__ __forceinline__ u32x4 ld128_sc1(const void* sbase, unsigned voff) {
  u32x4 r; asm volatile("global_load_dwordx4 %0, %1, %2 sc1" : "=v"(r) : "v"(voff), "s"(sbase) : "memory"); return r;
}
__device__ __forceinline__ u32x2 ld64_asm(const void* sbase, unsigned voff) {
  u32x2 r; asm volatile("global_load_dwordx2 %0, %1, %2" : "=v"(r) : "v"(voff), "s"(sbase) : "memory"); return r;
}
__device__ __forceinline__ u32x2 ld64_sc1(const void* sbase, unsigned voff) {
  u32x2 r; asm volatile("global_load_dwordx2 %0, %1, %2 sc1" : "=v"(r) : "v"(voff), "s"(sbase) : "memory"); return r;
}
template <int CTRL>
__device__ __forceinline__ float dpp_add(float x) {
  return x + __builtin_bit_cast(float, __builtin_amdgcn_mov_dpp(__builtin_bit_cast(int, x), CTRL, 0xf, 0xf, true));
}
__device__ __forceinline__ float wave_sum(float x) {
  x = dpp_add<0xB1>(x); x = dpp_add<0x4E>(x); x = dpp_add<0x141>(x); x = dpp_add<0x140>(x);
  auto s = __builtin_amdgcn_permlane16_swap(__float_as_uint(x), __float_as_uint(x), false, false);
  x = __uint_as_float(s[0]) + __uint_as_float(s[1]);
  auto u = __builtin_amdgcn_permlane32_swap(__float_as_uint(x), __float_as_uint(x), false, false);
  return __uint_as_float(u[0]) + __uint_as_float(u[1]);
}
__device__ __forceinline__ u32x4 widen16(u32x2 X, u32x2 Y) {
  auto a = __builtin_amdgcn_permlane16_swap(X[0], Y[0], false, false);
  auto b = __builtin_amdgcn_permlane16_swap(X[1], Y[1], false, false);
  return (u32x4){a[0], b[0], a[1], b[1]};
}

struct Params {
  const u16 *q, *k, *v, *shk, *ssmk, *ssmD, *w1, *w2;
  u16* out;
  char* ws;
};

__device__ __forceinline__ void wait_flags256(const char* ws, unsigned off, unsigned epoch, int lane) {
  gu32* f = (gu32*)(ws + off) + lane * FLAG_STRIDE;
#pragma unroll 1
  for (unsigned spins = 0; spins < SPIN_CAP; ++spins) {
    unsigned a = __hip_atomic_load(f, RLX_AGENT), b = __hip_atomic_load(f + 64 * FLAG_STRIDE, RLX_AGENT);
    unsigned c = __hip_atomic_load(f + 128 * FLAG_STRIDE, RLX_AGENT), d = __hip_atomic_load(f + 192 * FLAG_STRIDE, RLX_AGENT);
    if (__all((a == epoch) & (b == epoch) & (c == epoch) & (d == epoch))) break;
    __builtin_amdgcn_s_sleep(1);
  }
  __builtin_amdgcn_fence(__ATOMIC_ACQUIRE, "wavefront");
}

constexpr int L_KT = 32768, L_VT = L_KT + 72 * 128, L_QT = L_VT + 8192, L_OKV = L_QT + 8192, L_OQ = L_OKV + 8192, L_SS = L_OQ + 8192;
constexpr int H_ROW = 1152, H_COPY = 4728, H_BYTES = 9344, X_BYTES = 4160;
constexpr int L_H = 0, L_X = L_H + 2 * H_BYTES, L_QD = L_X + 2 * X_BYTES;
constexpr int CY_BYTES = 513 * 16;
constexpr int L_CY = L_QD + 16384;
constexpr int L_JUNKA = 76800;
constexpr int L_W1 = 77824;
constexpr int L_W1B = 0;
static_assert(L_CY + 4 * CY_BYTES <= L_JUNKA && L_JUNKA + 1024 <= L_W1 && L_W1 + 7 * 12288 <= 163840 && L_SS + 64 <= L_JUNKA, "lds");
static_assert(L_X + 2 * X_BYTES <= L_KT, "lds A/B overlap");
constexpr int L_W2A = 4 * 16384;
constexpr int L_CS = 147456;
constexpr int L_SST = L_CS + 1024;
constexpr int L_FST = L_SST + 2048;
constexpr int L_OUT1B = L_FST + 1024;
constexpr int L_JUNKC = L_OUT1B + 8192;
constexpr int L_EX1 = 0;
constexpr int L_OUT1A = 49152;
static_assert(L_JUNKC + 1024 <= 163840, "lds");
constexpr int L_RES = 155648;
constexpr int L_RING = 0;
constexpr int L_JUNKD = 147456;
constexpr int L_EX2 = 0;
constexpr int LDS_BYTES = 163840;

template <class F, int... I>
__device__ __forceinline__ void static_for_impl(F&& f, std::integer_sequence<int, I...>) { (f(std::integral_constant<int, I>{}), ...); }
template <int N, class F>
__device__ __forceinline__ void static_for(F&& f) { static_for_impl(f, std::make_integer_sequence<int, N>{}); }

template <int TS, int PAR, class F>
__device__ __forceinline__ void conv_main(const LAS char* xa, const LAS char* hb, f32x4 (&acc)[4][4], F&& job) {
  constexpr int U[4] = {TS == 0 ? 0 : 1, TS == 0 ? 3 : 2, TS == 0 ? 4 : 5, TS == 0 ? 7 : 6};
  constexpr int T[4] = {U[PAR ? 2 : 0], U[PAR ? 3 : 1], U[PAR ? 0 : 2], U[PAR ? 1 : 3]};
  constexpr int NA = U[3] + 1;
  bf16x8_t A[8], B[3][4];
#pragma unroll
  for (int p = 0; p < 4; ++p) B[0][p] = *(const LAS bf16x8_t*)(hb + p * H_ROW + 896);
#pragma unroll
  for (int a = NA - 1; a >= 0; --a) A[a] = tr_frag(xa + 512 * a, 32);
#pragma unroll
  for (int p = 0; p < 4; ++p) B[1][p] = *(const LAS bf16x8_t*)(hb + p * H_ROW + 896 - 128);
  SCHED();
  static_for<NA>([&](auto BB) {
    constexpr int bb = decltype(BB)::value;
    if (bb + 2 < NA) {
#pragma unroll
      for (int p = 0; p < 4; ++p) B[(bb + 2) % 3][p] = *(const LAS bf16x8_t*)(hb + p * H_ROW + 896 - 128 * (bb + 2));
    }
#pragma unroll
    for (int p = 0; p < 4; ++p)
#pragma unroll
      for (int j = 3; j >= 0; --j)
        if (T[j] >= bb) acc[j][p] = mfma16(A[T[j] - bb], B[bb % 3][p], acc[j][p]);
    if (bb + 2 < NA) interleave_mfma_ds<4>();
    SCHED();
    if constexpr (bb < 3) { job(std::integral_constant<int, 2 * bb>{}); job(std::integral_constant<int, 2 * bb + 1>{}); }
    else if constexpr (bb < 7) job(std::integral_constant<int, bb + 3>{});
    SCHED();
  });
}

__global__ __launch_bounds__(512, 2) void h3_fused(Params p) {
  extern __shared__ __attribute__((aligned(16))) char smem_[];
  LAS char* sm = (LAS char*)smem_;
  const unsigned lb = (unsigned)(size_t)sm;
  const int b = blockIdx.x, t = threadIdx.x, lane = t & 63;
  const int w = __builtin_amdgcn_readfirstlane(t >> 6);
  const int x = b & 7, jb = b >> 3;
  const int g = lane >> 4, fr = lane & 15, q_ = fr >> 2, pp = lane & 3;
  const int rl = 4 * g + q_;
  char* ws = p.ws;
  const __amdgpu_buffer_rsrc_t rs = __builtin_amdgcn_make_buffer_rsrc(ws, 0, WS_BYTES, 0x00020000);
  STAMP(0); CSTAMP(0);

  const int dB = x * 64 + jb * 2;
  {
    const unsigned vo = (lane >> 3) * (D_DIM * 2) + (lane & 7) * 16;
    const size_t so = ((size_t)(jb * 64 + w * 8) * D_DIM + x * 64);
    dma16<0>(p.k + so, vo, lb + L_KT + 1024 + w * 1024);
    dma16<0>(p.v + so, vo, lb + L_VT + w * 1024);
    dma16<0>(p.q + so, vo, lb + L_QT + w * 1024);
    if (w == 0) {
      if (jb > 0) dma16<0>(p.k + so - 8 * D_DIM, vo, lb + L_KT);
      else *(LAS u32x4*)(sm + L_KT + lane * 16) = (u32x4){0, 0, 0, 0};
    }
  }
  const int cb = w & 3, sl = 8 * (4 * (w >> 2) + g);
  u32x4 sq = ld128_asm(p.q + (size_t)(b * 8 + w) * D_DIM, lane * 16);
  u32x2 wv = ld64_asm(p.shk + (size_t)(x * 64 + cb * 16) * 4, fr * 8);
  const unsigned epoch = __hip_atomic_load((gu32*)(ws + WS_EPOCH), RLX_AGENT) + 1u;
  const float Dv = bf_lo(p.ssmD[dB + (w >> 2)]);
  const bool c_on = jb < 30;
  const int panel = x * 2 + (jb >= 15 ? 1 : 0), ct = jb >= 15 ? jb - 15 : jb;
  const int m0 = panel * 128, n0 = ct * 96;
  unsigned w1o[6];
#pragma unroll
  for (int u = 0; u < 6; ++u) {
    const int idx = ((w & 1) * 6 + u) * 64 + lane, r = (idx * 43691) >> 19, cp = idx - r * 12;
    int col = n0 + (cp ^ (2 * ((r >> 2) & 1))) * 8;
    col = col > F_DIM - 8 ? F_DIM - 8 : col;
    w1o[u] = (r * F_DIM + col) * 2;
  }
  auto w1_half = [&](int chunk, int ldst, int step) {
#pragma unroll
    for (int u = 0; u < 6; ++u) dma16<0>(p.w1 + (size_t)chunk * 64 * F_DIM, w1o[u], lb + ldst + ((w & 1) * 6 + u) * step);
  };
  WAIT_V(0);
  asm volatile("" : "+v"(sq), "+v"(wv));
  {
    f32x2 xs[4];
#pragma unroll
    for (int i = 0; i < 4; ++i) xs[i] = (f32x2){bf_lo(sq[i]), bf_hi(sq[i])};
    const f32x2 s2 = (xs[0] + xs[1]) + (xs[2] + xs[3]);
    const float mean = wave_sum(s2[0] + s2[1]) * (1.0f / D_DIM);
    f32x2 vs = {0.f, 0.f};
#pragma unroll
    for (int i = 0; i < 4; ++i) { const f32x2 dd = xs[i] - mean; vs += dd * dd; }
    const float var = rbf(wave_sum(vs[0] + vs[1]) * (1.0f / D_DIM));
    if (lane == 0) *(LAS unsigned*)(sm + L_SS + w * 4) = pk_bf16(mean, __builtin_amdgcn_sqrtf(rbf(var + 1e-5f)));
  }
  LBAR();
  STAMP(1);
  {
    const int tro = q_ * 128 + cb * 32 + 8 * pp + sl * 128;
    const u32x2 k0 = __builtin_bit_cast(u32x2, tr_read(sm + L_KT + 4 * 128 + tro));
    const u32x2 k1 = __builtin_bit_cast(u32x2, tr_read(sm + L_KT + 8 * 128 + tro));
    const u32x2 k2 = __builtin_bit_cast(u32x2, tr_read(sm + L_KT + 12 * 128 + tro));
    const u32x2 v0 = __builtin_bit_cast(u32x2, tr_read(sm + L_VT + tro));
    const u32x2 v1 = __builtin_bit_cast(u32x2, tr_read(sm + L_VT + 4 * 128 + tro));
    const u32x2 q0 = __builtin_bit_cast(u32x2, tr_read(sm + L_QT + tro));
    const u32x2 q1 = __builtin_bit_cast(u32x2, tr_read(sm + L_QT + 4 * 128 + tro));
    const float w0 = bf_lo(wv[0]), w1 = bf_hi(wv[0]), w2 = bf_lo(wv[1]), w3 = bf_hi(wv[1]);
    const float kx[11] = {bf_hi(k0[0]), bf_lo(k0[1]), bf_hi(k0[1]), bf_lo(k1[0]), bf_hi(k1[0]), bf_lo(k1[1]), bf_hi(k1[1]),
                          bf_lo(k2[0]), bf_hi(k2[0]), bf_lo(k2[1]), bf_hi(k2[1])};
    const float vx[8] = {bf_lo(v0[0]), bf_hi(v0[0]), bf_lo(v0[1]), bf_hi(v0[1]), bf_lo(v1[0]), bf_hi(v1[0]), bf_lo(v1[1]), bf_hi(v1[1])};
    float o[8];
#pragma unroll
    for (int e = 0; e < 8; ++e) o[e] = (kx[e] * w3 + kx[e + 1] * w2 + kx[e + 2] * w1 + kx[e + 3] * w0) * vx[e];
    const int c = cb * 16 + fr;
    const int oo = c * 128 + (((sl >> 3) ^ (c & 7)) << 4);
    *(LAS u32x4*)(sm + L_OKV + oo) = (u32x4){pk_bf16(o[0], o[1]), pk_bf16(o[2], o[3]), pk_bf16(o[4], o[5]), pk_bf16(o[6], o[7])};
    *(LAS u32x4*)(sm + L_OQ + oo) = (u32x4){q0[0], q0[1], q1[0], q1[1]};
  }
  LBAR();
  STAMP(15);
  {
    const int ch = t >> 3, c = t & 7;
    const int off = ch * 128 + ((c ^ (ch & 7)) << 4);
    const u32x4 a = *(const LAS u32x4*)(sm + L_OKV + off), bq = *(const LAS u32x4*)(sm + L_OQ + off);
    const unsigned go = ((x * 64 + ch) * S_LEN + jb * 64 + c * 8) * 2;
    __builtin_amdgcn_raw_buffer_store_b128(a, rs, WS_KVT + go, 0, AUX_SC1);
    __builtin_amdgcn_raw_buffer_store_b128(bq, rs, WS_QT + go, 0, AUX_SC1);
    if (t < 2) __builtin_amdgcn_raw_buffer_store_b128(*(const LAS u32x4*)(sm + L_SS + t * 16), rs, WS_STQ + b * 32 + t * 16, 0, AUX_SC1);
  }
  STAMP(31);
  WAIT_V(0);
  LBAR();
  if (t == 0) __hip_atomic_store((gu32*)(ws + WS_FL1) + b * FLAG_STRIDE, epoch, RLX_AGENT);
  RAW_BAR();
  STAMP(2);
  {
    const int hc = t >> 8, hw = t & 255;
    u32x4 hv = ld128_asm(p.ssmk + (size_t)dB * S_LEN, (hc * S_LEN + hw * 8) * 2);
    if (w < 4) { w1_half(w >> 1, L_W1 + (w >> 1) * 12288, 1024); WAIT_V(6); } else { WAIT_V(0); }
    asm volatile("" : "+v"(hv));
    LAS char* hb = sm + L_H + hc * H_BYTES + 1020 - 4 * hw;
    const unsigned h0 = __builtin_amdgcn_perm(hv[1], hv[3], 0x07060302u), h1 = __builtin_amdgcn_perm(hv[1], hv[3], 0x05040100u);
    const unsigned h2 = __builtin_amdgcn_perm(hv[0], hv[2], 0x07060302u), h3 = __builtin_amdgcn_perm(hv[0], hv[2], 0x05040100u);
    *(LAS unsigned*)(hb + 0 * H_ROW) = h0; *(LAS unsigned*)(hb + H_COPY + 0 * H_ROW) = h0;
    *(LAS unsigned*)(hb + 1 * H_ROW) = h1; *(LAS unsigned*)(hb + H_COPY + 1 * H_ROW) = h1;
    *(LAS unsigned*)(hb + 2 * H_ROW) = h2; *(LAS unsigned*)(hb + H_COPY + 2 * H_ROW) = h2;
    *(LAS unsigned*)(hb + 3 * H_ROW) = h3; *(LAS unsigned*)(hb + H_COPY + 3 * H_ROW) = h3;
    if (t < 256) {
      const int r = t >> 4;
      *(LAS u32x2*)(sm + L_H + (r >> 3) * H_BYTES + ((r >> 2) & 1) * H_COPY + (r & 3) * H_ROW + 1024 + (t & 15) * 8) = (u32x2){0, 0};
    } else if (t < 260) *(LAS u32x4*)(sm + L_X + ((t - 256) >> 1) * X_BYTES + (t & 1) * 16) = (u32x4){0, 0, 0, 0};
    else if (t < 264) *(LAS u32x4*)(sm + L_CY + (t - 260) * CY_BYTES) = (u32x4){0, 0, 0, 0};
  }

  const int c2 = w >> 2, ts = (w >> 1) & 1, par = w & 1;
  const int tl0 = ts == 0 ? 0 : 1, tl1 = ts == 0 ? 3 : 2, tl2 = ts == 0 ? 4 : 5, tl3 = ts == 0 ? 7 : 6;
  const int tf0 = par ? tl2 : tl0, tf1 = par ? tl3 : tl1, tg0 = par ? tl0 : tl2, tg1 = par ? tl1 : tl3;
  const int sa = tf0 * 256 + fr * 16 + g * 4, sb = tf1 * 256 + fr * 16 + g * 4;
  if (w == 0) wait_flags256(ws, WS_FL1, epoch, lane);
  LBAR();
  STAMP(3);
  {
    const int d = dB + c2;
    dma16<AUX_SC1>(ws + WS_KVT + ((size_t)d * S_LEN + (w & 3) * 512) * 2, lane * 16, lb + L_X + c2 * X_BYTES + 32 + (w & 3) * 1024);
    u32x2 qv0 = ld64_sc1(ws + WS_QT + (size_t)d * S_LEN * 2, sa * 2), qv1 = ld64_sc1(ws + WS_QT + (size_t)d * S_LEN * 2, sb * 2);
    u32x4 sv0 = ld128_sc1(ws + WS_STQ, sa * 4), sv1 = ld128_sc1(ws + WS_STQ, sb * 4);
    WAIT_V(4);
    RAW_BAR();
    STAMP(4); CSTAMP(1);
    f32x4 acc[4][4];
#pragma unroll
    for (int j = 0; j < 4; ++j)
#pragma unroll
      for (int pz = 0; pz < 4; ++pz) acc[j][pz] = (f32x4){0, 0, 0, 0};
    const LAS char* xa = sm + L_X + c2 * X_BYTES + 8 + 64 * g + 8 * q_ + 8 * pp + 256 * par;
    const LAS char* hb = sm + L_H + c2 * H_BYTES + ((fr & 1) ? 0 : H_COPY) + 1016 - 8 * fr + 16 * g + 64 * par - 896;
    auto job = [&](auto J) {
      constexpr int j = decltype(J)::value;
      if constexpr (j < 6) {
        const int ch = 2 + (w >> 1);
        dma16<0>(p.w1 + (size_t)ch * 64 * F_DIM, w1o[j], lb + L_W1 + ch * 12288 + ((w & 1) * 6 + j) * 1024);
      } else if constexpr (j < 9) {
        if (w < 4) {
          const int u = (w >> 1) * 3 + (j - 6);
          dma16<0>(p.w1 + (size_t)6 * 64 * F_DIM, (w & 2) ? w1o[3 + j - 6] : w1o[j - 6], lb + L_W1 + 6 * 12288 + ((w & 1) * 6 + u) * 1024);
        }
      } else {
        int r2 = ((b * 43) >> 3) + w; r2 = r2 > F_DIM - 1 ? F_DIM - 1 : r2;
        dma16<0>(p.w2 + (size_t)r2 * D_DIM, lane * 16, lb + L_JUNKA);
      }
    };
    if (ts == 0) { if (par == 0) conv_main<0, 0>(xa, hb, acc, job); else conv_main<0, 1>(xa, hb, acc, job); }
    else         { if (par == 0) conv_main<1, 0>(xa, hb, acc, job); else conv_main<1, 1>(xa, hb, acc, job); }
    STAMP(5); CSTAMP(2);
#pragma unroll
    for (int j = 0; j < 4; ++j) {
      acc[j][3][1] += acc[j][2][0];
      acc[j][3][2] += acc[j][2][1] + acc[j][1][0];
      acc[j][3][3] += (acc[j][2][2] + acc[j][1][1]) + acc[j][0][0];
      acc[j][0][1] += acc[j][2][3] + acc[j][1][2];
      acc[j][0][2] += acc[j][1][3];
    }
    LAS char* cy = sm + L_CY + (c2 * 2 + par) * CY_BYTES + 16 + lane * 16;
    *(LAS f32x4*)(cy + tf0 * 1024) = acc[0][0];
    *(LAS f32x4*)(cy + tf1 * 1024) = acc[1][0];
    *(LAS f32x4*)(cy + tg0 * 1024) = acc[2][0];
    *(LAS f32x4*)(cy + tg1 * 1024) = acc[3][0];
    LAS char* qd = sm + L_QD + w * 2048 + lane * 16;
    *(LAS f32x4*)(qd) = acc[2][3]; *(LAS f32x4*)(qd + 1024) = acc[3][3];
    const u32x2 xv0 = *(const LAS u32x2*)(sm + L_X + c2 * X_BYTES + 32 + sa * 2), xv1 = *(const LAS u32x2*)(sm + L_X + c2 * X_BYTES + 32 + sb * 2);
    LBAR();
    STAMP(29);
    const int prev = g > 0 ? lane - 16 : (fr > 0 ? 47 + fr : -1);
    const LAS char* cy0 = sm + L_CY + (c2 * 2) * CY_BYTES + 16 + prev * 16;
    const LAS char* qs = sm + L_QD + (w ^ 1) * 2048 + lane * 16;
    WAIT_V(7);
    asm volatile("" : "+v"(qv0), "+v"(qv1), "+v"(sv0), "+v"(sv1));
    u32x2 ov[2];
#pragma unroll
    for (int i = 0; i < 2; ++i) {
      const int tf = i ? tf1 : tf0;
      const f32x4 pq = *(const LAS f32x4*)(qs + i * 1024);
      const f32x4 ca = *(const LAS f32x4*)(cy0 + tf * 1024), cb_ = *(const LAS f32x4*)(cy0 + CY_BYTES + tf * 1024);
      f32x4 y = acc[i][3] + pq;
      y[0] += ca[1] + cb_[1]; y[1] += ca[2] + cb_[2]; y[2] += ca[3] + cb_[3];
      const u32x2 xv = i ? xv1 : xv0, qv = i ? qv1 : qv0;
      const u32x4 sv = i ? sv1 : sv0;
      const float xx[4] = {bf_lo(xv[0]), bf_hi(xv[0]), bf_lo(xv[1]), bf_hi(xv[1])};
      const float qx[4] = {bf_lo(qv[0]), bf_hi(qv[0]), bf_lo(qv[1]), bf_hi(qv[1])};
      float o[4];
#pragma unroll
      for (int e = 0; e < 4; ++e) o[e] = qx[e] + ((qx[e] - bf_lo(sv[e])) * __builtin_amdgcn_rcpf(bf_hi(sv[e]))) * (y[e] + xx[e] * Dv);
      ov[i] = (u32x2){pk_bf16(o[0], o[1]), pk_bf16(o[2], o[3])};
    }
    const u32x4 o16 = widen16(ov[0], ov[1]);
    const int so = ((g & 1) ? tf1 : tf0) * 256 + fr * 16 + (g >> 1) * 8;
    __builtin_amdgcn_raw_buffer_store_b128(o16, rs, WS_HT + (d * S_LEN + so) * 2, 0, AUX_SC1);
    STAMP(30);
  }
  WAIT_V(0);
  LBAR();
  if (t == 0) __hip_atomic_store((gu32*)(ws + WS_FL2) + b * FLAG_STRIDE, epoch, RLX_AGENT);
  RAW_BAR();
  STAMP(6);

  const int kp = w >> 2, wm = (w >> 1) & 1, wn = w & 1;
  const int mt = x * 4 + (jb >> 3), dt = jb & 7;
  const int md0 = mt * 64, d0 = dt * 64;
  const int kq = w >> 1, dh = w & 1;
  unsigned wvo[2], hvo[2];
#pragma unroll
  for (int u = 0; u < 2; ++u) {
    const int r = ((w & 1) * 2 + u) * 8 + (lane >> 3), c = ((lane & 7) ^ (2 * ((r >> 1) & 3))) * 16;
    wvo[u] = r * (D_DIM * 2) + c; hvo[u] = r * (S_LEN * 2) + c;
  }
  auto load_w2 = [&](int step, int lds) {
    int ks = kq * 11 + step; ks = ks > 42 ? 42 : ks;
#pragma unroll
    for (int u = 0; u < 2; ++u) dma16<0>(p.w2 + (size_t)ks * 32 * D_DIM + d0, wvo[u], lb + lds + kq * 4096 + ((w & 1) * 2 + u) * 1024);
  };
  const bf16x8_t ONES = __builtin_bit_cast(bf16x8_t, (u32x4){0x3f803f80u, 0x3f803f80u, 0x3f803f80u, 0x3f803f80u});
  if (c_on) {
    bf16x8_t W1[8][3];
    f32x4 csa[3];
    if (w < 2) w1_half(7, L_W1B, 1024);
#pragma unroll
    for (int i = 0; i < 3; ++i) csa[i] = (f32x4){0, 0, 0, 0};
    int wo[3];
#pragma unroll
    for (int i = 0; i < 3; ++i) wo[i] = (kp * 32 + rl) * 192 + 8 * pp + (((wn * 3 + i) ^ ((rl >> 2) & 1)) << 5);
#pragma unroll
    for (int c = 0; c < 7; ++c)
#pragma unroll
      for (int i = 0; i < 3; ++i) {
        W1[c][i] = tr_frag(sm + L_W1 + c * 12288 + wo[i], 16 * 192);
        csa[i] = mfma16(ONES, W1[c][i], csa[i]);
      }
    WAIT_V(0);
    RAW_BAR();
    STAMP(28);
#pragma unroll
    for (int i = 0; i < 3; ++i) {
      W1[7][i] = tr_frag(sm + L_W1B + wo[i], 16 * 192);
      csa[i] = mfma16(ONES, W1[7][i], csa[i]);
    }
    LBAR();
    load_w2(0, L_W2A); load_w2(1, L_W2A + 16384); load_w2(2, L_W2A + 2 * 16384);
    STAMP(7);
    if (w == 0) wait_flags256(ws, WS_FL2, epoch, lane);
    LBAR();
    STAMP(8);
    if (b == 0 && t == 0) __hip_atomic_store((gu32*)(ws + WS_EPOCH), epoch, RLX_AGENT);
    const char* hsrc = ws + WS_HT + ((size_t)(w * 8) * S_LEN + m0) * 2;
    const int ra = lane >> 4, rb = ra + 4;
    const unsigned va = ra * (S_LEN * 2) + (((lane & 15) ^ (2 * ra)) << 4), vb = rb * (S_LEN * 2) + (((lane & 15) ^ (2 * rb)) << 4);
    auto load_ht = [&](int c) {
      dma16<AUX_SC1>(hsrc + (size_t)c * (64 * S_LEN * 2), va, lb + (c & 3) * 16384 + (w * 2) * 1024);
      dma16<AUX_SC1>(hsrc + (size_t)c * (64 * S_LEN * 2), vb, lb + (c & 3) * 16384 + (w * 2 + 1) * 1024);
    };
    load_ht(0); load_ht(1);
    if (wm == 0 && g == 0) {
#pragma unroll
      for (int i = 0; i < 3; ++i) *(LAS float*)(sm + L_CS + (kp * 96 + wn * 48 + i * 16 + fr) * 4) = csa[i][0];
    }
    f32x4 acc[4][3], sta[4];
    int aoff[4];
#pragma unroll
    for (int i = 0; i < 4; ++i) {
      sta[i] = (f32x4){0, 0, 0, 0};
      aoff[i] = (kp * 32 + rl) * 256 + (((wm * 4 + (i ^ (2 * kp))) ^ (rl & 7)) << 5) + 8 * pp;
#pragma unroll
      for (int jn = 0; jn < 3; ++jn) acc[i][jn] = (f32x4){0, 0, 0, 0};
    }
    auto g1_loop = [&](auto WN) {
      bf16x8_t A[2][4];
      auto half = [&](bf16x8_t (&Af)[4], const bf16x8_t (&Wf)[3], int i0) {
#pragma unroll
        for (int i = i0; i < i0 + 2; ++i) {
#pragma unroll
          for (int jn = 0; jn < 3; ++jn) acc[i][jn] = mfma16(Af[i], Wf[jn], acc[i][jn]);
          sta[i] = mfma16(Af[i], decltype(WN)::value ? Af[i] : ONES, sta[i]);
        }
      };
      auto warm_w2 = [&](int i) {
        int ks = kq * 11 + 5 + 2 * i + (w & 1); ks = ks > 42 ? 42 : ks;
        dma16<0>(p.w2 + (size_t)(ks * 32 + (jb >> 3) * 8) * D_DIM + d0, (lane >> 3) * (D_DIM * 2) + (lane & 7) * 16, lb + L_JUNKC);
      };
#define G1_ITER(c, N)                                                                                        \
      WAIT_V(N); RAW_BAR();                                                                                  \
      if ((c) == 0) STAMP(16); if ((c) == 1) { STAMP(17); CSTAMP(3); } if ((c) == 4) STAMP(18); if ((c) == 7) STAMP(19); \
      _Pragma("unroll") for (int i = 0; i < 4; ++i) A[(c) & 1][i] = tr_frag(sm + aoff[i] + ((c) & 3) * 16384, 4096); \
      if ((c) > 0) { half(A[((c) - 1) & 1], W1[(c) > 0 ? (c) - 1 : 0], 0); interleave_mfma_ds<8>(); }        \
      SCHED();                                                                                               \
      if ((c) + 2 < 8) load_ht((c) + 2);                                                                     \
      if ((c) == 1) load_w2(3, L_W2A + 3 * 16384);                                                           \
      if ((c) == 2) load_w2(4, L_W2A + 4 * 16384);                                                           \
      if ((c) >= 4 && (c) <= 6) warm_w2((c) - 4);                                                            \
      SCHED();                                                                                               \
      if ((c) > 0) half(A[((c) - 1) & 1], W1[(c) > 0 ? (c) - 1 : 0], 2);                                     \
      SCHED();
      G1_ITER(0, 2) G1_ITER(1, 2) G1_ITER(2, 4) G1_ITER(3, 6) G1_ITER(4, 4) G1_ITER(5, 3) G1_ITER(6, 4) G1_ITER(7, 2)
#undef G1_ITER
      half(A[1], W1[7], 0); half(A[1], W1[7], 2);
    };
    if (wn) g1_loop(std::integral_constant<bool, true>{}); else g1_loop(std::integral_constant<bool, false>{});
    STAMP(9); CSTAMP(4);
    LBAR();
    {
      LAS char* sp = sm + L_SST + ((kp * 2 + wn) * 128 + wm * 64) * 4 + kp * 128, *sq_ = sp - kp * 256;
      if (wn == 0) {
        if (fr == 0) {
          *(LAS f32x4*)(sp + g * 16) = sta[0]; *(LAS f32x4*)(sp + 64 + g * 16) = sta[1];
          *(LAS f32x4*)(sq_ + 128 + g * 16) = sta[2]; *(LAS f32x4*)(sq_ + 192 + g * 16) = sta[3];
        }
      } else if (q_ == g) {
        float dg[4];
#pragma unroll
        for (int i = 0; i < 4; ++i) dg[i] = pp == 0 ? sta[i][0] : (pp == 1 ? sta[i][1] : (pp == 2 ? sta[i][2] : sta[i][3]));
        *(LAS float*)(sp + fr * 4) = dg[0]; *(LAS float*)(sp + 64 + fr * 4) = dg[1];
        *(LAS float*)(sq_ + 128 + fr * 4) = dg[2]; *(LAS float*)(sq_ + 192 + fr * 4) = dg[3];
      }
    }
#pragma unroll
    for (int i = 0; i < 2; ++i)
#pragma unroll
      for (int jn = 0; jn < 3; ++jn) *(LAS f32x4*)(sm + L_EX1 + w * 6144 + (i * 3 + jn) * 1024 + lane * 16) = acc[2 + i][jn];
    LBAR();
    STAMP(20);
    if (t < 128) {
      const LAS float* sp = (const LAS float*)(sm + L_SST) + t;
      const float mean = (sp[0] + sp[256]) * (1.0f / D_DIM);
      const float var = fmaxf((sp[128] + sp[384]) * (1.0f / D_DIM) - mean * mean, 0.f);
      *(LAS float*)(sm + L_FST + t * 4) = -rbf(mean);
      *(LAS float*)(sm + L_FST + 512 + t * 4) = -1.44269504f * __builtin_amdgcn_rcpf(rbf(__builtin_amdgcn_sqrtf(rbf(rbf(var) + 1e-5f))));
    }
    f32x2 csv[3];
#pragma unroll
    for (int jn = 0; jn < 3; ++jn) {
      const int nl = wn * 48 + jn * 16 + fr;
      const float c = *(const LAS float*)(sm + L_CS + nl * 4) + *(const LAS float*)(sm + L_CS + (96 + nl) * 4);
      csv[jn] = (f32x2){c, c};
    }
#pragma unroll
    for (int i = 0; i < 2; ++i)
#pragma unroll
      for (int jn = 0; jn < 3; ++jn) acc[i][jn] += *(const LAS f32x4*)(sm + L_EX1 + (w ^ 4) * 6144 + (i * 3 + jn) * 1024 + lane * 16);
    LBAR();
    u32x2 ov[2][3];
#pragma unroll
    for (int i = 0; i < 2; ++i) {
      const int ml = wm * 64 + (kp * 2 + i) * 16 + g * 4;
      const f32x4 nmu = *(const LAS f32x4*)(sm + L_FST + ml * 4), zz = *(const LAS f32x4*)(sm + L_FST + 512 + ml * 4);
      const f32x2 nm0 = {nmu[0], nmu[1]}, nm1 = {nmu[2], nmu[3]}, z0 = {zz[0], zz[1]}, z1 = {zz[2], zz[3]};
      const f32x2 cL = {-1.44269504f, -1.44269504f};
#pragma unroll
      for (int jn = 0; jn < 3; ++jn) {
        const f32x4 a = acc[i][jn];
        const f32x2 e0 = (nm0 * csv[jn] + (f32x2){a[0], a[1]}) * z0, e1 = (nm1 * csv[jn] + (f32x2){a[2], a[3]}) * z1;
        const f32x2 d0_ = (f32x2){__builtin_amdgcn_exp2f(e0[0]), __builtin_amdgcn_exp2f(e0[1])} * cL + cL;
        const f32x2 d1_ = (f32x2){__builtin_amdgcn_exp2f(e1[0]), __builtin_amdgcn_exp2f(e1[1])} * cL + cL;
        const f32x2 o0 = e0 * (f32x2){__builtin_amdgcn_rcpf(d0_[0]), __builtin_amdgcn_rcpf(d0_[1])};
        const f32x2 o1 = e1 * (f32x2){__builtin_amdgcn_rcpf(d1_[0]), __builtin_amdgcn_rcpf(d1_[1])};
        ov[i][jn] = (u32x2){pk_bf16(o0[0], o0[1]), pk_bf16(o1[0], o1[1])};
      }
    }
#pragma unroll
    for (int jn = 0; jn < 3; ++jn) {
      const u32x4 o16 = widen16(ov[0][jn], ov[1][jn]);
      const int nl = wn * 48 + jn * 16 + fr;
      const int ch = wm * 8 + (kp * 2 + (g & 1)) * 2 + (g >> 1);
      const int base = nl < 64 ? L_OUT1A + nl * 256 : L_OUT1B + (nl - 64) * 256;
      *(LAS u32x4*)(sm + base + ((ch ^ (nl & 7)) << 4)) = o16;
    }
    LBAR();
    STAMP(21);
#pragma unroll
    for (int i = 0; i < 3; ++i) {
      const int nl = i * 32 + (t >> 4), ch = t & 15;
      const int base = i < 2 ? L_OUT1A + nl * 256 : L_OUT1B + (nl - 64) * 256;
      const u32x4 o16 = *(const LAS u32x4*)(sm + base + ((ch ^ (nl & 7)) << 4));
      if (n0 + nl < F_DIM) __builtin_amdgcn_raw_buffer_store_b128(o16, rs, WS_H1T + ((n0 + nl) * S_LEN + m0 + ch * 8) * 2, 0, AUX_SC1);
    }
  } else {
    if (w == 0) wait_flags256(ws, WS_FL2, epoch, lane);
    RAW_BAR();
    load_w2(0, L_W2A); load_w2(1, L_W2A + 16384); load_w2(2, L_W2A + 2 * 16384); load_w2(3, L_W2A + 3 * 16384); load_w2(4, L_W2A + 4 * 16384);
  }
  WAIT_V(0);
  STAMP(22);
  LBAR();
  if (c_on && t == 0) __hip_atomic_store((gu32*)(ws + WS_FL3) + b * FLAG_STRIDE, epoch, RLX_AGENT);
  RAW_BAR();
  STAMP(10);
  if (c_on) {
#pragma unroll
    for (int i = 0; i < 3; ++i) {
      int nn = n0 + i * 32 + w * 4; nn = nn > F_DIM - 4 ? F_DIM - 4 : nn;
      dma16<AUX_SC1>(ws + WS_H1T + ((size_t)nn * S_LEN + m0) * 2, (lane >> 4) * (S_LEN * 2) + (lane & 15) * 16, lb + L_JUNKD + w * 1024);
    }
  }

  {
    load_w2(5, 0); load_w2(6, 16384); load_w2(7, 2 * 16384); load_w2(8, 3 * 16384);
    const int f3 = (rl >> 1) & 3;
    bf16x8_t W2[11][2];
    int woff[2], boff[4];
#pragma unroll
    for (int i = 0; i < 2; ++i) woff[i] = kq * 4096 + rl * 128 + (((dh * 2 + i) ^ f3) << 5) + 8 * pp;
#pragma unroll
    for (int i = 0; i < 4; ++i) boff[i] = L_RING + kq * 4096 + rl * 128 + ((i ^ f3) << 5) + 8 * pp;
#pragma unroll
    for (int s = 0; s < 5; ++s)
#pragma unroll
      for (int i = 0; i < 2; ++i) W2[s][i] = tr_frag(sm + woff[i] + L_W2A + s * 16384, 2048);
    LBAR();
    load_w2(9, 4 * 16384); load_w2(10, 5 * 16384);
    {
      const int r = lane >> 3;
      dma16<AUX_SC1>(ws + WS_HT + ((size_t)(d0 + w * 8) * S_LEN + md0) * 2, r * (S_LEN * 2) + (((lane & 7) ^ (2 * ((r >> 1) & 3))) << 4), lb + L_RES + w * 1024);
    }
    WAIT_V(5);
    RAW_BAR();
    STAMP(23);
#pragma unroll
    for (int s = 5; s < 9; ++s)
#pragma unroll
      for (int i = 0; i < 2; ++i) W2[s][i] = tr_frag(sm + woff[i] + (s - 5) * 16384, 2048);
    WAIT_V(0);
    RAW_BAR();
#pragma unroll
    for (int s = 9; s < 11; ++s)
#pragma unroll
      for (int i = 0; i < 2; ++i) W2[s][i] = tr_frag(sm + woff[i] + (s - 5) * 16384, 2048);
    STAMP(11);
    if (w == 0) {
      const int h = (mt >> 1) & 1;
      gu32* f = (gu32*)(ws + WS_FL3) + (x + 8 * (15 * h + (lane < 15 ? lane : 0))) * FLAG_STRIDE;
#pragma unroll 1
      for (unsigned spins = 0; spins < SPIN_CAP; ++spins) {
        unsigned a = __hip_atomic_load(f, RLX_AGENT);
        if (__all(a == epoch)) break;
        __builtin_amdgcn_s_sleep(1);
      }
      __builtin_amdgcn_fence(__ATOMIC_ACQUIRE, "wavefront");
    }
    LBAR();
    STAMP(12);
    auto load_h1 = [&](int step) {
      int ks = kq * 11 + step; ks = ks > 42 ? 42 : ks;
#pragma unroll
      for (int u = 0; u < 2; ++u)
        dma16<AUX_SC1>(ws + WS_H1T + ((size_t)ks * 32 * S_LEN + md0) * 2, hvo[u], lb + L_RING + (step % 6) * 16384 + kq * 4096 + ((w & 1) * 2 + u) * 1024);
    };
    load_h1(0); load_h1(1); load_h1(2);
    f32x4 acc[2][4];
#pragma unroll
    for (int i = 0; i < 2; ++i)
#pragma unroll
      for (int jm = 0; jm < 4; ++jm) acc[i][jm] = (f32x4){0, 0, 0, 0};
    bf16x8_t B[2][4];
    auto half2 = [&](const bf16x8_t (&Wf)[2], const bf16x8_t (&Bf)[4], int j0) {
#pragma unroll
      for (int jm = j0; jm < j0 + 2; ++jm)
#pragma unroll
        for (int i = 0; i < 2; ++i) acc[i][jm] = mfma16(Wf[i], Bf[jm], acc[i][jm]);
    };
#define G2_ITER(r, N)                                                                                                   \
    WAIT_V(N); LBAR();                                                                                                  \
    if ((r) == 0) STAMP(24); if ((r) == 1) { STAMP(25); CSTAMP(5); } if ((r) == 5) STAMP(26); if ((r) == 10) STAMP(27); \
    _Pragma("unroll") for (int i = 0; i < 4; ++i) B[(r) & 1][i] = tr_frag(sm + boff[i] + ((r) % 6) * 16384, 2048);      \
    if ((r) > 0) { half2(W2[(r) > 0 ? (r) - 1 : 0], B[((r) - 1) & 1], 0);                                               \
      _Pragma("unroll") for (int i = 0; i < 4; ++i) { __builtin_amdgcn_sched_group_barrier(0x8, 1, 0); __builtin_amdgcn_sched_group_barrier(0x100, 2, 0); } } \
    SCHED();                                                                                                            \
    if ((r) + 3 <= 10) load_h1((r) + 3);                                                                                \
    SCHED();                                                                                                            \
    if ((r) > 0) half2(W2[(r) > 0 ? (r) - 1 : 0], B[((r) - 1) & 1], 2);                                                 \
    SCHED();
    G2_ITER(0, 4) G2_ITER(1, 4) G2_ITER(2, 4) G2_ITER(3, 4) G2_ITER(4, 4) G2_ITER(5, 4)
    G2_ITER(6, 4) G2_ITER(7, 4) G2_ITER(8, 4) G2_ITER(9, 2) G2_ITER(10, 0)
#undef G2_ITER
    if (kq < 3) { half2(W2[10], B[0], 0); half2(W2[10], B[0], 2); }
    STAMP(13); CSTAMP(6);
    u32x2 ru[2];
#pragma unroll
    for (int i = 0; i < 2; ++i) {
      const int r = (dh * 2 + i) * 16 + rl;
      ru[i] = __builtin_bit_cast(u32x2, tr_read(sm + L_RES + r * 128 + ((kq ^ f3) << 5) + 8 * pp));
    }
    LBAR();
#pragma unroll
    for (int i = 0; i < 2; ++i)
#pragma unroll
      for (int jm = 0; jm < 4; ++jm) *(LAS f32x4*)(sm + L_EX2 + ((dh * 4 + jm) * 8 + kq * 2 + i) * 1024 + lane * 16) = acc[i][jm];
    LBAR();
    u32x2 ov[2];
#pragma unroll
    for (int i = 0; i < 2; ++i) {
      const LAS char* e = sm + L_EX2 + ((dh * 4 + kq) * 8 + i) * 1024 + lane * 16;
      const f32x4 a = (*(const LAS f32x4*)(e) + *(const LAS f32x4*)(e + 2048)) + (*(const LAS f32x4*)(e + 4096) + *(const LAS f32x4*)(e + 6144));
      ov[i] = (u32x2){pk_bf16(bf_lo(ru[i][0]) + rbf(a[0]), bf_hi(ru[i][0]) + rbf(a[1])), pk_bf16(bf_lo(ru[i][1]) + rbf(a[2]), bf_hi(ru[i][1]) + rbf(a[3]))};
    }
    const u32x4 o16 = widen16(ov[0], ov[1]);
    *(u32x4*)(p.out + (size_t)(md0 + kq * 16 + fr) * D_DIM + d0 + (dh * 2 + (g & 1)) * 16 + (g >> 1) * 8) = o16;
  }
  STAMP(14); CSTAMP(7);
}

extern "C" void kernel_launch(void* const* d_in, const int* in_sizes, int n_in, void* d_out, int out_size, void* d_ws, size_t ws_size,
                              hipStream_t stream) {
  Params p;
  p.q = (const u16*)d_in[0];
  p.k = (const u16*)d_in[1];
  p.v = (const u16*)d_in[2];
  p.shk = (const u16*)d_in[3];
  p.ssmk = (const u16*)d_in[4];
  p.ssmD = (const u16*)d_in[5];
  p.w1 = (const u16*)d_in[7];
  p.w2 = (const u16*)d_in[8];
  p.out = (u16*)d_out;
  p.ws = (char*)d_ws;
  (void)hipFuncSetAttribute((const void*)h3_fused, hipFuncAttributeMaxDynamicSharedMemorySize, LDS_BYTES);
  h3_fused<<<256, 512, LDS_BYTES, stream>>>(p);
}
```

```cpp
#include <hip/hip_runtime.h>
#include <hip/hip_bf16.h>
#include <stdint.h>
#include <type_traits>
#include <utility>

typedef unsigned short u16;
typedef short bf16x8 __attribute__((ext_vector_type(8)));
typedef short s16x4 __attribute__((ext_vector_type(4)));
typedef float f32x4 __attribute__((ext_vector_type(4)));
typedef float f32x2 __attribute__((ext_vector_type(2)));
typedef unsigned u32x4 __attribute__((ext_vector_type(4)));
typedef unsigned u32x2 __attribute__((ext_vector_type(2)));
typedef __bf16 bf16x2_t __attribute__((ext_vector_type(2)));
typedef __bf16 bf16x8_t __attribute__((ext_vector_type(8)));
typedef short v4i16_t __attribute__((ext_vector_type(4)));
#define LAS __attribute__((address_space(3)))
#define GAS __attribute__((address_space(1)))
typedef GAS unsigned gu32;
#define RLX_AGENT __ATOMIC_RELAXED, __HIP_MEMORY_SCOPE_AGENT

constexpr int S_LEN = 2048, D_DIM = 512, F_DIM = 1376;
constexpr int AUX_SC1 = 16;

constexpr unsigned WS_KVT = 0, WS_QT = 2u << 20, WS_HT = 4u << 20, WS_H1T = 6u << 20, WS_STQ = 12u << 20;
constexpr unsigned WS_FL1 = WS_STQ + 65536, WS_FL2 = WS_FL1 + 32768, WS_FL3 = WS_FL2 + 32768, WS_EPOCH = WS_FL3 + 32768;
constexpr unsigned WS_STAMP = WS_EPOCH + 4096;
constexpr unsigned WS_BYTES = 16u << 20;
constexpr int FLAG_STRIDE = 16;
constexpr unsigned SPIN_CAP = 1u << 18;

#define WAIT_V(n) asm volatile("s_waitcnt vmcnt(%0)" ::"n"(n) : "memory")
#define WAIT_L0() asm volatile("s_waitcnt lgkmcnt(0)" ::: "memory")
#define RAW_BAR() do { asm volatile("" ::: "memory"); __builtin_amdgcn_s_barrier(); asm volatile("" ::: "memory"); } while (0)
#define LBAR() do { WAIT_L0(); RAW_BAR(); } while (0)
#define SCHED() __builtin_amdgcn_sched_barrier(0)
template <int N> __device__ __forceinline__ void interleave_mfma_ds() {
#pragma unroll
  for (int i = 0; i < N; ++i) { __builtin_amdgcn_sched_group_barrier(0x8, 1, 0); __builtin_amdgcn_sched_group_barrier(0x100, 1, 0); }
}

#define STAMP(i)
#define CSTAMP(i)

__device__ __forceinline__ float bf_lo(unsigned x) { return __uint_as_float(x << 16); }
__device__ __forceinline__ float bf_hi(unsigned x) { return __uint_as_float(x & 0xffff0000u); }
__device__ __forceinline__ unsigned pk_bf16(float lo, float hi) {
  bf16x2_t v; v.x = (__bf16)lo; v.y = (__bf16)hi;
  return __builtin_bit_cast(unsigned, v);
}
__device__ __forceinline__ float rbf(float x) { return __uint_as_float(((unsigned)__builtin_bit_cast(u16, (__bf16)x)) << 16); }
__device__ __forceinline__ s16x4 tr_read(const LAS char* p) {
  return __builtin_bit_cast(s16x4, __builtin_amdgcn_ds_read_tr16_b64_v4i16((LAS v4i16_t*)p));
}
__device__ __forceinline__ bf16x8_t cat8(s16x4 a, s16x4 b) {
  bf16x8 r = __builtin_shufflevector(a, b, 0, 1, 2, 3, 4, 5, 6, 7);
  return __builtin_bit_cast(bf16x8_t, r);
}
__device__ __forceinline__ bf16x8_t tr_frag(const LAS char* p, int second) { return cat8(tr_read(p), tr_read(p + second)); }
__device__ __forceinline__ f32x4 mfma16(bf16x8_t a, bf16x8_t b, f32x4 c) {
  return __builtin_amdgcn_mfma_f32_16x16x32_bf16(a, b, c, 0, 0, 0);
}
template <int AUX>
__device__ __forceinline__ void dma16(const void* sbase, unsigned voff, unsigned lds) {
  if constexpr (AUX == AUX_SC1)
    asm volatile("s_mov_b32 m0, %2\n\ts_nop 0\n\tglobal_load_lds_dwordx4 %0, %1 sc1" ::"v"(voff), "s"(sbase), "s"(lds) : "memory");
  else
    asm volatile("s_mov_b32 m0, %2\n\ts_nop 0\n\tglobal_load_lds_dwordx4 %0, %1" ::"v"(voff), "s"(sbase), "s"(lds) : "memory");
}
__device__ __forceinline__ void dma4(const void* sbase, unsigned voff, unsigned lds) {
  asm volatile("s_mov_b32 m0, %2\n\ts_nop 0\n\tglobal_load_lds_dword %0, %1" ::"v"(voff), "s"(sbase), "s"(lds) : "memory");
}
__device__ __forceinline__ u32x4 ld128_asm(const void* sbase, unsigned voff) {
  u32x4 r; asm volatile("global_load_dwordx4 %0, %1, %2" : "=v"(r) : "v"(voff), "s"(sbase) : "memory"); return r;
}
__device__ __forceinline__ u32x4 ld128_sc1(const void* sbase, unsigned voff) {
  u32x4 r; asm volatile("global_load_dwordx4 %0, %1, %2 sc1" : "=v"(r) : "v"(voff), "s"(sbase) : "memory"); return r;
}
__device__ __forceinline__ u32x2 ld64_asm(const void* sbase, unsigned voff) {
  u32x2 r; asm volatile("global_load_dwordx2 %0, %1, %2" : "=v"(r) : "v"(voff), "s"(sbase) : "memory"); return r;
}
__device__ __forceinline__ u32x2 ld64_sc1(const void* sbase, unsigned voff) {
  u32x2 r; asm volatile("global_load_dwordx2 %0, %1, %2 sc1" : "=v"(r) : "v"(voff), "s"(sbase) : "memory"); return r;
}
template <int CTRL>
__device__ __forceinline__ float dpp_add(float x) {
  return x + __builtin_bit_cast(float, __builtin_amdgcn_mov_dpp(__builtin_bit_cast(int, x), CTRL, 0xf, 0xf, true));
}
__device__ __forceinline__ float wave_sum(float x) {
  x = dpp_add<0xB1>(x); x = dpp_add<0x4E>(x); x = dpp_add<0x141>(x); x = dpp_add<0x140>(x);
  auto s = __builtin_amdgcn_permlane16_swap(__float_as_uint(x), __float_as_uint(x), false, false);
  x = __uint_as_float(s[0]) + __uint_as_float(s[1]);
  auto u = __builtin_amdgcn_permlane32_swap(__float_as_uint(x), __float_as_uint(x), false, false);
  return __uint_as_float(u[0]) + __uint_as_float(u[1]);
}
__device__ __forceinline__ u32x4 widen16(u32x2 X, u32x2 Y) {
  auto a = __builtin_amdgcn_permlane16_swap(X[0], Y[0], false, false);
  auto b = __builtin_amdgcn_permlane16_swap(X[1], Y[1], false, false);
  return (u32x4){a[0], b[0], a[1], b[1]};
}

struct Params {
  const u16 *q, *k, *v, *shk, *ssmk, *ssmD, *w1, *w2;
  u16* out;
  char* ws;
};

__device__ __forceinline__ void wait_flags256(const char* ws, unsigned off, unsigned epoch, int lane) {
  gu32* f = (gu32*)(ws + off) + lane * FLAG_STRIDE;
#pragma unroll 1
  for (unsigned spins = 0; spins < SPIN_CAP; ++spins) {
    unsigned a = __hip_atomic_load(f, RLX_AGENT), b = __hip_atomic_load(f + 64 * FLAG_STRIDE, RLX_AGENT);
    unsigned c = __hip_atomic_load(f + 128 * FLAG_STRIDE, RLX_AGENT), d = __hip_atomic_load(f + 192 * FLAG_STRIDE, RLX_AGENT);
    if (__all((a == epoch) & (b == epoch) & (c == epoch) & (d == epoch))) break;
    __builtin_amdgcn_s_sleep(1);
  }
  __builtin_amdgcn_fence(__ATOMIC_ACQUIRE, "wavefront");
}

constexpr int L_KT = 32768, L_VT = L_KT + 72 * 128, L_QT = L_VT + 8192, L_OKV = L_QT + 8192, L_OQ = L_OKV + 8192, L_SS = L_OQ + 8192;
constexpr int H_ROW = 1152, H_COPY = 4728, H_BYTES = 9344, X_BYTES = 4160;
constexpr int L_H = 0, L_X = L_H + 2 * H_BYTES, L_QD = L_X + 2 * X_BYTES;
constexpr int CY_BYTES = 513 * 16;
constexpr int L_CY = L_QD + 16384;
constexpr int L_JUNKA = 76800;
constexpr int L_W1 = 77824;
constexpr int L_W1B = 0;
static_assert(L_CY + 4 * CY_BYTES <= L_JUNKA && L_JUNKA + 1024 <= L_W1 && L_W1 + 7 * 12288 <= 163840 && L_SS + 64 <= L_JUNKA, "lds");
static_assert(L_X + 2 * X_BYTES <= L_KT, "lds A/B overlap");
constexpr int L_W2A = 4 * 16384;
constexpr int L_CS = 147456;
constexpr int L_SST = L_CS + 1024;
constexpr int L_FST = L_SST + 2048;
constexpr int L_OUT1B = L_FST + 1024;
constexpr int L_JUNKC = L_OUT1B + 8192;
constexpr int L_EX1 = 0;
constexpr int L_OUT1A = 49152;
static_assert(L_JUNKC + 1024 <= 163840, "lds");
constexpr int L_RES = 155648;
constexpr int L_RING = 0;
constexpr int L_JUNKD = 147456;
constexpr int L_EX2 = 0;
constexpr int LDS_BYTES = 163840;

template <class F, int... I>
__device__ __forceinline__ void static_for_impl(F&& f, std::integer_sequence<int, I...>) { (f(std::integral_constant<int, I>{}), ...); }
template <int N, class F>
__device__ __forceinline__ void static_for(F&& f) { static_for_impl(f, std::make_integer_sequence<int, N>{}); }

template <int TS, int PAR, class F>
__device__ __forceinline__ void conv_main(const LAS char* xa, const LAS char* hb, f32x4 (&acc)[4][4], F&& job) {
  constexpr int U[4] = {TS == 0 ? 0 : 1, TS == 0 ? 3 : 2, TS == 0 ? 4 : 5, TS == 0 ? 7 : 6};
  constexpr int T[4] = {U[PAR ? 2 : 0], U[PAR ? 3 : 1], U[PAR ? 0 : 2], U[PAR ? 1 : 3]};
  constexpr int NA = U[3] + 1;
  bf16x8_t A[8], B[3][4];
#pragma unroll
  for (int p = 0; p < 4; ++p) B[0][p] = *(const LAS bf16x8_t*)(hb + p * H_ROW + 896);
#pragma unroll
  for (int a = NA - 1; a >= 0; --a) A[a] = tr_frag(xa + 512 * a, 32);
#pragma unroll
  for (int p = 0; p < 4; ++p) B[1][p] = *(const LAS bf16x8_t*)(hb + p * H_ROW + 896 - 128);
  SCHED();
  static_for<NA>([&](auto BB) {
    constexpr int bb = decltype(BB)::value;
    if (bb + 2 < NA) {
#pragma unroll
      for (int p = 0; p < 4; ++p) B[(bb + 2) % 3][p] = *(const LAS bf16x8_t*)(hb + p * H_ROW + 896 - 128 * (bb + 2));
    }
#pragma unroll
    for (int p = 0; p < 4; ++p)
#pragma unroll
      for (int j = 3; j >= 0; --j)
        if (T[j] >= bb) acc[j][p] = mfma16(A[T[j] - bb], B[bb % 3][p], acc[j][p]);
    if (bb + 2 < NA) interleave_mfma_ds<4>();
    SCHED();
    if constexpr (bb < 3) { job(std::integral_constant<int, 2 * bb>{}); job(std::integral_constant<int, 2 * bb + 1>{}); }
    else if constexpr (bb < 7) job(std::integral_constant<int, bb + 3>{});
    SCHED();
  });
}

__global__ __launch_bounds__(512, 2) void h3_fused(Params p) {
  extern __shared__ __attribute__((aligned(16))) char smem_[];
  LAS char* sm = (LAS char*)smem_;
  const unsigned lb = (unsigned)(size_t)sm;
  const int b = blockIdx.x, t = threadIdx.x, lane = t & 63;
  const int w = __builtin_amdgcn_readfirstlane(t >> 6);
  const int x = b & 7, jb = b >> 3;
  const int g = lane >> 4, fr = lane & 15, q_ = fr >> 2, pp = lane & 3;
  const int rl = 4 * g + q_;
  char* ws = p.ws;
  const __amdgpu_buffer_rsrc_t rs = __builtin_amdgcn_make_buffer_rsrc(ws, 0, WS_BYTES, 0x00020000);
  STAMP(0); CSTAMP(0);

  const int dB = x * 64 + jb * 2;
  {
    const unsigned vo = (lane >> 3) * (D_DIM * 2) + (lane & 7) * 16;
    const size_t so = ((size_t)(jb * 64 + w * 8) * D_DIM + x * 64);
    dma16<0>(p.k + so, vo, lb + L_KT + 1024 + w * 1024);
    dma16<0>(p.v + so, vo, lb + L_VT + w * 1024);
    dma16<0>(p.q + so, vo, lb + L_QT + w * 1024);
    if (w == 0) {
      if (jb > 0) dma16<0>(p.k + so - 8 * D_DIM, vo, lb + L_KT);
      else *(LAS u32x4*)(sm + L_KT + lane * 16) = (u32x4){0, 0, 0, 0};
    }
  }
  const int cb = w & 3, sl = 8 * (4 * (w >> 2) + g);
  u32x4 sq = ld128_asm(p.q + (size_t)(b * 8 + w) * D_DIM, lane * 16);
  u32x2 wv = ld64_asm(p.shk + (size_t)(x * 64 + cb * 16) * 4, fr * 8);
  if (w == 7) {
    unsigned long long pc; unsigned len;
    asm volatile("s_getpc_b64 %0\n.Lh3_ref_%=:\n\ts_mov_b32 %1, .Lh3_code_end-.Lh3_ref_%=" : "=s"(pc), "=s"(len));
    const unsigned nl = len >> 7;
    unsigned line = (unsigned)(jb * 8 + lane); line = line >= nl ? line - nl : line; line = line >= nl ? 0 : line;
    dma4((const void*)pc, line << 7, lb + L_JUNKA);
  }
  const unsigned epoch = __hip_atomic_load((gu32*)(ws + WS_EPOCH), RLX_AGENT) + 1u;
  const float Dv = bf_lo(p.ssmD[dB + (w >> 2)]);
  const bool c_on = jb < 30;
  const int panel = x * 2 + (jb >= 15 ? 1 : 0), ct = jb >= 15 ? jb - 15 : jb;
  const int m0 = panel * 128, n0 = ct * 96;
  unsigned w1o[6];
#pragma unroll
  for (int u = 0; u < 6; ++u) {
    const int idx = ((w & 1) * 6 + u) * 64 + lane, r = (idx * 43691) >> 19, cp = idx - r * 12;
    int col = n0 + (cp ^ (2 * ((r >> 2) & 1))) * 8;
    col = col > F_DIM - 8 ? F_DIM - 8 : col;
    w1o[u] = (r * F_DIM + col) * 2;
  }
  auto w1_half = [&](int chunk, int ldst, int step) {
#pragma unroll
    for (int u = 0; u < 6; ++u) dma16<0>(p.w1 + (size_t)chunk * 64 * F_DIM, w1o[u], lb + ldst + ((w & 1) * 6 + u) * step);
  };
  WAIT_V(0);
  asm volatile("" : "+v"(sq), "+v"(wv));
  {
    f32x2 xs[4];
#pragma unroll
    for (int i = 0; i < 4; ++i) xs[i] = (f32x2){bf_lo(sq[i]), bf_hi(sq[i])};
    const f32x2 s2 = (xs[0] + xs[1]) + (xs[2] + xs[3]);
    const float mean = wave_sum(s2[0] + s2[1]) * (1.0f / D_DIM);
    f32x2 vs = {0.f, 0.f};
#pragma unroll
    for (int i = 0; i < 4; ++i) { const f32x2 dd = xs[i] - mean; vs += dd * dd; }
    const float var = rbf(wave_sum(vs[0] + vs[1]) * (1.0f / D_DIM));
    if (lane == 0) *(LAS unsigned*)(sm + L_SS + w * 4) = pk_bf16(mean, __builtin_amdgcn_sqrtf(rbf(var + 1e-5f)));
  }
  LBAR();
  STAMP(1);
  {
    const int tro = q_ * 128 + cb * 32 + 8 * pp + sl * 128;
    const u32x2 k0 = __builtin_bit_cast(u32x2, tr_read(sm + L_KT + 4 * 128 + tro));
    const u32x2 k1 = __builtin_bit_cast(u32x2, tr_read(sm + L_KT + 8 * 128 + tro));
    const u32x2 k2 = __builtin_bit_cast(u32x2, tr_read(sm + L_KT + 12 * 128 + tro));
    const u32x2 v0 = __builtin_bit_cast(u32x2, tr_read(sm + L_VT + tro));
    const u32x2 v1 = __builtin_bit_cast(u32x2, tr_read(sm + L_VT + 4 * 128 + tro));
    const u32x2 q0 = __builtin_bit_cast(u32x2, tr_read(sm + L_QT + tro));
    const u32x2 q1 = __builtin_bit_cast(u32x2, tr_read(sm + L_QT + 4 * 128 + tro));
    const float w0 = bf_lo(wv[0]), w1 = bf_hi(wv[0]), w2 = bf_lo(wv[1]), w3 = bf_hi(wv[1]);
    const float kx[11] = {bf_hi(k0[0]), bf_lo(k0[1]), bf_hi(k0[1]), bf_lo(k1[0]), bf_hi(k1[0]), bf_lo(k1[1]), bf_hi(k1[1]),
                          bf_lo(k2[0]), bf_hi(k2[0]), bf_lo(k2[1]), bf_hi(k2[1])};
    const float vx[8] = {bf_lo(v0[0]), bf_hi(v0[0]), bf_lo(v0[1]), bf_hi(v0[1]), bf_lo(v1[0]), bf_hi(v1[0]), bf_lo(v1[1]), bf_hi(v1[1])};
    float o[8];
#pragma unroll
    for (int e = 0; e < 8; ++e) o[e] = (kx[e] * w3 + kx[e + 1] * w2 + kx[e + 2] * w1 + kx[e + 3] * w0) * vx[e];
    const int c = cb * 16 + fr;
    const int oo = c * 128 + (((sl >> 3) ^ (c & 7)) << 4);
    *(LAS u32x4*)(sm + L_OKV + oo) = (u32x4){pk_bf16(o[0], o[1]), pk_bf16(o[2], o[3]), pk_bf16(o[4], o[5]), pk_bf16(o[6], o[7])};
    *(LAS u32x4*)(sm + L_OQ + oo) = (u32x4){q0[0], q0[1], q1[0], q1[1]};
  }
  LBAR();
  STAMP(15);
  {
    const int ch = t >> 3, c = t & 7;
    const int off = ch * 128 + ((c ^ (ch & 7)) << 4);
    const u32x4 a = *(const LAS u32x4*)(sm + L_OKV + off), bq = *(const LAS u32x4*)(sm + L_OQ + off);
    const unsigned go = ((x * 64 + ch) * S_LEN + jb * 64 + c * 8) * 2;
    __builtin_amdgcn_raw_buffer_store_b128(a, rs, WS_KVT + go, 0, AUX_SC1);
    __builtin_amdgcn_raw_buffer_store_b128(bq, rs, WS_QT + go, 0, AUX_SC1);
    if (t < 2) __builtin_amdgcn_raw_buffer_store_b128(*(const LAS u32x4*)(sm + L_SS + t * 16), rs, WS_STQ + b * 32 + t * 16, 0, AUX_SC1);
  }
  STAMP(31);
  WAIT_V(0);
  LBAR();
  if (t == 0) __hip_atomic_store((gu32*)(ws + WS_FL1) + b * FLAG_STRIDE, epoch, RLX_AGENT);
  RAW_BAR();
  STAMP(2);
  {
    const int hc = t >> 8, hw = t & 255;
    u32x4 hv = ld128_asm(p.ssmk + (size_t)dB * S_LEN, (hc * S_LEN + hw * 8) * 2);
    if (w < 4) { w1_half(w >> 1, L_W1 + (w >> 1) * 12288, 1024); WAIT_V(6); } else { WAIT_V(0); }
    asm volatile("" : "+v"(hv));
    LAS char* hb = sm + L_H + hc * H_BYTES + 1020 - 4 * hw;
    const unsigned h0 = __builtin_amdgcn_perm(hv[1], hv[3], 0x07060302u), h1 = __builtin_amdgcn_perm(hv[1], hv[3], 0x05040100u);
    const unsigned h2 = __builtin_amdgcn_perm(hv[0], hv[2], 0x07060302u), h3 = __builtin_amdgcn_perm(hv[0], hv[2], 0x05040100u);
    *(LAS unsigned*)(hb + 0 * H_ROW) = h0; *(LAS unsigned*)(hb + H_COPY + 0 * H_ROW) = h0;
    *(LAS unsigned*)(hb + 1 * H_ROW) = h1; *(LAS unsigned*)(hb + H_COPY + 1 * H_ROW) = h1;
    *(LAS unsigned*)(hb + 2 * H_ROW) = h2; *(LAS unsigned*)(hb + H_COPY + 2 * H_ROW) = h2;
    *(LAS unsigned*)(hb + 3 * H_ROW) = h3; *(LAS unsigned*)(hb + H_COPY + 3 * H_ROW) = h3;
    if (t < 256) {
      const int r = t >> 4;
      *(LAS u32x2*)(sm + L_H + (r >> 3) * H_BYTES + ((r >> 2) & 1) * H_COPY + (r & 3) * H_ROW + 1024 + (t & 15) * 8) = (u32x2){0, 0};
    } else if (t < 260) *(LAS u32x4*)(sm + L_X + ((t - 256) >> 1) * X_BYTES + (t & 1) * 16) = (u32x4){0, 0, 0, 0};
    else if (t < 264) *(LAS u32x4*)(sm + L_CY + (t - 260) * CY_BYTES) = (u32x4){0, 0, 0, 0};
  }

  const int c2 = w >> 2, ts = (w >> 1) & 1, par = w & 1;
  const int tl0 = ts == 0 ? 0 : 1, tl1 = ts == 0 ? 3 : 2, tl2 = ts == 0 ? 4 : 5, tl3 = ts == 0 ? 7 : 6;
  const int tf0 = par ? tl2 : tl0, tf1 = par ? tl3 : tl1, tg0 = par ? tl0 : tl2, tg1 = par ? tl1 : tl3;
  const int sa = tf0 * 256 + fr * 16 + g * 4, sb = tf1 * 256 + fr * 16 + g * 4;
  if (w == 0) wait_flags256(ws, WS_FL1, epoch, lane);
  LBAR();
  STAMP(3);
  {
    const int d = dB + c2;
    dma16<AUX_SC1>(ws + WS_KVT + ((size_t)d * S_LEN + (w & 3) * 512) * 2, lane * 16, lb + L_X + c2 * X_BYTES + 32 + (w & 3) * 1024);
    u32x2 qv0 = ld64_sc1(ws + WS_QT + (size_t)d * S_LEN * 2, sa * 2), qv1 = ld64_sc1(ws + WS_QT + (size_t)d * S_LEN * 2, sb * 2);
    u32x4 sv0 = ld128_sc1(ws + WS_STQ, sa * 4), sv1 = ld128_sc1(ws + WS_STQ, sb * 4);
    WAIT_V(4);
    RAW_BAR();
    STAMP(4); CSTAMP(1);
    f32x4 acc[4][4];
#pragma unroll
    for (int j = 0; j < 4; ++j)
#pragma unroll
      for (int pz = 0; pz < 4; ++pz) acc[j][pz] = (f32x4){0, 0, 0, 0};
    const LAS char* xa = sm + L_X + c2 * X_BYTES + 8 + 64 * g + 8 * q_ + 8 * pp + 256 * par;
    const LAS char* hb = sm + L_H + c2 * H_BYTES + ((fr & 1) ? 0 : H_COPY) + 1016 - 8 * fr + 16 * g + 64 * par - 896;
    auto job = [&](auto J) {
      constexpr int j = decltype(J)::value;
      if constexpr (j < 6) {
        const int ch = 2 + (w >> 1);
        dma16<0>(p.w1 + (size_t)ch * 64 * F_DIM, w1o[j], lb + L_W1 + ch * 12288 + ((w & 1) * 6 + j) * 1024);
      } else if constexpr (j < 9) {
        if (w < 4) {
          const int u = (w >> 1) * 3 + (j - 6);
          dma16<0>(p.w1 + (size_t)6 * 64 * F_DIM, (w & 2) ? w1o[3 + j - 6] : w1o[j - 6], lb + L_W1 + 6 * 12288 + ((w & 1) * 6 + u) * 1024);
        }
      } else {
        int r2 = ((b * 43) >> 3) + w; r2 = r2 > F_DIM - 1 ? F_DIM - 1 : r2;
        dma16<0>(p.w2 + (size_t)r2 * D_DIM, lane * 16, lb + L_JUNKA);
      }
    };
    if (ts == 0) { if (par == 0) conv_main<0, 0>(xa, hb, acc, job); else conv_main<0, 1>(xa, hb, acc, job); }
    else         { if (par == 0) conv_main<1, 0>(xa, hb, acc, job); else conv_main<1, 1>(xa, hb, acc, job); }
    STAMP(5); CSTAMP(2);
#pragma unroll
    for (int j = 0; j < 4; ++j) {
      acc[j][3][1] += acc[j][2][0];
      acc[j][3][2] += acc[j][2][1] + acc[j][1][0];
      acc[j][3][3] += (acc[j][2][2] + acc[j][1][1]) + acc[j][0][0];
      acc[j][0][1] += acc[j][2][3] + acc[j][1][2];
      acc[j][0][2] += acc[j][1][3];
    }
    LAS char* cy = sm + L_CY + (c2 * 2 + par) * CY_BYTES + 16 + lane * 16;
    *(LAS f32x4*)(cy + tf0 * 1024) = acc[0][0];
    *(LAS f32x4*)(cy + tf1 * 1024) = acc[1][0];
    *(LAS f32x4*)(cy + tg0 * 1024) = acc[2][0];
    *(LAS f32x4*)(cy + tg1 * 1024) = acc[3][0];
    LAS char* qd = sm + L_QD + w * 2048 + lane * 16;
    *(LAS f32x4*)(qd) = acc[2][3]; *(LAS f32x4*)(qd + 1024) = acc[3][3];
    const u32x2 xv0 = *(const LAS u32x2*)(sm + L_X + c2 * X_BYTES + 32 + sa * 2), xv1 = *(const LAS u32x2*)(sm + L_X + c2 * X_BYTES + 32 + sb * 2);
    LBAR();
    STAMP(29);
    const int prev = g > 0 ? lane - 16 : (fr > 0 ? 47 + fr : -1);
    const LAS char* cy0 = sm + L_CY + (c2 * 2) * CY_BYTES + 16 + prev * 16;
    const LAS char* qs = sm + L_QD + (w ^ 1) * 2048 + lane * 16;
    WAIT_V(7);
    asm volatile("" : "+v"(qv0), "+v"(qv1), "+v"(sv0), "+v"(sv1));
    u32x2 ov[2];
#pragma unroll
    for (int i = 0; i < 2; ++i) {
      const int tf = i ? tf1 : tf0;
      const f32x4 pq = *(const LAS f32x4*)(qs + i * 1024);
      const f32x4 ca = *(const LAS f32x4*)(cy0 + tf * 1024), cb_ = *(const LAS f32x4*)(cy0 + CY_BYTES + tf * 1024);
      f32x4 y = acc[i][3] + pq;
      y[0] += ca[1] + cb_[1]; y[1] += ca[2] + cb_[2]; y[2] += ca[3] + cb_[3];
      const u32x2 xv = i ? xv1 : xv0, qv = i ? qv1 : qv0;
      const u32x4 sv = i ? sv1 : sv0;
      const float xx[4] = {bf_lo(xv[0]), bf_hi(xv[0]), bf_lo(xv[1]), bf_hi(xv[1])};
      const float qx[4] = {bf_lo(qv[0]), bf_hi(qv[0]), bf_lo(qv[1]), bf_hi(qv[1])};
      float o[4];
#pragma unroll
      for (int e = 0; e < 4; ++e) o[e] = qx[e] + ((qx[e] - bf_lo(sv[e])) * __builtin_amdgcn_rcpf(bf_hi(sv[e]))) * (y[e] + xx[e] * Dv);
      ov[i] = (u32x2){pk_bf16(o[0], o[1]), pk_bf16(o[2], o[3])};
    }
    const u32x4 o16 = widen16(ov[0], ov[1]);
    const int so = ((g & 1) ? tf1 : tf0) * 256 + fr * 16 + (g >> 1) * 8;
    __builtin_amdgcn_raw_buffer_store_b128(o16, rs, WS_HT + (d * S_LEN + so) * 2, 0, AUX_SC1);
    STAMP(30);
  }
  WAIT_V(0);
  LBAR();
  if (t == 0) __hip_atomic_store((gu32*)(ws + WS_FL2) + b * FLAG_STRIDE, epoch, RLX_AGENT);
  RAW_BAR();
  STAMP(6);

  const int kp = w >> 2, wm = (w >> 1) & 1, wn = w & 1;
  const int mt = x * 4 + (jb >> 3), dt = jb & 7;
  const int md0 = mt * 64, d0 = dt * 64;
  const int kq = w >> 1, dh = w & 1;
  unsigned wvo[2], hvo[2];
#pragma unroll
  for (int u = 0; u < 2; ++u) {
    const int r = ((w & 1) * 2 + u) * 8 + (lane >> 3), c = ((lane & 7) ^ (2 * ((r >> 1) & 3))) * 16;
    wvo[u] = r * (D_DIM * 2) + c; hvo[u] = r * (S_LEN * 2) + c;
  }
  auto load_w2 = [&](int step, int lds) {
    int ks = kq * 11 + step; ks = ks > 42 ? 42 : ks;
#pragma unroll
    for (int u = 0; u < 2; ++u) dma16<0>(p.w2 + (size_t)ks * 32 * D_DIM + d0, wvo[u], lb + lds + kq * 4096 + ((w & 1) * 2 + u) * 1024);
  };
  const bf16x8_t ONES = __builtin_bit_cast(bf16x8_t, (u32x4){0x3f803f80u, 0x3f803f80u, 0x3f803f80u, 0x3f803f80u});
  if (c_on) {
    bf16x8_t W1[8][3];
    f32x4 csa[3];
    if (w < 2) w1_half(7, L_W1B, 1024);
#pragma unroll
    for (int i = 0; i < 3; ++i) csa[i] = (f32x4){0, 0, 0, 0};
    int wo[3];
#pragma unroll
    for (int i = 0; i < 3; ++i) wo[i] = (kp * 32 + rl) * 192 + 8 * pp + (((wn * 3 + i) ^ ((rl >> 2) & 1)) << 5);
#pragma unroll
    for (int c = 0; c < 7; ++c)
#pragma unroll
      for (int i = 0; i < 3; ++i) {
        W1[c][i] = tr_frag(sm + L_W1 + c * 12288 + wo[i], 16 * 192);
        csa[i] = mfma16(ONES, W1[c][i], csa[i]);
      }
    WAIT_V(0);
    RAW_BAR();
    STAMP(28);
#pragma unroll
    for (int i = 0; i < 3; ++i) {
      W1[7][i] = tr_frag(sm + L_W1B + wo[i], 16 * 192);
      csa[i] = mfma16(ONES, W1[7][i], csa[i]);
    }
    LBAR();
    load_w2(0, L_W2A); load_w2(1, L_W2A + 16384); load_w2(2, L_W2A + 2 * 16384);
    STAMP(7);
    if (w == 0) wait_flags256(ws, WS_FL2, epoch, lane);
    LBAR();
    STAMP(8);
    if (b == 0 && t == 0) __hip_atomic_store((gu32*)(ws + WS_EPOCH), epoch, RLX_AGENT);
    const char* hsrc = ws + WS_HT + ((size_t)(w * 8) * S_LEN + m0) * 2;
    const int ra = lane >> 4, rb = ra + 4;
    const unsigned va = ra * (S_LEN * 2) + (((lane & 15) ^ (2 * ra)) << 4), vb = rb * (S_LEN * 2) + (((lane & 15) ^ (2 * rb)) << 4);
    auto load_ht = [&](int c) {
      dma16<AUX_SC1>(hsrc + (size_t)c * (64 * S_LEN * 2), va, lb + (c & 3) * 16384 + (w * 2) * 1024);
      dma16<AUX_SC1>(hsrc + (size_t)c * (64 * S_LEN * 2), vb, lb + (c & 3) * 16384 + (w * 2 + 1) * 1024);
    };
    load_ht(0); load_ht(1);
    if (wm == 0 && g == 0) {
#pragma unroll
      for (int i = 0; i < 3; ++i) *(LAS float*)(sm + L_CS + (kp * 96 + wn * 48 + i * 16 + fr) * 4) = csa[i][0];
    }
    f32x4 acc[4][3], sta[4];
    int aoff[4];
#pragma unroll
    for (int i = 0; i < 4; ++i) {
      sta[i] = (f32x4){0, 0, 0, 0};
      aoff[i] = (kp * 32 + rl) * 256 + (((wm * 4 + (i ^ (2 * kp))) ^ (rl & 7)) << 5) + 8 * pp;
#pragma unroll
      for (int jn = 0; jn < 3; ++jn) acc[i][jn] = (f32x4){0, 0, 0, 0};
    }
    auto g1_loop = [&](auto WN) {
      bf16x8_t A[2][4];
      auto half = [&](bf16x8_t (&Af)[4], const bf16x8_t (&Wf)[3], int i0) {
#pragma unroll
        for (int i = i0; i < i0 + 2; ++i) {
#pragma unroll
          for (int jn = 0; jn < 3; ++jn) acc[i][jn] = mfma16(Af[i], Wf[jn], acc[i][jn]);
          sta[i] = mfma16(Af[i], decltype(WN)::value ? Af[i] : ONES, sta[i]);
        }
      };
      auto warm_w2 = [&](int i) {
        int ks = kq * 11 + 5 + 2 * i + (w & 1); ks = ks > 42 ? 42 : ks;
        dma16<0>(p.w2 + (size_t)(ks * 32 + (jb >> 3) * 8) * D_DIM + d0, (lane >> 3) * (D_DIM * 2) + (lane & 7) * 16, lb + L_JUNKC);
      };
#define G1_ITER(c, N)                                                                                        \
      WAIT_V(N); RAW_BAR();                                                                                  \
      if ((c) == 0) STAMP(16); if ((c) == 1) { STAMP(17); CSTAMP(3); } if ((c) == 4) STAMP(18); if ((c) == 7) STAMP(19); \
      _Pragma("unroll") for (int i = 0; i < 4; ++i) A[(c) & 1][i] = tr_frag(sm + aoff[i] + ((c) & 3) * 16384, 4096); \
      if ((c) > 0) { half(A[((c) - 1) & 1], W1[(c) > 0 ? (c) - 1 : 0], 0); interleave_mfma_ds<8>(); }        \
      SCHED();                                                                                               \
      if ((c) + 2 < 8) load_ht((c) + 2);                                                                     \
      if ((c) == 1) load_w2(3, L_W2A + 3 * 16384);                                                           \
      if ((c) == 2) load_w2(4, L_W2A + 4 * 16384);                                                           \
      if ((c) >= 4 && (c) <= 6) warm_w2((c) - 4);                                                            \
      SCHED();                                                                                               \
      if ((c) > 0) half(A[((c) - 1) & 1], W1[(c) > 0 ? (c) - 1 : 0], 2);                                     \
      SCHED();
      G1_ITER(0, 2) G1_ITER(1, 2) G1_ITER(2, 4) G1_ITER(3, 6) G1_ITER(4, 4) G1_ITER(5, 3) G1_ITER(6, 4) G1_ITER(7, 2)
#undef G1_ITER
      half(A[1], W1[7], 0); half(A[1], W1[7], 2);
    };
    if (wn) g1_loop(std::integral_constant<bool, true>{}); else g1_loop(std::integral_constant<bool, false>{});
    STAMP(9); CSTAMP(4);
    LBAR();
    {
      LAS char* sp = sm + L_SST + ((kp * 2 + wn) * 128 + wm * 64) * 4 + kp * 128, *sq_ = sp - kp * 256;
      if (wn == 0) {
        if (fr == 0) {
          *(LAS f32x4*)(sp + g * 16) = sta[0]; *(LAS f32x4*)(sp + 64 + g * 16) = sta[1];
          *(LAS f32x4*)(sq_ + 128 + g * 16) = sta[2]; *(LAS f32x4*)(sq_ + 192 + g * 16) = sta[3];
        }
      } else if (q_ == g) {
        float dg[4];
#pragma unroll
        for (int i = 0; i < 4; ++i) dg[i] = pp == 0 ? sta[i][0] : (pp == 1 ? sta[i][1] : (pp == 2 ? sta[i][2] : sta[i][3]));
        *(LAS float*)(sp + fr * 4) = dg[0]; *(LAS float*)(sp + 64 + fr * 4) = dg[1];
        *(LAS float*)(sq_ + 128 + fr * 4) = dg[2]; *(LAS float*)(sq_ + 192 + fr * 4) = dg[3];
      }
    }
#pragma unroll
    for (int i = 0; i < 2; ++i)
#pragma unroll
      for (int jn = 0; jn < 3; ++jn) *(LAS f32x4*)(sm + L_EX1 + w * 6144 + (i * 3 + jn) * 1024 + lane * 16) = acc[2 + i][jn];
    LBAR();
    STAMP(20);
    if (t < 128) {
      const LAS float* sp = (const LAS float*)(sm + L_SST) + t;
      const float mean = (sp[0] + sp[256]) * (1.0f / D_DIM);
      const float var = fmaxf((sp[128] + sp[384]) * (1.0f / D_DIM) - mean * mean, 0.f);
      *(LAS float*)(sm + L_FST + t * 4) = -rbf(mean);
      *(LAS float*)(sm + L_FST + 512 + t * 4) = -1.44269504f * __builtin_amdgcn_rcpf(rbf(__builtin_amdgcn_sqrtf(rbf(rbf(var) + 1e-5f))));
    }
    f32x2 csv[3];
#pragma unroll
    for (int jn = 0; jn < 3; ++jn) {
      const int nl = wn * 48 + jn * 16 + fr;
      const float c = *(const LAS float*)(sm + L_CS + nl * 4) + *(const LAS float*)(sm + L_CS + (96 + nl) * 4);
      csv[jn] = (f32x2){c, c};
    }
#pragma unroll
    for (int i = 0; i < 2; ++i)
#pragma unroll
      for (int jn = 0; jn < 3; ++jn) acc[i][jn] += *(const LAS f32x4*)(sm + L_EX1 + (w ^ 4) * 6144 + (i * 3 + jn) * 1024 + lane * 16);
    LBAR();
    u32x2 ov[2][3];
#pragma unroll
    for (int i = 0; i < 2; ++i) {
      const int ml = wm * 64 + (kp * 2 + i) * 16 + g * 4;
      const f32x4 nmu = *(const LAS f32x4*)(sm + L_FST + ml * 4), zz = *(const LAS f32x4*)(sm + L_FST + 512 + ml * 4);
      const f32x2 nm0 = {nmu[0], nmu[1]}, nm1 = {nmu[2], nmu[3]}, z0 = {zz[0], zz[1]}, z1 = {zz[2], zz[3]};
      const f32x2 cL = {-1.44269504f, -1.44269504f};
#pragma unroll
      for (int jn = 0; jn < 3; ++jn) {
        const f32x4 a = acc[i][jn];
        const f32x2 e0 = (nm0 * csv[jn] + (f32x2){a[0], a[1]}) * z0, e1 = (nm1 * csv[jn] + (f32x2){a[2], a[3]}) * z1;
        const f32x2 d0_ = (f32x2){__builtin_amdgcn_exp2f(e0[0]), __builtin_amdgcn_exp2f(e0[1])} * cL + cL;
        const f32x2 d1_ = (f32x2){__builtin_amdgcn_exp2f(e1[0]), __builtin_amdgcn_exp2f(e1[1])} * cL + cL;
        const f32x2 o0 = e0 * (f32x2){__builtin_amdgcn_rcpf(d0_[0]), __builtin_amdgcn_rcpf(d0_[1])};
        const f32x2 o1 = e1 * (f32x2){__builtin_amdgcn_rcpf(d1_[0]), __builtin_amdgcn_rcpf(d1_[1])};
        ov[i][jn] = (u32x2){pk_bf16(o0[0], o0[1]), pk_bf16(o1[0], o1[1])};
      }
    }
#pragma unroll
    for (int jn = 0; jn < 3; ++jn) {
      const u32x4 o16 = widen16(ov[0][jn], ov[1][jn]);
      const int nl = wn * 48 + jn * 16 + fr;
      const int ch = wm * 8 + (kp * 2 + (g & 1)) * 2 + (g >> 1);
      const int base = nl < 64 ? L_OUT1A + nl * 256 : L_OUT1B + (nl - 64) * 256;
      *(LAS u32x4*)(sm + base + ((ch ^ (nl & 7)) << 4)) = o16;
    }
    LBAR();
    STAMP(21);
#pragma unroll
    for (int i = 0; i < 3; ++i) {
      const int nl = i * 32 + (t >> 4), ch = t & 15;
      const int base = i < 2 ? L_OUT1A + nl * 256 : L_OUT1B + (nl - 64) * 256;
      const u32x4 o16 = *(const LAS u32x4*)(sm + base + ((ch ^ (nl & 7)) << 4));
      if (n0 + nl < F_DIM) __builtin_amdgcn_raw_buffer_store_b128(o16, rs, WS_H1T + ((n0 + nl) * S_LEN + m0 + ch * 8) * 2, 0, AUX_SC1);
    }
  } else {
    if (w == 0) wait_flags256(ws, WS_FL2, epoch, lane);
    RAW_BAR();
    load_w2(0, L_W2A); load_w2(1, L_W2A + 16384); load_w2(2, L_W2A + 2 * 16384); load_w2(3, L_W2A + 3 * 16384); load_w2(4, L_W2A + 4 * 16384);
  }
  WAIT_V(0);
  STAMP(22);
  LBAR();
  if (c_on && t == 0) __hip_atomic_store((gu32*)(ws + WS_FL3) + b * FLAG_STRIDE, epoch, RLX_AGENT);
  RAW_BAR();
  STAMP(10);
  if (c_on) {
#pragma unroll
    for (int i = 0; i < 3; ++i) {
      int nn = n0 + i * 32 + w * 4; nn = nn > F_DIM - 4 ? F_DIM - 4 : nn;
      dma16<AUX_SC1>(ws + WS_H1T + ((size_t)nn * S_LEN + m0) * 2, (lane >> 4) * (S_LEN * 2) + (lane & 15) * 16, lb + L_JUNKD + w * 1024);
    }
  }

  {
    load_w2(5, 0); load_w2(6, 16384); load_w2(7, 2 * 16384); load_w2(8, 3 * 16384);
    const int f3 = (rl >> 1) & 3;
    bf16x8_t W2[11][2];
    int woff[2], boff[4];
#pragma unroll
    for (int i = 0; i < 2; ++i) woff[i] = kq * 4096 + rl * 128 + (((dh * 2 + i) ^ f3) << 5) + 8 * pp;
#pragma unroll
    for (int i = 0; i < 4; ++i) boff[i] = L_RING + kq * 4096 + rl * 128 + ((i ^ f3) << 5) + 8 * pp;
#pragma unroll
    for (int s = 0; s < 5; ++s)
#pragma unroll
      for (int i = 0; i < 2; ++i) W2[s][i] = tr_frag(sm + woff[i] + L_W2A + s * 16384, 2048);
    LBAR();
    load_w2(9, 4 * 16384); load_w2(10, 5 * 16384);
    {
      const int r = lane >> 3;
      dma16<AUX_SC1>(ws + WS_HT + ((size_t)(d0 + w * 8) * S_LEN + md0) * 2, r * (S_LEN * 2) + (((lane & 7) ^ (2 * ((r >> 1) & 3))) << 4), lb + L_RES + w * 1024);
    }
    WAIT_V(5);
    RAW_BAR();
    STAMP(23);
#pragma unroll
    for (int s = 5; s < 9; ++s)
#pragma unroll
      for (int i = 0; i < 2; ++i) W2[s][i] = tr_frag(sm + woff[i] + (s - 5) * 16384, 2048);
    WAIT_V(0);
    RAW_BAR();
#pragma unroll
    for (int s = 9; s < 11; ++s)
#pragma unroll
      for (int i = 0; i < 2; ++i) W2[s][i] = tr_frag(sm + woff[i] + (s - 5) * 16384, 2048);
    STAMP(11);
    if (w == 0) {
      const int h = (mt >> 1) & 1;
      gu32* f = (gu32*)(ws + WS_FL3) + (x + 8 * (15 * h + (lane < 15 ? lane : 0))) * FLAG_STRIDE;
#pragma unroll 1
      for (unsigned spins = 0; spins < SPIN_CAP; ++spins) {
        unsigned a = __hip_atomic_load(f, RLX_AGENT);
        if (__all(a == epoch)) break;
        __builtin_amdgcn_s_sleep(1);
      }
      __builtin_amdgcn_fence(__ATOMIC_ACQUIRE, "wavefront");
    }
    LBAR();
    STAMP(12);
    auto load_h1 = [&](int step) {
      int ks = kq * 11 + step; ks = ks > 42 ? 42 : ks;
#pragma unroll
      for (int u = 0; u < 2; ++u)
        dma16<AUX_SC1>(ws + WS_H1T + ((size_t)ks * 32 * S_LEN + md0) * 2, hvo[u], lb + L_RING + (step % 6) * 16384 + kq * 4096 + ((w & 1) * 2 + u) * 1024);
    };
    load_h1(0); load_h1(1); load_h1(2);
    f32x4 acc[2][4];
#pragma unroll
    for (int i = 0; i < 2; ++i)
#pragma unroll
      for (int jm = 0; jm < 4; ++jm) acc[i][jm] = (f32x4){0, 0, 0, 0};
    bf16x8_t B[2][4];
    auto half2 = [&](const bf16x8_t (&Wf)[2], const bf16x8_t (&Bf)[4], int j0) {
#pragma unroll
      for (int jm = j0; jm < j0 + 2; ++jm)
#pragma unroll
        for (int i = 0; i < 2; ++i) acc[i][jm] = mfma16(Wf[i], Bf[jm], acc[i][jm]);
    };
#define G2_ITER(r, N)                                                                                                   \
    WAIT_V(N); LBAR();                                                                                                  \
    if ((r) == 0) STAMP(24); if ((r) == 1) { STAMP(25); CSTAMP(5); } if ((r) == 5) STAMP(26); if ((r) == 10) STAMP(27); \
    _Pragma("unroll") for (int i = 0; i < 4; ++i) B[(r) & 1][i] = tr_frag(sm + boff[i] + ((r) % 6) * 16384, 2048);      \
    if ((r) > 0) { half2(W2[(r) > 0 ? (r) - 1 : 0], B[((r) - 1) & 1], 0);                                               \
      _Pragma("unroll") for (int i = 0; i < 4; ++i) { __builtin_amdgcn_sched_group_barrier(0x8, 1, 0); __builtin_amdgcn_sched_group_barrier(0x100, 2, 0); } } \
    SCHED();                                                                                                            \
    if ((r) + 3 <= 10) load_h1((r) + 3);                                                                                \
    SCHED();                                                                                                            \
    if ((r) > 0) half2(W2[(r) > 0 ? (r) - 1 : 0], B[((r) - 1) & 1], 2);                                                 \
    SCHED();
    G2_ITER(0, 4) G2_ITER(1, 4) G2_ITER(2, 4) G2_ITER(3, 4) G2_ITER(4, 4) G2_ITER(5, 4)
    G2_ITER(6, 4) G2_ITER(7, 4) G2_ITER(8, 4) G2_ITER(9, 2) G2_ITER(10, 0)
#undef G2_ITER
    if (kq < 3) { half2(W2[10], B[0], 0); half2(W2[10], B[0], 2); }
    STAMP(13); CSTAMP(6);
    u32x2 ru[2];
#pragma unroll
    for (int i = 0; i < 2; ++i) {
      const int r = (dh * 2 + i) * 16 + rl;
      ru[i] = __builtin_bit_cast(u32x2, tr_read(sm + L_RES + r * 128 + ((kq ^ f3) << 5) + 8 * pp));
    }
    LBAR();
#pragma unroll
    for (int i = 0; i < 2; ++i)
#pragma unroll
      for (int jm = 0; jm < 4; ++jm) *(LAS f32x4*)(sm + L_EX2 + ((dh * 4 + jm) * 8 + kq * 2 + i) * 1024 + lane * 16) = acc[i][jm];
    LBAR();
    u32x2 ov[2];
#pragma unroll
    for (int i = 0; i < 2; ++i) {
      const LAS char* e = sm + L_EX2 + ((dh * 4 + kq) * 8 + i) * 1024 + lane * 16;
      const f32x4 a = (*(const LAS f32x4*)(e) + *(const LAS f32x4*)(e + 2048)) + (*(const LAS f32x4*)(e + 4096) + *(const LAS f32x4*)(e + 6144));
      ov[i] = (u32x2){pk_bf16(bf_lo(ru[i][0]) + rbf(a[0]), bf_hi(ru[i][0]) + rbf(a[1])), pk_bf16(bf_lo(ru[i][1]) + rbf(a[2]), bf_hi(ru[i][1]) + rbf(a[3]))};
    }
    const u32x4 o16 = widen16(ov[0], ov[1]);
    *(u32x4*)(p.out + (size_t)(md0 + kq * 16 + fr) * D_DIM + d0 + (dh * 2 + (g & 1)) * 16 + (g >> 1) * 8) = o16;
  }
  asm volatile(".Lh3_code_end:");
  STAMP(14); CSTAMP(7);
}

extern "C" void kernel_launch(void* const* d_in, const int* in_sizes, int n_in, void* d_out, int out_size, void* d_ws, size_t ws_size,
                              hipStream_t stream) {
  Params p;
  p.q = (const u16*)d_in[0];
  p.k = (const u16*)d_in[1];
  p.v = (const u16*)d_in[2];
  p.shk = (const u16*)d_in[3];
  p.ssmk = (const u16*)d_in[4];
  p.ssmD = (const u16*)d_in[5];
  p.w1 = (const u16*)d_in[7];
  p.w2 = (const u16*)d_in[8];
  p.out = (u16*)d_out;
  p.ws = (char*)d_ws;
  (void)hipFuncSetAttribute((const void*)h3_fused, hipFuncAttributeMaxDynamicSharedMemorySize, LDS_BYTES);
  h3_fused<<<256, 512, LDS_BYTES, stream>>>(p);
}
```

```cpp
#include <hip/hip_runtime.h>
#include <hip/hip_bf16.h>
#include <stdint.h>
#include <type_traits>
#include <utility>

typedef unsigned short u16;
typedef short bf16x8 __attribute__((ext_vector_type(8)));
typedef short s16x4 __attribute__((ext_vector_type(4)));
typedef float f32x4 __attribute__((ext_vector_type(4)));
typedef float f32x2 __attribute__((ext_vector_type(2)));
typedef unsigned u32x4 __attribute__((ext_vector_type(4)));
typedef unsigned u32x2 __attribute__((ext_vector_type(2)));
typedef __bf16 bf16x2_t __attribute__((ext_vector_type(2)));
typedef __bf16 bf16x8_t __attribute__((ext_vector_type(8)));
typedef short v4i16_t __attribute__((ext_vector_type(4)));
#define LAS __attribute__((address_space(3)))
#define GAS __attribute__((address_space(1)))
typedef GAS unsigned gu32;
#define RLX_AGENT __ATOMIC_RELAXED, __HIP_MEMORY_SCOPE_AGENT

constexpr int S_LEN = 2048, D_DIM = 512, F_DIM = 1376;
constexpr int AUX_SC1 = 16;

constexpr unsigned WS_KVT = 0, WS_QT = 2u << 20, WS_HT = 4u << 20, WS_H1T = 6u << 20, WS_STQ = 12u << 20;
constexpr unsigned WS_FL1 = WS_STQ + 65536, WS_FL2 = WS_FL1 + 32768, WS_FL3 = WS_FL2 + 32768, WS_EPOCH = WS_FL3 + 32768;
constexpr unsigned WS_STAMP = WS_EPOCH + 4096;
constexpr unsigned WS_BYTES = 16u << 20;
constexpr int FLAG_STRIDE = 16;
constexpr unsigned SPIN_CAP = 1u << 18;

#define WAIT_V(n) asm volatile("s_waitcnt vmcnt(%0)" ::"n"(n) : "memory")
#define WAIT_L0() asm volatile("s_waitcnt lgkmcnt(0)" ::: "memory")
#define RAW_BAR() do { asm volatile("" ::: "memory"); __builtin_amdgcn_s_barrier(); asm volatile("" ::: "memory"); } while (0)
#define LBAR() do { WAIT_L0(); RAW_BAR(); } while (0)
#define SCHED() __builtin_amdgcn_sched_barrier(0)
template <int N> __device__ __forceinline__ void interleave_mfma_ds() {
#pragma unroll
  for (int i = 0; i < N; ++i) { __builtin_amdgcn_sched_group_barrier(0x8, 1, 0); __builtin_amdgcn_sched_group_barrier(0x100, 1, 0); }
}

#define STAMP(i)
#define CSTAMP(i)

__device__ __forceinline__ float bf_lo(unsigned x) { return __uint_as_float(x << 16); }
__device__ __forceinline__ float bf_hi(unsigned x) { return __uint_as_float(x & 0xffff0000u); }
__device__ __forceinline__ unsigned pk_bf16(float lo, float hi) {
  bf16x2_t v; v.x = (__bf16)lo; v.y = (__bf16)hi;
  return __builtin_bit_cast(unsigned, v);
}
__device__ __forceinline__ float rbf(float x) { return __uint_as_float(((unsigned)__builtin_bit_cast(u16, (__bf16)x)) << 16); }
__device__ __forceinline__ s16x4 tr_read(const LAS char* p) {
  return __builtin_bit_cast(s16x4, __builtin_amdgcn_ds_read_tr16_b64_v4i16((LAS v4i16_t*)p));
}
__device__ __forceinline__ bf16x8_t cat8(s16x4 a, s16x4 b) {
  bf16x8 r = __builtin_shufflevector(a, b, 0, 1, 2, 3, 4, 5, 6, 7);
  return __builtin_bit_cast(bf16x8_t, r);
}
__device__ __forceinline__ bf16x8_t tr_frag(const LAS char* p, int second) { return cat8(tr_read(p), tr_read(p + second)); }
__device__ __forceinline__ f32x4 mfma16(bf16x8_t a, bf16x8_t b, f32x4 c) {
  return __builtin_amdgcn_mfma_f32_16x16x32_bf16(a, b, c, 0, 0, 0);
}
template <int AUX>
__device__ __forceinline__ void dma16(const void* sbase, unsigned voff, unsigned lds) {
  if constexpr (AUX == AUX_SC1)
    asm volatile("s_mov_b32 m0, %2\n\ts_nop 0\n\tglobal_load_lds_dwordx4 %0, %1 sc1" ::"v"(voff), "s"(sbase), "s"(lds) : "memory");
  else
    asm volatile("s_mov_b32 m0, %2\n\ts_nop 0\n\tglobal_load_lds_dwordx4 %0, %1" ::"v"(voff), "s"(sbase), "s"(lds) : "memory");
}
__device__ __forceinline__ void dma4(const void* sbase, unsigned voff, unsigned lds) {
  asm volatile("s_mov_b32 m0, %2\n\ts_nop 0\n\tglobal_load_lds_dword %0, %1" ::"v"(voff), "s"(sbase), "s"(lds) : "memory");
}
__device__ __forceinline__ void dma4v(const void* vaddr, unsigned lds) {
  asm volatile("s_mov_b32 m0, %1\n\ts_nop 0\n\tglobal_load_lds_dword %0, off" ::"v"(vaddr), "s"(lds) : "memory");
}
__device__ __forceinline__ u32x4 ld128_asm(const void* sbase, unsigned voff) {
  u32x4 r; asm volatile("global_load_dwordx4 %0, %1, %2" : "=v"(r) : "v"(voff), "s"(sbase) : "memory"); return r;
}
__device__ __forceinline__ u32x4 ld128_sc1(const void* sbase, unsigned voff) {
  u32x4 r; asm volatile("global_load_dwordx4 %0, %1, %2 sc1" : "=v"(r) : "v"(voff), "s"(sbase) : "memory"); return r;
}
__device__ __forceinline__ u32x2 ld64_asm(const void* sbase, unsigned voff) {
  u32x2 r; asm volatile("global_load_dwordx2 %0, %1, %2" : "=v"(r) : "v"(voff), "s"(sbase) : "memory"); return r;
}
__device__ __forceinline__ u32x2 ld64_sc1(const void* sbase, unsigned voff) {
  u32x2 r; asm volatile("global_load_dwordx2 %0, %1, %2 sc1" : "=v"(r) : "v"(voff), "s"(sbase) : "memory"); return r;
}
template <int CTRL>
__device__ __forceinline__ float dpp_add(float x) {
  return x + __builtin_bit_cast(float, __builtin_amdgcn_mov_dpp(__builtin_bit_cast(int, x), CTRL, 0xf, 0xf, true));
}
__device__ __forceinline__ float wave_sum(float x) {
  x = dpp_add<0xB1>(x); x = dpp_add<0x4E>(x); x = dpp_add<0x141>(x); x = dpp_add<0x140>(x);
  auto s = __builtin_amdgcn_permlane16_swap(__float_as_uint(x), __float_as_uint(x), false, false);
  x = __uint_as_float(s[0]) + __uint_as_float(s[1]);
  auto u = __builtin_amdgcn_permlane32_swap(__float_as_uint(x), __float_as_uint(x), false, false);
  return __uint_as_float(u[0]) + __uint_as_float(u[1]);
}
__device__ __forceinline__ u32x4 widen16(u32x2 X, u32x2 Y) {
  auto a = __builtin_amdgcn_permlane16_swap(X[0], Y[0], false, false);
  auto b = __builtin_amdgcn_permlane16_swap(X[1], Y[1], false, false);
  return (u32x4){a[0], b[0], a[1], b[1]};
}

struct Params {
  const u16 *q, *k, *v, *shk, *ssmk, *ssmD, *w1, *w2;
  u16* out;
  char* ws;
};

__device__ __forceinline__ void wait_flags256(const char* ws, unsigned off, unsigned epoch, int lane) {
  gu32* f = (gu32*)(ws + off) + lane * FLAG_STRIDE;
#pragma unroll 1
  for (unsigned spins = 0; spins < SPIN_CAP; ++spins) {
    unsigned a = __hip_atomic_load(f, RLX_AGENT), b = __hip_atomic_load(f + 64 * FLAG_STRIDE, RLX_AGENT);
    unsigned c = __hip_atomic_load(f + 128 * FLAG_STRIDE, RLX_AGENT), d = __hip_atomic_load(f + 192 * FLAG_STRIDE, RLX_AGENT);
    if (__all((a == epoch) & (b == epoch) & (c == epoch) & (d == epoch))) break;
    __builtin_amdgcn_s_sleep(1);
  }
  __builtin_amdgcn_fence(__ATOMIC_ACQUIRE, "wavefront");
}

constexpr int L_KT = 32768, L_VT = L_KT + 72 * 128, L_QT = L_VT + 8192, L_OKV = L_QT + 8192, L_OQ = L_OKV + 8192, L_SS = L_OQ + 8192;
constexpr int H_ROW = 1152, H_COPY = 4728, H_BYTES = 9344, X_BYTES = 4160;
constexpr int L_H = 0, L_X = L_H + 2 * H_BYTES, L_QD = L_X + 2 * X_BYTES;
constexpr int CY_BYTES = 513 * 16;
constexpr int L_CY = L_QD + 16384;
constexpr int L_JUNKA = 76800;
constexpr int L_W1 = 77824;
constexpr int L_W1B = 0;
static_assert(L_CY + 4 * CY_BYTES <= L_JUNKA && L_JUNKA + 1024 <= L_W1 && L_W1 + 7 * 12288 <= 163840 && L_SS + 64 <= L_JUNKA, "lds");
static_assert(L_X + 2 * X_BYTES <= L_KT, "lds A/B overlap");
constexpr int L_W2A = 4 * 16384;
constexpr int L_CS = 147456;
constexpr int L_SST = L_CS + 1024;
constexpr int L_FST = L_SST + 2048;
constexpr int L_OUT1B = L_FST + 1024;
constexpr int L_JUNKC = L_OUT1B + 8192;
constexpr int L_EX1 = 0;
constexpr int L_OUT1A = 49152;
static_assert(L_JUNKC + 1024 <= 163840, "lds");
constexpr int L_RES = 155648;
constexpr int L_RING = 0;
constexpr int L_JUNKD = 147456;
constexpr int L_EX2 = 0;
constexpr int LDS_BYTES = 163840;

template <class F, int... I>
__device__ __forceinline__ void static_for_impl(F&& f, std::integer_sequence<int, I...>) { (f(std::integral_constant<int, I>{}), ...); }
template <int N, class F>
__device__ __forceinline__ void static_for(F&& f) { static_for_impl(f, std::make_integer_sequence<int, N>{}); }

template <int TS, int PAR, class F>
__device__ __forceinline__ void conv_main(const LAS char* xa, const LAS char* hb, f32x4 (&acc)[4][4], F&& job) {
  constexpr int U[4] = {TS == 0 ? 0 : 1, TS == 0 ? 3 : 2, TS == 0 ? 4 : 5, TS == 0 ? 7 : 6};
  constexpr int T[4] = {U[PAR ? 2 : 0], U[PAR ? 3 : 1], U[PAR ? 0 : 2], U[PAR ? 1 : 3]};
  constexpr int NA = U[3] + 1;
  bf16x8_t A[8], B[3][4];
#pragma unroll
  for (int p = 0; p < 4; ++p) B[0][p] = *(const LAS bf16x8_t*)(hb + p * H_ROW + 896);
#pragma unroll
  for (int a = NA - 1; a >= 0; --a) A[a] = tr_frag(xa + 512 * a, 32);
#pragma unroll
  for (int p = 0; p < 4; ++p) B[1][p] = *(const LAS bf16x8_t*)(hb + p * H_ROW + 896 - 128);
  SCHED();
  static_for<NA>([&](auto BB) {
    constexpr int bb = decltype(BB)::value;
    if (bb + 2 < NA) {
#pragma unroll
      for (int p = 0; p < 4; ++p) B[(bb + 2) % 3][p] = *(const LAS bf16x8_t*)(hb + p * H_ROW + 896 - 128 * (bb + 2));
    }
#pragma unroll
    for (int p = 0; p < 4; ++p)
#pragma unroll
      for (int j = 3; j >= 0; --j)
        if (T[j] >= bb) acc[j][p] = mfma16(A[T[j] - bb], B[bb % 3][p], acc[j][p]);
    if (bb + 2 < NA) interleave_mfma_ds<4>();
    SCHED();
    if constexpr (bb < 3) { job(std::integral_constant<int, 2 * bb>{}); job(std::integral_constant<int, 2 * bb + 1>{}); }
    else if constexpr (bb < 7) job(std::integral_constant<int, bb + 3>{});
    SCHED();
  });
}

__global__ __launch_bounds__(512, 2) void h3_fused(Params p) {
  extern __shared__ __attribute__((aligned(16))) char smem_[];
  LAS char* sm = (LAS char*)smem_;
  const unsigned lb = (unsigned)(size_t)sm;
  const int b = blockIdx.x, t = threadIdx.x, lane = t & 63;
  const int w = __builtin_amdgcn_readfirstlane(t >> 6);
  const int x = b & 7, jb = b >> 3;
  const int g = lane >> 4, fr = lane & 15, q_ = fr >> 2, pp = lane & 3;
  const int rl = 4 * g + q_;
  char* ws = p.ws;
  const __amdgpu_buffer_rsrc_t rs = __builtin_amdgcn_make_buffer_rsrc(ws, 0, WS_BYTES, 0x00020000);
  STAMP(0); CSTAMP(0);

  const int dB = x * 64 + jb * 2;
  {
    const unsigned vo = (lane >> 3) * (D_DIM * 2) + (lane & 7) * 16;
    const size_t so = ((size_t)(jb * 64 + w * 8) * D_DIM + x * 64);
    dma16<0>(p.k + so, vo, lb + L_KT + 1024 + w * 1024);
    dma16<0>(p.v + so, vo, lb + L_VT + w * 1024);
    dma16<0>(p.q + so, vo, lb + L_QT + w * 1024);
    if (w == 0) {
      if (jb > 0) dma16<0>(p.k + so - 8 * D_DIM, vo, lb + L_KT);
      else *(LAS u32x4*)(sm + L_KT + lane * 16) = (u32x4){0, 0, 0, 0};
    }
  }
  const int cb = w & 3, sl = 8 * (4 * (w >> 2) + g);
  u32x4 sq = ld128_asm(p.q + (size_t)(b * 8 + w) * D_DIM, lane * 16);
  u32x2 wv = ld64_asm(p.shk + (size_t)(x * 64 + cb * 16) * 4, fr * 8);
  if (w == 7) {
    unsigned long long pc; unsigned len;
    asm volatile("s_getpc_b64 %0\n.Lh3_ref_%=:\n\ts_mov_b32 %1, .Lh3_code_end-.Lh3_ref_%=" : "=s"(pc), "=s"(len));
    const unsigned nl = len >> 7;
    unsigned line = (unsigned)(jb * 8 + lane); line = line >= nl ? line - nl : line; line = line >= nl ? 0 : line;
    dma4((const void*)pc, line << 7, lb + L_JUNKA);
  }
  if (w == 6) {
    const char* a = ws + (size_t)(jb * 8 + (lane & 7)) * 65536;
    if (lane >= 8) a = (const char*)p.w1 + jb * 44032;
    if (lane >= 16) a = (const char*)p.w2 + jb * 44032;
    if (lane >= 24) a = (const char*)p.out + jb * 65536;
    if (lane >= 32) a = (const char*)p.ssmk + jb * 65536;
    dma4v(a, lb + L_JUNKA + 256);
  }
  const unsigned epoch = __hip_atomic_load((gu32*)(ws + WS_EPOCH), RLX_AGENT) + 1u;
  const float Dv = bf_lo(p.ssmD[dB + (w >> 2)]);
  const bool c_on = jb < 30;
  const int panel = x * 2 + (jb >= 15 ? 1 : 0), ct = jb >= 15 ? jb - 15 : jb;
  const int m0 = panel * 128, n0 = ct * 96;
  unsigned w1o[6];
#pragma unroll
  for (int u = 0; u < 6; ++u) {
    const int idx = ((w & 1) * 6 + u) * 64 + lane, r = (idx * 43691) >> 19, cp = idx - r * 12;
    int col = n0 + (cp ^ (2 * ((r >> 2) & 1))) * 8;
    col = col > F_DIM - 8 ? F_DIM - 8 : col;
    w1o[u] = (r * F_DIM + col) * 2;
  }
  auto w1_half = [&](int chunk, int ldst, int step) {
#pragma unroll
    for (int u = 0; u < 6; ++u) dma16<0>(p.w1 + (size_t)chunk * 64 * F_DIM, w1o[u], lb + ldst + ((w & 1) * 6 + u) * step);
  };
  WAIT_V(0);
  asm volatile("" : "+v"(sq), "+v"(wv));
  {
    f32x2 xs[4];
#pragma unroll
    for (int i = 0; i < 4; ++i) xs[i] = (f32x2){bf_lo(sq[i]), bf_hi(sq[i])};
    const f32x2 s2 = (xs[0] + xs[1]) + (xs[2] + xs[3]);
    const float mean = wave_sum(s2[0] + s2[1]) * (1.0f / D_DIM);
    f32x2 vs = {0.f, 0.f};
#pragma unroll
    for (int i = 0; i < 4; ++i) { const f32x2 dd = xs[i] - mean; vs += dd * dd; }
    const float var = rbf(wave_sum(vs[0] + vs[1]) * (1.0f / D_DIM));
    if (lane == 0) *(LAS unsigned*)(sm + L_SS + w * 4) = pk_bf16(mean, __builtin_amdgcn_sqrtf(rbf(var + 1e-5f)));
  }
  LBAR();
  STAMP(1);
  {
    const int tro = q_ * 128 + cb * 32 + 8 * pp + sl * 128;
    const u32x2 k0 = __builtin_bit_cast(u32x2, tr_read(sm + L_KT + 4 * 128 + tro));
    const u32x2 k1 = __builtin_bit_cast(u32x2, tr_read(sm + L_KT + 8 * 128 + tro));
    const u32x2 k2 = __builtin_bit_cast(u32x2, tr_read(sm + L_KT + 12 * 128 + tro));
    const u32x2 v0 = __builtin_bit_cast(u32x2, tr_read(sm + L_VT + tro));
    const u32x2 v1 = __builtin_bit_cast(u32x2, tr_read(sm + L_VT + 4 * 128 + tro));
    const u32x2 q0 = __builtin_bit_cast(u32x2, tr_read(sm + L_QT + tro));
    const u32x2 q1 = __builtin_bit_cast(u32x2, tr_read(sm + L_QT + 4 * 128 + tro));
    const float w0 = bf_lo(wv[0]), w1 = bf_hi(wv[0]), w2 = bf_lo(wv[1]), w3 = bf_hi(wv[1]);
    const float kx[11] = {bf_hi(k0[0]), bf_lo(k0[1]), bf_hi(k0[1]), bf_lo(k1[0]), bf_hi(k1[0]), bf_lo(k1[1]), bf_hi(k1[1]),
                          bf_lo(k2[0]), bf_hi(k2[0]), bf_lo(k2[1]), bf_hi(k2[1])};
    const float vx[8] = {bf_lo(v0[0]), bf_hi(v0[0]), bf_lo(v0[1]), bf_hi(v0[1]), bf_lo(v1[0]), bf_hi(v1[0]), bf_lo(v1[1]), bf_hi(v1[1])};
    float o[8];
#pragma unroll
    for (int e = 0; e < 8; ++e) o[e] = (kx[e] * w3 + kx[e + 1] * w2 + kx[e + 2] * w1 + kx[e + 3] * w0) * vx[e];
    const int c = cb * 16 + fr;
    const int oo = c * 128 + (((sl >> 3) ^ (c & 7)) << 4);
    *(LAS u32x4*)(sm + L_OKV + oo) = (u32x4){pk_bf16(o[0], o[1]), pk_bf16(o[2], o[3]), pk_bf16(o[4], o[5]), pk_bf16(o[6], o[7])};
    *(LAS u32x4*)(sm + L_OQ + oo) = (u32x4){q0[0], q0[1], q1[0], q1[1]};
  }
  LBAR();
  STAMP(15);
  {
    const int ch = t >> 3, c = t & 7;
    const int off = ch * 128 + ((c ^ (ch & 7)) << 4);
    const u32x4 a = *(const LAS u32x4*)(sm + L_OKV + off), bq = *(const LAS u32x4*)(sm + L_OQ + off);
    const unsigned go = ((x * 64 + ch) * S_LEN + jb * 64 + c * 8) * 2;
    __builtin_amdgcn_raw_buffer_store_b128(a, rs, WS_KVT + go, 0, AUX_SC1);
    __builtin_amdgcn_raw_buffer_store_b128(bq, rs, WS_QT + go, 0, AUX_SC1);
    if (t < 2) __builtin_amdgcn_raw_buffer_store_b128(*(const LAS u32x4*)(sm + L_SS + t * 16), rs, WS_STQ + b * 32 + t * 16, 0, AUX_SC1);
  }
  STAMP(31);
  WAIT_V(0);
  LBAR();
  if (t == 0) __hip_atomic_store((gu32*)(ws + WS_FL1) + b * FLAG_STRIDE, epoch, RLX_AGENT);
  RAW_BAR();
  STAMP(2);
  {
    const int hc = t >> 8, hw = t & 255;
    u32x4 hv = ld128_asm(p.ssmk + (size_t)dB * S_LEN, (hc * S_LEN + hw * 8) * 2);
    if (w < 4) { w1_half(w >> 1, L_W1 + (w >> 1) * 12288, 1024); WAIT_V(6); } else { WAIT_V(0); }
    asm volatile("" : "+v"(hv));
    LAS char* hb = sm + L_H + hc * H_BYTES + 1020 - 4 * hw;
    const unsigned h0 = __builtin_amdgcn_perm(hv[1], hv[3], 0x07060302u), h1 = __builtin_amdgcn_perm(hv[1], hv[3], 0x05040100u);
    const unsigned h2 = __builtin_amdgcn_perm(hv[0], hv[2], 0x07060302u), h3 = __builtin_amdgcn_perm(hv[0], hv[2], 0x05040100u);
    *(LAS unsigned*)(hb + 0 * H_ROW) = h0; *(LAS unsigned*)(hb + H_COPY + 0 * H_ROW) = h0;
    *(LAS unsigned*)(hb + 1 * H_ROW) = h1; *(LAS unsigned*)(hb + H_COPY + 1 * H_ROW) = h1;
    *(LAS unsigned*)(hb + 2 * H_ROW) = h2; *(LAS unsigned*)(hb + H_COPY + 2 * H_ROW) = h2;
    *(LAS unsigned*)(hb + 3 * H_ROW) = h3; *(LAS unsigned*)(hb + H_COPY + 3 * H_ROW) = h3;
    if (t < 256) {
      const int r = t >> 4;
      *(LAS u32x2*)(sm + L_H + (r >> 3) * H_BYTES + ((r >> 2) & 1) * H_COPY + (r & 3) * H_ROW + 1024 + (t & 15) * 8) = (u32x2){0, 0};
    } else if (t < 260) *(LAS u32x4*)(sm + L_X + ((t - 256) >> 1) * X_BYTES + (t & 1) * 16) = (u32x4){0, 0, 0, 0};
    else if (t < 264) *(LAS u32x4*)(sm + L_CY + (t - 260) * CY_BYTES) = (u32x4){0, 0, 0, 0};
  }

  const int c2 = w >> 2, ts = (w >> 1) & 1, par = w & 1;
  const int tl0 = ts == 0 ? 0 : 1, tl1 = ts == 0 ? 3 : 2, tl2 = ts == 0 ? 4 : 5, tl3 = ts == 0 ? 7 : 6;
  const int tf0 = par ? tl2 : tl0, tf1 = par ? tl3 : tl1, tg0 = par ? tl0 : tl2, tg1 = par ? tl1 : tl3;
  const int sa = tf0 * 256 + fr * 16 + g * 4, sb = tf1 * 256 + fr * 16 + g * 4;
  if (w == 0) wait_flags256(ws, WS_FL1, epoch, lane);
  LBAR();
  STAMP(3);
  {
    const int d = dB + c2;
    dma16<AUX_SC1>(ws + WS_KVT + ((size_t)d * S_LEN + (w & 3) * 512) * 2, lane * 16, lb + L_X + c2 * X_BYTES + 32 + (w & 3) * 1024);
    u32x2 qv0 = ld64_sc1(ws + WS_QT + (size_t)d * S_LEN * 2, sa * 2), qv1 = ld64_sc1(ws + WS_QT + (size_t)d * S_LEN * 2, sb * 2);
    u32x4 sv0 = ld128_sc1(ws + WS_STQ, sa * 4), sv1 = ld128_sc1(ws + WS_STQ, sb * 4);
    WAIT_V(4);
    RAW_BAR();
    STAMP(4); CSTAMP(1);
    f32x4 acc[4][4];
#pragma unroll
    for (int j = 0; j < 4; ++j)
#pragma unroll
      for (int pz = 0; pz < 4; ++pz) acc[j][pz] = (f32x4){0, 0, 0, 0};
    const LAS char* xa = sm + L_X + c2 * X_BYTES + 8 + 64 * g + 8 * q_ + 8 * pp + 256 * par;
    const LAS char* hb = sm + L_H + c2 * H_BYTES + ((fr & 1) ? 0 : H_COPY) + 1016 - 8 * fr + 16 * g + 64 * par - 896;
    auto job = [&](auto J) {
      constexpr int j = decltype(J)::value;
      if constexpr (j < 6) {
        const int ch = 2 + (w >> 1);
        dma16<0>(p.w1 + (size_t)ch * 64 * F_DIM, w1o[j], lb + L_W1 + ch * 12288 + ((w & 1) * 6 + j) * 1024);
      } else if constexpr (j < 9) {
        if (w < 4) {
          const int u = (w >> 1) * 3 + (j - 6);
          dma16<0>(p.w1 + (size_t)6 * 64 * F_DIM, (w & 2) ? w1o[3 + j - 6] : w1o[j - 6], lb + L_W1 + 6 * 12288 + ((w & 1) * 6 + u) * 1024);
        }
      } else {
        int r2 = ((b * 43) >> 3) + w; r2 = r2 > F_DIM - 1 ? F_DIM - 1 : r2;
        dma16<0>(p.w2 + (size_t)r2 * D_DIM, lane * 16, lb + L_JUNKA);
      }
    };
    if (ts == 0) { if (par == 0) conv_main<0, 0>(xa, hb, acc, job); else conv_main<0, 1>(xa, hb, acc, job); }
    else         { if (par == 0) conv_main<1, 0>(xa, hb, acc, job); else conv_main<1, 1>(xa, hb, acc, job); }
    STAMP(5); CSTAMP(2);
#pragma unroll
    for (int j = 0; j < 4; ++j) {
      acc[j][3][1] += acc[j][2][0];
      acc[j][3][2] += acc[j][2][1] + acc[j][1][0];
      acc[j][3][3] += (acc[j][2][2] + acc[j][1][1]) + acc[j][0][0];
      acc[j][0][1] += acc[j][2][3] + acc[j][1][2];
      acc[j][0][2] += acc[j][1][3];
    }
    LAS char* cy = sm + L_CY + (c2 * 2 + par) * CY_BYTES + 16 + lane * 16;
    *(LAS f32x4*)(cy + tf0 * 1024) = acc[0][0];
    *(LAS f32x4*)(cy + tf1 * 1024) = acc[1][0];
    *(LAS f32x4*)(cy + tg0 * 1024) = acc[2][0];
    *(LAS f32x4*)(cy + tg1 * 1024) = acc[3][0];
    LAS char* qd = sm + L_QD + w * 2048 + lane * 16;
    *(LAS f32x4*)(qd) = acc[2][3]; *(LAS f32x4*)(qd + 1024) = acc[3][3];
    const u32x2 xv0 = *(const LAS u32x2*)(sm + L_X + c2 * X_BYTES + 32 + sa * 2), xv1 = *(const LAS u32x2*)(sm + L_X + c2 * X_BYTES + 32 + sb * 2);
    LBAR();
    STAMP(29);
    const int prev = g > 0 ? lane - 16 : (fr > 0 ? 47 + fr : -1);
    const LAS char* cy0 = sm + L_CY + (c2 * 2) * CY_BYTES + 16 + prev * 16;
    const LAS char* qs = sm + L_QD + (w ^ 1) * 2048 + lane * 16;
    WAIT_V(7);
    asm volatile("" : "+v"(qv0), "+v"(qv1), "+v"(sv0), "+v"(sv1));
    u32x2 ov[2];
#pragma unroll
    for (int i = 0; i < 2; ++i) {
      const int tf = i ? tf1 : tf0;
      const f32x4 pq = *(const LAS f32x4*)(qs + i * 1024);
      const f32x4 ca = *(const LAS f32x4*)(cy0 + tf * 1024), cb_ = *(const LAS f32x4*)(cy0 + CY_BYTES + tf * 1024);
      f32x4 y = acc[i][3] + pq;
      y[0] += ca[1] + cb_[1]; y[1] += ca[2] + cb_[2]; y[2] += ca[3] + cb_[3];
      const u32x2 xv = i ? xv1 : xv0, qv = i ? qv1 : qv0;
      const u32x4 sv = i ? sv1 : sv0;
      const float xx[4] = {bf_lo(xv[0]), bf_hi(xv[0]), bf_lo(xv[1]), bf_hi(xv[1])};
      const float qx[4] = {bf_lo(qv[0]), bf_hi(qv[0]), bf_lo(qv[1]), bf_hi(qv[1])};
      float o[4];
#pragma unroll
      for (int e = 0; e < 4; ++e) o[e] = qx[e] + ((qx[e] - bf_lo(sv[e])) * __builtin_amdgcn_rcpf(bf_hi(sv[e]))) * (y[e] + xx[e] * Dv);
      ov[i] = (u32x2){pk_bf16(o[0], o[1]), pk_bf16(o[2], o[3])};
    }
    const u32x4 o16 = widen16(ov[0], ov[1]);
    const int so = ((g & 1) ? tf1 : tf0) * 256 + fr * 16 + (g >> 1) * 8;
    __builtin_amdgcn_raw_buffer_store_b128(o16, rs, WS_HT + (d * S_LEN + so) * 2, 0, AUX_SC1);
    STAMP(30);
  }
  WAIT_V(0);
  LBAR();
  if (t == 0) __hip_atomic_store((gu32*)(ws + WS_FL2) + b * FLAG_STRIDE, epoch, RLX_AGENT);
  RAW_BAR();
  STAMP(6);

  const int kp = w >> 2, wm = (w >> 1) & 1, wn = w & 1;
  const int mt = x * 4 + (jb >> 3), dt = jb & 7;
  const int md0 = mt * 64, d0 = dt * 64;
  const int kq = w >> 1, dh = w & 1;
  unsigned wvo[2], hvo[2];
#pragma unroll
  for (int u = 0; u < 2; ++u) {
    const int r = ((w & 1) * 2 + u) * 8 + (lane >> 3), c = ((lane & 7) ^ (2 * ((r >> 1) & 3))) * 16;
    wvo[u] = r * (D_DIM * 2) + c; hvo[u] = r * (S_LEN * 2) + c;
  }
  auto load_w2 = [&](int step, int lds) {
    int ks = kq * 11 + step; ks = ks > 42 ? 42 : ks;
#pragma unroll
    for (int u = 0; u < 2; ++u) dma16<0>(p.w2 + (size_t)ks * 32 * D_DIM + d0, wvo[u], lb + lds + kq * 4096 + ((w & 1) * 2 + u) * 1024);
  };
  const bf16x8_t ONES = __builtin_bit_cast(bf16x8_t, (u32x4){0x3f803f80u, 0x3f803f80u, 0x3f803f80u, 0x3f803f80u});
  if (c_on) {
    bf16x8_t W1[8][3];
    f32x4 csa[3];
    if (w < 2) w1_half(7, L_W1B, 1024);
#pragma unroll
    for (int i = 0; i < 3; ++i) csa[i] = (f32x4){0, 0, 0, 0};
    int wo[3];
#pragma unroll
    for (int i = 0; i < 3; ++i) wo[i] = (kp * 32 + rl) * 192 + 8 * pp + (((wn * 3 + i) ^ ((rl >> 2) & 1)) << 5);
#pragma unroll
    for (int c = 0; c < 7; ++c)
#pragma unroll
      for (int i = 0; i < 3; ++i) {
        W1[c][i] = tr_frag(sm + L_W1 + c * 12288 + wo[i], 16 * 192);
        csa[i] = mfma16(ONES, W1[c][i], csa[i]);
      }
    WAIT_V(0);
    RAW_BAR();
    STAMP(28);
#pragma unroll
    for (int i = 0; i < 3; ++i) {
      W1[7][i] = tr_frag(sm + L_W1B + wo[i], 16 * 192);
      csa[i] = mfma16(ONES, W1[7][i], csa[i]);
    }
    LBAR();
    load_w2(0, L_W2A); load_w2(1, L_W2A + 16384); load_w2(2, L_W2A + 2 * 16384);
    STAMP(7);
    if (w == 0) wait_flags256(ws, WS_FL2, epoch, lane);
    LBAR();
    STAMP(8);
    if (b == 0 && t == 0) __hip_atomic_store((gu32*)(ws + WS_EPOCH), epoch, RLX_AGENT);
    const char* hsrc = ws + WS_HT + ((size_t)(w * 8) * S_LEN + m0) * 2;
    const int ra = lane >> 4, rb = ra + 4;
    const unsigned va = ra * (S_LEN * 2) + (((lane & 15) ^ (2 * ra)) << 4), vb = rb * (S_LEN * 2) + (((lane & 15) ^ (2 * rb)) << 4);
    auto load_ht = [&](int c) {
      dma16<AUX_SC1>(hsrc + (size_t)c * (64 * S_LEN * 2), va, lb + (c & 3) * 16384 + (w * 2) * 1024);
      dma16<AUX_SC1>(hsrc + (size_t)c * (64 * S_LEN * 2), vb, lb + (c & 3) * 16384 + (w * 2 + 1) * 1024);
    };
    load_ht(0); load_ht(1);
    if (wm == 0 && g == 0) {
#pragma unroll
      for (int i = 0; i < 3; ++i) *(LAS float*)(sm + L_CS + (kp * 96 + wn * 48 + i * 16 + fr) * 4) = csa[i][0];
    }
    f32x4 acc[4][3], sta[4];
    int aoff[4];
#pragma unroll
    for (int i = 0; i < 4; ++i) {
      sta[i] = (f32x4){0, 0, 0, 0};
      aoff[i] = (kp * 32 + rl) * 256 + (((wm * 4 + (i ^ (2 * kp))) ^ (rl & 7)) << 5) + 8 * pp;
#pragma unroll
      for (int jn = 0; jn < 3; ++jn) acc[i][jn] = (f32x4){0, 0, 0, 0};
    }
    auto g1_loop = [&](auto WN) {
      bf16x8_t A[2][4];
      auto half = [&](bf16x8_t (&Af)[4], const bf16x8_t (&Wf)[3], int i0) {
#pragma unroll
        for (int i = i0; i < i0 + 2; ++i) {
#pragma unroll
          for (int jn = 0; jn < 3; ++jn) acc[i][jn] = mfma16(Af[i], Wf[jn], acc[i][jn]);
          sta[i] = mfma16(Af[i], decltype(WN)::value ? Af[i] : ONES, sta[i]);
        }
      };
      auto warm_w2 = [&](int i) {
        int ks = kq * 11 + 5 + 2 * i + (w & 1); ks = ks > 42 ? 42 : ks;
        dma16<0>(p.w2 + (size_t)(ks * 32 + (jb >> 3) * 8) * D_DIM + d0, (lane >> 3) * (D_DIM * 2) + (lane & 7) * 16, lb + L_JUNKC);
      };
#define G1_ITER(c, N)                                                                                        \
      WAIT_V(N); RAW_BAR();                                                                                  \
      if ((c) == 0) STAMP(16); if ((c) == 1) { STAMP(17); CSTAMP(3); } if ((c) == 4) STAMP(18); if ((c) == 7) STAMP(19); \
      _Pragma("unroll") for (int i = 0; i < 4; ++i) A[(c) & 1][i] = tr_frag(sm + aoff[i] + ((c) & 3) * 16384, 4096); \
      if ((c) > 0) { half(A[((c) - 1) & 1], W1[(c) > 0 ? (c) - 1 : 0], 0); interleave_mfma_ds<8>(); }        \
      SCHED();                                                                                               \
      if ((c) + 2 < 8) load_ht((c) + 2);                                                                     \
      if ((c) == 1) load_w2(3, L_W2A + 3 * 16384);                                                           \
      if ((c) == 2) load_w2(4, L_W2A + 4 * 16384);                                                           \
      if ((c) >= 4 && (c) <= 6) warm_w2((c) - 4);                                                            \
      SCHED();                                                                                               \
      if ((c) > 0) half(A[((c) - 1) & 1], W1[(c) > 0 ? (c) - 1 : 0], 2);                                     \
      SCHED();
      G1_ITER(0, 2) G1_ITER(1, 2) G1_ITER(2, 4) G1_ITER(3, 6) G1_ITER(4, 4) G1_ITER(5, 3) G1_ITER(6, 4) G1_ITER(7, 2)
#undef G1_ITER
      half(A[1], W1[7], 0); half(A[1], W1[7], 2);
    };
    if (wn) g1_loop(std::integral_constant<bool, true>{}); else g1_loop(std::integral_constant<bool, false>{});
    STAMP(9); CSTAMP(4);
    LBAR();
    {
      LAS char* sp = sm + L_SST + ((kp * 2 + wn) * 128 + wm * 64) * 4 + kp * 128, *sq_ = sp - kp * 256;
      if (wn == 0) {
        if (fr == 0) {
          *(LAS f32x4*)(sp + g * 16) = sta[0]; *(LAS f32x4*)(sp + 64 + g * 16) = sta[1];
          *(LAS f32x4*)(sq_ + 128 + g * 16) = sta[2]; *(LAS f32x4*)(sq_ + 192 + g * 16) = sta[3];
        }
      } else if (q_ == g) {
        float dg[4];
#pragma unroll
        for (int i = 0; i < 4; ++i) dg[i] = pp == 0 ? sta[i][0] : (pp == 1 ? sta[i][1] : (pp == 2 ? sta[i][2] : sta[i][3]));
        *(LAS float*)(sp + fr * 4) = dg[0]; *(LAS float*)(sp + 64 + fr * 4) = dg[1];
        *(LAS float*)(sq_ + 128 + fr * 4) = dg[2]; *(LAS float*)(sq_ + 192 + fr * 4) = dg[3];
      }
    }
#pragma unroll
    for (int i = 0; i < 2; ++i)
#pragma unroll
      for (int jn = 0; jn < 3; ++jn) *(LAS f32x4*)(sm + L_EX1 + w * 6144 + (i * 3 + jn) * 1024 + lane * 16) = acc[2 + i][jn];
    LBAR();
    STAMP(20);
    if (t < 128) {
      const LAS float* sp = (const LAS float*)(sm + L_SST) + t;
      const float mean = (sp[0] + sp[256]) * (1.0f / D_DIM);
      const float var = fmaxf((sp[128] + sp[384]) * (1.0f / D_DIM) - mean * mean, 0.f);
      *(LAS float*)(sm + L_FST + t * 4) = -rbf(mean);
      *(LAS float*)(sm + L_FST + 512 + t * 4) = -1.44269504f * __builtin_amdgcn_rcpf(rbf(__builtin_amdgcn_sqrtf(rbf(rbf(var) + 1e-5f))));
    }
    f32x2 csv[3];
#pragma unroll
    for (int jn = 0; jn < 3; ++jn) {
      const int nl = wn * 48 + jn * 16 + fr;
      const float c = *(const LAS float*)(sm + L_CS + nl * 4) + *(const LAS float*)(sm + L_CS + (96 + nl) * 4);
      csv[jn] = (f32x2){c, c};
    }
#pragma unroll
    for (int i = 0; i < 2; ++i)
#pragma unroll
      for (int jn = 0; jn < 3; ++jn) acc[i][jn] += *(const LAS f32x4*)(sm + L_EX1 + (w ^ 4) * 6144 + (i * 3 + jn) * 1024 + lane * 16);
    LBAR();
    u32x2 ov[2][3];
#pragma unroll
    for (int i = 0; i < 2; ++i) {
      const int ml = wm * 64 + (kp * 2 + i) * 16 + g * 4;
      const f32x4 nmu = *(const LAS f32x4*)(sm + L_FST + ml * 4), zz = *(const LAS f32x4*)(sm + L_FST + 512 + ml * 4);
      const f32x2 nm0 = {nmu[0], nmu[1]}, nm1 = {nmu[2], nmu[3]}, z0 = {zz[0], zz[1]}, z1 = {zz[2], zz[3]};
      const f32x2 cL = {-1.44269504f, -1.44269504f};
#pragma unroll
      for (int jn = 0; jn < 3; ++jn) {
        const f32x4 a = acc[i][jn];
        const f32x2 e0 = (nm0 * csv[jn] + (f32x2){a[0], a[1]}) * z0, e1 = (nm1 * csv[jn] + (f32x2){a[2], a[3]}) * z1;
        const f32x2 d0_ = (f32x2){__builtin_amdgcn_exp2f(e0[0]), __builtin_amdgcn_exp2f(e0[1])} * cL + cL;
        const f32x2 d1_ = (f32x2){__builtin_amdgcn_exp2f(e1[0]), __builtin_amdgcn_exp2f(e1[1])} * cL + cL;
        const f32x2 o0 = e0 * (f32x2){__builtin_amdgcn_rcpf(d0_[0]), __builtin_amdgcn_rcpf(d0_[1])};
        const f32x2 o1 = e1 * (f32x2){__builtin_amdgcn_rcpf(d1_[0]), __builtin_amdgcn_rcpf(d1_[1])};
        ov[i][jn] = (u32x2){pk_bf16(o0[0], o0[1]), pk_bf16(o1[0], o1[1])};
      }
    }
#pragma unroll
    for (int jn = 0; jn < 3; ++jn) {
      const u32x4 o16 = widen16(ov[0][jn], ov[1][jn]);
      const int nl = wn * 48 + jn * 16 + fr;
      const int ch = wm * 8 + (kp * 2 + (g & 1)) * 2 + (g >> 1);
      const int base = nl < 64 ? L_OUT1A + nl * 256 : L_OUT1B + (nl - 64) * 256;
      *(LAS u32x4*)(sm + base + ((ch ^ (nl & 7)) << 4)) = o16;
    }
    LBAR();
    STAMP(21);
#pragma unroll
    for (int i = 0; i < 3; ++i) {
      const int nl = i * 32 + (t >> 4), ch = t & 15;
      const int base = i < 2 ? L_OUT1A + nl * 256 : L_OUT1B + (nl - 64) * 256;
      const u32x4 o16 = *(const LAS u32x4*)(sm + base + ((ch ^ (nl & 7)) << 4));
      if (n0 + nl < F_DIM) __builtin_amdgcn_raw_buffer_store_b128(o16, rs, WS_H1T + ((n0 + nl) * S_LEN + m0 + ch * 8) * 2, 0, AUX_SC1);
    }
  } else {
    if (w == 0) wait_flags256(ws, WS_FL2, epoch, lane);
    RAW_BAR();
    load_w2(0, L_W2A); load_w2(1, L_W2A + 16384); load_w2(2, L_W2A + 2 * 16384); load_w2(3, L_W2A + 3 * 16384); load_w2(4, L_W2A + 4 * 16384);
  }
  WAIT_V(0);
  STAMP(22);
  LBAR();
  if (c_on && t == 0) __hip_atomic_store((gu32*)(ws + WS_FL3) + b * FLAG_STRIDE, epoch, RLX_AGENT);
  RAW_BAR();
  STAMP(10);
  if (c_on) {
#pragma unroll
    for (int i = 0; i < 3; ++i) {
      int nn = n0 + i * 32 + w * 4; nn = nn > F_DIM - 4 ? F_DIM - 4 : nn;
      dma16<AUX_SC1>(ws + WS_H1T + ((size_t)nn * S_LEN + m0) * 2, (lane >> 4) * (S_LEN * 2) + (lane & 15) * 16, lb + L_JUNKD + w * 1024);
    }
  }

  {
    load_w2(5, 0); load_w2(6, 16384); load_w2(7, 2 * 16384); load_w2(8, 3 * 16384);
    const int f3 = (rl >> 1) & 3;
    bf16x8_t W2[11][2];
    int woff[2], boff[4];
#pragma unroll
    for (int i = 0; i < 2; ++i) woff[i] = kq * 4096 + rl * 128 + (((dh * 2 + i) ^ f3) << 5) + 8 * pp;
#pragma unroll
    for (int i = 0; i < 4; ++i) boff[i] = L_RING + kq * 4096 + rl * 128 + ((i ^ f3) << 5) + 8 * pp;
#pragma unroll
    for (int s = 0; s < 5; ++s)
#pragma unroll
      for (int i = 0; i < 2; ++i) W2[s][i] = tr_frag(sm + woff[i] + L_W2A + s * 16384, 2048);
    LBAR();
    load_w2(9, 4 * 16384); load_w2(10, 5 * 16384);
    {
      const int r = lane >> 3;
      dma16<AUX_SC1>(ws + WS_HT + ((size_t)(d0 + w * 8) * S_LEN + md0) * 2, r * (S_LEN * 2) + (((lane & 7) ^ (2 * ((r >> 1) & 3))) << 4), lb + L_RES + w * 1024);
    }
    WAIT_V(5);
    RAW_BAR();
    STAMP(23);
#pragma unroll
    for (int s = 5; s < 9; ++s)
#pragma unroll
      for (int i = 0; i < 2; ++i) W2[s][i] = tr_frag(sm + woff[i] + (s - 5) * 16384, 2048);
    WAIT_V(0);
    RAW_BAR();
#pragma unroll
    for (int s = 9; s < 11; ++s)
#pragma unroll
      for (int i = 0; i < 2; ++i) W2[s][i] = tr_frag(sm + woff[i] + (s - 5) * 16384, 2048);
    STAMP(11);
    if (w == 0) {
      const int h = (mt >> 1) & 1;
      gu32* f = (gu32*)(ws + WS_FL3) + (x + 8 * (15 * h + (lane < 15 ? lane : 0))) * FLAG_STRIDE;
#pragma unroll 1
      for (unsigned spins = 0; spins < SPIN_CAP; ++spins) {
        unsigned a = __hip_atomic_load(f, RLX_AGENT);
        if (__all(a == epoch)) break;
        __builtin_amdgcn_s_sleep(1);
      }
      __builtin_amdgcn_fence(__ATOMIC_ACQUIRE, "wavefront");
    }
    LBAR();
    STAMP(12);
    auto load_h1 = [&](int step) {
      int ks = kq * 11 + step; ks = ks > 42 ? 42 : ks;
#pragma unroll
      for (int u = 0; u < 2; ++u)
        dma16<AUX_SC1>(ws + WS_H1T + ((size_t)ks * 32 * S_LEN + md0) * 2, hvo[u], lb + L_RING + (step % 6) * 16384 + kq * 4096 + ((w & 1) * 2 + u) * 1024);
    };
    load_h1(0); load_h1(1); load_h1(2);
    f32x4 acc[2][4];
#pragma unroll
    for (int i = 0; i < 2; ++i)
#pragma unroll
      for (int jm = 0; jm < 4; ++jm) acc[i][jm] = (f32x4){0, 0, 0, 0};
    bf16x8_t B[2][4];
    auto half2 = [&](const bf16x8_t (&Wf)[2], const bf16x8_t (&Bf)[4], int j0) {
#pragma unroll
      for (int jm = j0; jm < j0 + 2; ++jm)
#pragma unroll
        for (int i = 0; i < 2; ++i) acc[i][jm] = mfma16(Wf[i], Bf[jm], acc[i][jm]);
    };
#define G2_ITER(r, N)                                                                                                   \
    WAIT_V(N); LBAR();                                                                                                  \
    if ((r) == 0) STAMP(24); if ((r) == 1) { STAMP(25); CSTAMP(5); } if ((r) == 5) STAMP(26); if ((r) == 10) STAMP(27); \
    _Pragma("unroll") for (int i = 0; i < 4; ++i) B[(r) & 1][i] = tr_frag(sm + boff[i] + ((r) % 6) * 16384, 2048);      \
    if ((r) > 0) { half2(W2[(r) > 0 ? (r) - 1 : 0], B[((r) - 1) & 1], 0);                                               \
      _Pragma("unroll") for (int i = 0; i < 4; ++i) { __builtin_amdgcn_sched_group_barrier(0x8, 1, 0); __builtin_amdgcn_sched_group_barrier(0x100, 2, 0); } } \
    SCHED();                                                                                                            \
    if ((r) + 3 <= 10) load_h1((r) + 3);                                                                                \
    SCHED();                                                                                                            \
    if ((r) > 0) half2(W2[(r) > 0 ? (r) - 1 : 0], B[((r) - 1) & 1], 2);                                                 \
    SCHED();
    G2_ITER(0, 4) G2_ITER(1, 4) G2_ITER(2, 4) G2_ITER(3, 4) G2_ITER(4, 4) G2_ITER(5, 4)
    G2_ITER(6, 4) G2_ITER(7, 4) G2_ITER(8, 4) G2_ITER(9, 2) G2_ITER(10, 0)
#undef G2_ITER
    if (kq < 3) { half2(W2[10], B[0], 0); half2(W2[10], B[0], 2); }
    STAMP(13); CSTAMP(6);
    u32x2 ru[2];
#pragma unroll
    for (int i = 0; i < 2; ++i) {
      const int r = (dh * 2 + i) * 16 + rl;
      ru[i] = __builtin_bit_cast(u32x2, tr_read(sm + L_RES + r * 128 + ((kq ^ f3) << 5) + 8 * pp));
    }
    LBAR();
#pragma unroll
    for (int i = 0; i < 2; ++i)
#pragma unroll
      for (int jm = 0; jm < 4; ++jm) *(LAS f32x4*)(sm + L_EX2 + ((dh * 4 + jm) * 8 + kq * 2 + i) * 1024 + lane * 16) = acc[i][jm];
    LBAR();
    u32x2 ov[2];
#pragma unroll
    for (int i = 0; i < 2; ++i) {
      const LAS char* e = sm + L_EX2 + ((dh * 4 + kq) * 8 + i) * 1024 + lane * 16;
      const f32x4 a = (*(const LAS f32x4*)(e) + *(const LAS f32x4*)(e + 2048)) + (*(const LAS f32x4*)(e + 4096) + *(const LAS f32x4*)(e + 6144));
      ov[i] = (u32x2){pk_bf16(bf_lo(ru[i][0]) + rbf(a[0]), bf_hi(ru[i][0]) + rbf(a[1])), pk_bf16(bf_lo(ru[i][1]) + rbf(a[2]), bf_hi(ru[i][1]) + rbf(a[3]))};
    }
    const u32x4 o16 = widen16(ov[0], ov[1]);
    *(u32x4*)(p.out + (size_t)(md0 + kq * 16 + fr) * D_DIM + d0 + (dh * 2 + (g & 1)) * 16 + (g >> 1) * 8) = o16;
  }
  asm volatile(".Lh3_code_end:");
  STAMP(14); CSTAMP(7);
}

extern "C" void kernel_launch(void* const* d_in, const int* in_sizes, int n_in, void* d_out, int out_size, void* d_ws, size_t ws_size,
                              hipStream_t stream) {
  Params p;
  p.q = (const u16*)d_in[0];
  p.k = (const u16*)d_in[1];
  p.v = (const u16*)d_in[2];
  p.shk = (const u16*)d_in[3];
  p.ssmk = (const u16*)d_in[4];
  p.ssmD = (const u16*)d_in[5];
  p.w1 = (const u16*)d_in[7];
  p.w2 = (const u16*)d_in[8];
  p.out = (u16*)d_out;
  p.ws = (char*)d_ws;
  (void)hipFuncSetAttribute((const void*)h3_fused, hipFuncAttributeMaxDynamicSharedMemorySize, LDS_BYTES);
  h3_fused<<<256, 512, LDS_BYTES, stream>>>(p);
}
```

```cpp
#include <hip/hip_runtime.h>
#include <hip/hip_bf16.h>
#include <stdint.h>
#include <type_traits>
#include <utility>

typedef unsigned short u16;
typedef short bf16x8 __attribute__((ext_vector_type(8)));
typedef short s16x4 __attribute__((ext_vector_type(4)));
typedef float f32x4 __attribute__((ext_vector_type(4)));
typedef float f32x2 __attribute__((ext_vector_type(2)));
typedef unsigned u32x4 __attribute__((ext_vector_type(4)));
typedef unsigned u32x2 __attribute__((ext_vector_type(2)));
typedef __bf16 bf16x2_t __attribute__((ext_vector_type(2)));
typedef __bf16 bf16x8_t __attribute__((ext_vector_type(8)));
typedef short v4i16_t __attribute__((ext_vector_type(4)));
#define LAS __attribute__((address_space(3)))
#define GAS __attribute__((address_space(1)))
typedef GAS unsigned gu32;
#define RLX_AGENT __ATOMIC_RELAXED, __HIP_MEMORY_SCOPE_AGENT

constexpr int S_LEN = 2048, D_DIM = 512, F_DIM = 1376;
constexpr int AUX_SC1 = 16;

constexpr unsigned WS_KVT = 0, WS_QT = 2u << 20, WS_HT = 4u << 20, WS_H1T = 6u << 20, WS_STQ = 12u << 20;
constexpr unsigned WS_FL1 = WS_STQ + 65536, WS_FL2 = WS_FL1 + 32768, WS_FL3 = WS_FL2 + 32768, WS_EPOCH = WS_FL3 + 32768;
constexpr unsigned WS_STAMP = WS_EPOCH + 4096;
constexpr unsigned WS_BYTES = 16u << 20;
constexpr int FLAG_STRIDE = 16;
constexpr unsigned SPIN_CAP = 1u << 18;

#define WAIT_V(n) asm volatile("s_waitcnt vmcnt(%0)" ::"n"(n) : "memory")
#define WAIT_L0() asm volatile("s_waitcnt lgkmcnt(0)" ::: "memory")
#define RAW_BAR() do { asm volatile("" ::: "memory"); __builtin_amdgcn_s_barrier(); asm volatile("" ::: "memory"); } while (0)
#define LBAR() do { WAIT_L0(); RAW_BAR(); } while (0)
#define SCHED() __builtin_amdgcn_sched_barrier(0)
template <int N> __device__ __forceinline__ void interleave_mfma_ds() {
#pragma unroll
  for (int i = 0; i < N; ++i) { __builtin_amdgcn_sched_group_barrier(0x8, 1, 0); __builtin_amdgcn_sched_group_barrier(0x100, 1, 0); }
}

#define STAMP(i)
#define CSTAMP(i)

__device__ __forceinline__ float bf_lo(unsigned x) { return __uint_as_float(x << 16); }
__device__ __forceinline__ float bf_hi(unsigned x) { return __uint_as_float(x & 0xffff0000u); }
__device__ __forceinline__ unsigned pk_bf16(float lo, float hi) {
  bf16x2_t v; v.x = (__bf16)lo; v.y = (__bf16)hi;
  return __builtin_bit_cast(unsigned, v);
}
__device__ __forceinline__ float rbf(float x) { return __uint_as_float(((unsigned)__builtin_bit_cast(u16, (__bf16)x)) << 16); }
__device__ __forceinline__ s16x4 tr_read(const LAS char* p) {
  return __builtin_bit_cast(s16x4, __builtin_amdgcn_ds_read_tr16_b64_v4i16((LAS v4i16_t*)p));
}
__device__ __forceinline__ bf16x8_t cat8(s16x4 a, s16x4 b) {
  bf16x8 r = __builtin_shufflevector(a, b, 0, 1, 2, 3, 4, 5, 6, 7);
  return __builtin_bit_cast(bf16x8_t, r);
}
__device__ __forceinline__ bf16x8_t tr_frag(const LAS char* p, int second) { return cat8(tr_read(p), tr_read(p + second)); }
__device__ __forceinline__ f32x4 mfma16(bf16x8_t a, bf16x8_t b, f32x4 c) {
  return __builtin_amdgcn_mfma_f32_16x16x32_bf16(a, b, c, 0, 0, 0);
}
template <int AUX>
__device__ __forceinline__ void dma16(const void* sbase, unsigned voff, unsigned lds) {
  if constexpr (AUX == AUX_SC1)
    asm volatile("s_mov_b32 m0, %2\n\ts_nop 0\n\tglobal_load_lds_dwordx4 %0, %1 sc1" ::"v"(voff), "s"(sbase), "s"(lds) : "memory");
  else
    asm volatile("s_mov_b32 m0, %2\n\ts_nop 0\n\tglobal_load_lds_dwordx4 %0, %1" ::"v"(voff), "s"(sbase), "s"(lds) : "memory");
}
__device__ __forceinline__ void dma4(const void* sbase, unsigned voff, unsigned lds) {
  asm volatile("s_mov_b32 m0, %2\n\ts_nop 0\n\tglobal_load_lds_dword %0, %1" ::"v"(voff), "s"(sbase), "s"(lds) : "memory");
}
__device__ __forceinline__ void dma4v(const void* vaddr, unsigned lds) {
  asm volatile("s_mov_b32 m0, %1\n\ts_nop 0\n\tglobal_load_lds_dword %0, off" ::"v"(vaddr), "s"(lds) : "memory");
}
__device__ __forceinline__ u32x4 ld128_asm(const void* sbase, unsigned voff) {
  u32x4 r; asm volatile("global_load_dwordx4 %0, %1, %2" : "=v"(r) : "v"(voff), "s"(sbase) : "memory"); return r;
}
__device__ __forceinline__ u32x4 ld128_sc1(const void* sbase, unsigned voff) {
  u32x4 r; asm volatile("global_load_dwordx4 %0, %1, %2 sc1" : "=v"(r) : "v"(voff), "s"(sbase) : "memory"); return r;
}
__device__ __forceinline__ unsigned ld32_sc1(const void* sbase, unsigned voff) {
  unsigned r; asm volatile("global_load_dword %0, %1, %2 sc1" : "=v"(r) : "v"(voff), "s"(sbase) : "memory"); return r;
}
__device__ __forceinline__ unsigned ld16_asm(const void* sbase, unsigned voff) {
  unsigned r; asm volatile("global_load_ushort %0, %1, %2" : "=v"(r) : "v"(voff), "s"(sbase) : "memory"); return r;
}
__device__ __forceinline__ u32x2 ld64_asm(const void* sbase, unsigned voff) {
  u32x2 r; asm volatile("global_load_dwordx2 %0, %1, %2" : "=v"(r) : "v"(voff), "s"(sbase) : "memory"); return r;
}
__device__ __forceinline__ u32x2 ld64_sc1(const void* sbase, unsigned voff) {
  u32x2 r; asm volatile("global_load_dwordx2 %0, %1, %2 sc1" : "=v"(r) : "v"(voff), "s"(sbase) : "memory"); return r;
}
template <int CTRL>
__device__ __forceinline__ float dpp_add(float x) {
  return x + __builtin_bit_cast(float, __builtin_amdgcn_mov_dpp(__builtin_bit_cast(int, x), CTRL, 0xf, 0xf, true));
}
__device__ __forceinline__ float wave_sum(float x) {
  x = dpp_add<0xB1>(x); x = dpp_add<0x4E>(x); x = dpp_add<0x141>(x); x = dpp_add<0x140>(x);
  auto s = __builtin_amdgcn_permlane16_swap(__float_as_uint(x), __float_as_uint(x), false, false);
  x = __uint_as_float(s[0]) + __uint_as_float(s[1]);
  auto u = __builtin_amdgcn_permlane32_swap(__float_as_uint(x), __float_as_uint(x), false, false);
  return __uint_as_float(u[0]) + __uint_as_float(u[1]);
}
__device__ __forceinline__ u32x4 widen16(u32x2 X, u32x2 Y) {
  auto a = __builtin_amdgcn_permlane16_swap(X[0], Y[0], false, false);
  auto b = __builtin_amdgcn_permlane16_swap(X[1], Y[1], false, false);
  return (u32x4){a[0], b[0], a[1], b[1]};
}

struct Params {
  const u16 *q, *k, *v, *shk, *ssmk, *ssmD, *w1, *w2;
  u16* out;
  char* ws;
};

__device__ __forceinline__ void wait_flags256(const char* ws, unsigned off, unsigned epoch, int lane) {
  gu32* f = (gu32*)(ws + off) + lane * FLAG_STRIDE;
#pragma unroll 1
  for (unsigned spins = 0; spins < SPIN_CAP; ++spins) {
    unsigned a = __hip_atomic_load(f, RLX_AGENT), b = __hip_atomic_load(f + 64 * FLAG_STRIDE, RLX_AGENT);
    unsigned c = __hip_atomic_load(f + 128 * FLAG_STRIDE, RLX_AGENT), d = __hip_atomic_load(f + 192 * FLAG_STRIDE, RLX_AGENT);
    if (__all((a == epoch) & (b == epoch) & (c == epoch) & (d == epoch))) break;
    __builtin_amdgcn_s_sleep(1);
  }
  __builtin_amdgcn_fence(__ATOMIC_ACQUIRE, "wavefront");
}

constexpr int L_KT = 32768, L_VT = L_KT + 72 * 128, L_QT = L_VT + 8192, L_OKV = L_QT + 8192, L_OQ = L_OKV + 8192, L_SS = L_OQ + 8192;
constexpr int H_ROW = 1152, H_COPY = 4728, H_BYTES = 9344, X_BYTES = 4160;
constexpr int L_H = 0, L_X = L_H + 2 * H_BYTES, L_QD = L_X + 2 * X_BYTES;
constexpr int CY_BYTES = 513 * 16;
constexpr int L_CY = L_QD + 16384;
constexpr int L_JUNKA = 76800;
constexpr int L_W1 = 77824;
constexpr int L_W1B = 0;
static_assert(L_CY + 4 * CY_BYTES <= L_JUNKA && L_JUNKA + 1024 <= L_W1 && L_W1 + 7 * 12288 <= 163840 && L_SS + 64 <= L_JUNKA, "lds");
static_assert(L_X + 2 * X_BYTES <= L_KT, "lds A/B overlap");
constexpr int L_W2A = 4 * 16384;
constexpr int L_CS = 147456;
constexpr int L_SST = L_CS + 1024;
constexpr int L_FST = L_SST + 2048;
constexpr int L_OUT1B = L_FST + 1024;
constexpr int L_JUNKC = L_OUT1B + 8192;
constexpr int L_EX1 = 0;
constexpr int L_OUT1A = 49152;
static_assert(L_JUNKC + 1024 <= 163840, "lds");
constexpr int L_RES = 155648;
constexpr int L_RING = 0;
constexpr int L_JUNKD = 147456;
constexpr int L_EX2 = 0;
constexpr int LDS_BYTES = 163840;

template <class F, int... I>
__device__ __forceinline__ void static_for_impl(F&& f, std::integer_sequence<int, I...>) { (f(std::integral_constant<int, I>{}), ...); }
template <int N, class F>
__device__ __forceinline__ void static_for(F&& f) { static_for_impl(f, std::make_integer_sequence<int, N>{}); }

template <int TS, int PAR, class F>
__device__ __forceinline__ void conv_main(const LAS char* xa, const LAS char* hb, f32x4 (&acc)[4][4], F&& job) {
  constexpr int U[4] = {TS == 0 ? 0 : 1, TS == 0 ? 3 : 2, TS == 0 ? 4 : 5, TS == 0 ? 7 : 6};
  constexpr int T[4] = {U[PAR ? 2 : 0], U[PAR ? 3 : 1], U[PAR ? 0 : 2], U[PAR ? 1 : 3]};
  constexpr int NA = U[3] + 1;
  bf16x8_t A[8], B[3][4];
#pragma unroll
  for (int p = 0; p < 4; ++p) B[0][p] = *(const LAS bf16x8_t*)(hb + p * H_ROW + 896);
#pragma unroll
  for (int a = NA - 1; a >= 0; --a) A[a] = tr_frag(xa + 512 * a, 32);
#pragma unroll
  for (int p = 0; p < 4; ++p) B[1][p] = *(const LAS bf16x8_t*)(hb + p * H_ROW + 896 - 128);
  SCHED();
  static_for<NA>([&](auto BB) {
    constexpr int bb = decltype(BB)::value;
    if (bb + 2 < NA) {
#pragma unroll
      for (int p = 0; p < 4; ++p) B[(bb + 2) % 3][p] = *(const LAS bf16x8_t*)(hb + p * H_ROW + 896 - 128 * (bb + 2));
    }
#pragma unroll
    for (int p = 0; p < 4; ++p)
#pragma unroll
      for (int j = 3; j >= 0; --j)
        if (T[j] >= bb) acc[j][p] = mfma16(A[T[j] - bb], B[bb % 3][p], acc[j][p]);
    if (bb + 2 < NA) interleave_mfma_ds<4>();
    SCHED();
    if constexpr (bb < 5) { job(std::integral_constant<int, 2 * bb>{}); job(std::integral_constant<int, 2 * bb + 1>{}); }
    SCHED();
  });
}

__global__ __launch_bounds__(512, 2) void h3_fused(Params p) {
  extern __shared__ __attribute__((aligned(16))) char smem_[];
  LAS char* sm = (LAS char*)smem_;
  const unsigned lb = (unsigned)(size_t)sm;
  const int b = blockIdx.x, t = threadIdx.x, lane = t & 63;
  const int w = __builtin_amdgcn_readfirstlane(t >> 6);
  const int x = b & 7, jb = b >> 3;
  const int g = lane >> 4, fr = lane & 15, q_ = fr >> 2, pp = lane & 3;
  const int rl = 4 * g + q_;
  char* ws = p.ws;
  const __amdgpu_buffer_rsrc_t rs = __builtin_amdgcn_make_buffer_rsrc(ws, 0, WS_BYTES, 0x00020000);
  STAMP(0); CSTAMP(0);

  const int dB = x * 64 + jb * 2;
  const int cb = w & 3, sl = 8 * (4 * (w >> 2) + g);
  const unsigned tvo = (lane >> 3) * (D_DIM * 2) + (lane & 7) * 16;
  const size_t tso = ((size_t)(jb * 64 + w * 8) * D_DIM + x * 64);
  dma16<0>(p.k + tso, tvo, lb + L_KT + 1024 + w * 1024);
  dma16<0>(p.v + tso, tvo, lb + L_VT + w * 1024);
  if (w == 0) {
    if (jb > 0) dma16<0>(p.k + tso - 8 * D_DIM, tvo, lb + L_KT);
    else *(LAS u32x4*)(sm + L_KT + lane * 16) = (u32x4){0, 0, 0, 0};
  }
  u32x2 wv = ld64_asm(p.shk + (size_t)(x * 64 + cb * 16) * 4, fr * 8);
  STAMP(32);
  const bool c_on = jb < 30;
  const int panel = x * 2 + (jb >= 15 ? 1 : 0), ct = jb >= 15 ? jb - 15 : jb;
  const int m0 = panel * 128, n0 = ct * 96;
  unsigned w1o[6];
#pragma unroll
  for (int u = 0; u < 6; ++u) {
    const int idx = ((w & 1) * 6 + u) * 64 + lane, r = (idx * 43691) >> 19, cp = idx - r * 12;
    int col = n0 + (cp ^ (2 * ((r >> 2) & 1))) * 8;
    col = col > F_DIM - 8 ? F_DIM - 8 : col;
    w1o[u] = (r * F_DIM + col) * 2;
  }
  auto w1_half = [&](int chunk, int ldst, int step) {
#pragma unroll
    for (int u = 0; u < 6; ++u) dma16<0>(p.w1 + (size_t)chunk * 64 * F_DIM, w1o[u], lb + ldst + ((w & 1) * 6 + u) * step);
  };
  STAMP(33);
  WAIT_V(0);
  asm volatile("" : "+v"(wv));
  dma16<0>(p.q + tso, tvo, lb + L_QT + w * 1024);
  u32x4 sq = ld128_asm(p.q + (size_t)(b * 8 + w) * D_DIM, lane * 16);
  unsigned ep0 = ld32_sc1(ws + WS_EPOCH, 0);
  unsigned dv0 = ld16_asm(p.ssmD + dB + (w >> 2), 0);
  if (w == 7) {
    unsigned long long pc; unsigned len;
    asm volatile("s_getpc_b64 %0\n.Lh3_ref_%=:\n\ts_mov_b32 %1, .Lh3_code_end-.Lh3_ref_%=" : "=s"(pc), "=s"(len));
    const unsigned nl = len >> 7;
    unsigned line = (unsigned)(jb * 8 + lane); line = line >= nl ? line - nl : line; line = line >= nl ? 0 : line;
    dma4((const void*)pc, line << 7, lb + L_JUNKA);
  }
  if (w == 6) {
    const char* a = ws + (size_t)(jb * 8 + (lane & 7)) * 65536;
    if (lane >= 8) a = (const char*)p.w1 + jb * 44032;
    if (lane >= 16) a = (const char*)p.w2 + jb * 44032;
    if (lane >= 24) a = (const char*)p.out + jb * 65536;
    if (lane >= 32) a = (const char*)p.ssmk + jb * 65536;
    dma4v(a, lb + L_JUNKA + 256);
  }
  LBAR();
  STAMP(1);
  const int tro = q_ * 128 + cb * 32 + 8 * pp + sl * 128;
  const int oo = (cb * 16 + fr) * 128 + (((sl >> 3) ^ (fr & 7)) << 4);
  {
    const u32x2 k0 = __builtin_bit_cast(u32x2, tr_read(sm + L_KT + 4 * 128 + tro));
    const u32x2 k1 = __builtin_bit_cast(u32x2, tr_read(sm + L_KT + 8 * 128 + tro));
    const u32x2 k2 = __builtin_bit_cast(u32x2, tr_read(sm + L_KT + 12 * 128 + tro));
    const u32x2 v0 = __builtin_bit_cast(u32x2, tr_read(sm + L_VT + tro));
    const u32x2 v1 = __builtin_bit_cast(u32x2, tr_read(sm + L_VT + 4 * 128 + tro));
    const float w0 = bf_lo(wv[0]), w1 = bf_hi(wv[0]), w2 = bf_lo(wv[1]), w3 = bf_hi(wv[1]);
    const float kx[11] = {bf_hi(k0[0]), bf_lo(k0[1]), bf_hi(k0[1]), bf_lo(k1[0]), bf_hi(k1[0]), bf_lo(k1[1]), bf_hi(k1[1]),
                          bf_lo(k2[0]), bf_hi(k2[0]), bf_lo(k2[1]), bf_hi(k2[1])};
    const float vx[8] = {bf_lo(v0[0]), bf_hi(v0[0]), bf_lo(v0[1]), bf_hi(v0[1]), bf_lo(v1[0]), bf_hi(v1[0]), bf_lo(v1[1]), bf_hi(v1[1])};
    float o[8];
#pragma unroll
    for (int e = 0; e < 8; ++e) o[e] = (kx[e] * w3 + kx[e + 1] * w2 + kx[e + 2] * w1 + kx[e + 3] * w0) * vx[e];
    *(LAS u32x4*)(sm + L_OKV + oo) = (u32x4){pk_bf16(o[0], o[1]), pk_bf16(o[2], o[3]), pk_bf16(o[4], o[5]), pk_bf16(o[6], o[7])};
  }
  LBAR();
  STAMP(15);
  const int toff = (t >> 3) * 128 + (((t & 7) ^ ((t >> 3) & 7)) << 4);
  const unsigned tgo = ((x * 64 + (t >> 3)) * S_LEN + jb * 64 + (t & 7) * 8) * 2;
  __builtin_amdgcn_raw_buffer_store_b128(*(const LAS u32x4*)(sm + L_OKV + toff), rs, WS_KVT + tgo, 0, AUX_SC1);
  WAIT_V(1);
  asm volatile("" : "+v"(sq), "+v"(ep0), "+v"(dv0));
  RAW_BAR();
  const unsigned epoch = (unsigned)__builtin_amdgcn_readfirstlane(ep0) + 1u;
  const float Dv = bf_lo(dv0);
  {
    const u32x2 q0 = __builtin_bit_cast(u32x2, tr_read(sm + L_QT + tro));
    const u32x2 q1 = __builtin_bit_cast(u32x2, tr_read(sm + L_QT + 4 * 128 + tro));
    f32x2 xs[4];
#pragma unroll
    for (int i = 0; i < 4; ++i) xs[i] = (f32x2){bf_lo(sq[i]), bf_hi(sq[i])};
    const f32x2 s2 = (xs[0] + xs[1]) + (xs[2] + xs[3]);
    const float mean = wave_sum(s2[0] + s2[1]) * (1.0f / D_DIM);
    *(LAS u32x4*)(sm + L_OQ + oo) = (u32x4){q0[0], q0[1], q1[0], q1[1]};
    f32x2 vs = {0.f, 0.f};
#pragma unroll
    for (int i = 0; i < 4; ++i) { const f32x2 dd = xs[i] - mean; vs += dd * dd; }
    const float var = rbf(wave_sum(vs[0] + vs[1]) * (1.0f / D_DIM));
    if (lane == 0) *(LAS unsigned*)(sm + L_SS + w * 4) = pk_bf16(mean, __builtin_amdgcn_sqrtf(rbf(var + 1e-5f)));
  }
  LBAR();
  __builtin_amdgcn_raw_buffer_store_b128(*(const LAS u32x4*)(sm + L_OQ + toff), rs, WS_QT + tgo, 0, AUX_SC1);
  if (t < 2) __builtin_amdgcn_raw_buffer_store_b128(*(const LAS u32x4*)(sm + L_SS + t * 16), rs, WS_STQ + b * 32 + t * 16, 0, AUX_SC1);
  STAMP(31);
  WAIT_V(0);
  LBAR();
  if (t == 0) __hip_atomic_store((gu32*)(ws + WS_FL1) + b * FLAG_STRIDE, epoch, RLX_AGENT);
  RAW_BAR();
  STAMP(2);
  {
    const int hc = t >> 8, hw = t & 255;
    u32x4 hv = ld128_asm(p.ssmk + (size_t)dB * S_LEN, (hc * S_LEN + hw * 8) * 2);
    if (w < 4) { w1_half(w >> 1, L_W1 + (w >> 1) * 12288, 1024); WAIT_V(6); } else { WAIT_V(0); }
    asm volatile("" : "+v"(hv));
    LAS char* hb = sm + L_H + hc * H_BYTES + 1020 - 4 * hw;
    const unsigned h0 = __builtin_amdgcn_perm(hv[1], hv[3], 0x07060302u), h1 = __builtin_amdgcn_perm(hv[1], hv[3], 0x05040100u);
    const unsigned h2 = __builtin_amdgcn_perm(hv[0], hv[2], 0x07060302u), h3 = __builtin_amdgcn_perm(hv[0], hv[2], 0x05040100u);
    *(LAS unsigned*)(hb + 0 * H_ROW) = h0; *(LAS unsigned*)(hb + H_COPY + 0 * H_ROW) = h0;
    *(LAS unsigned*)(hb + 1 * H_ROW) = h1; *(LAS unsigned*)(hb + H_COPY + 1 * H_ROW) = h1;
    *(LAS unsigned*)(hb + 2 * H_ROW) = h2; *(LAS unsigned*)(hb + H_COPY + 2 * H_ROW) = h2;
    *(LAS unsigned*)(hb + 3 * H_ROW) = h3; *(LAS unsigned*)(hb + H_COPY + 3 * H_ROW) = h3;
    if (t < 256) {
      const int r = t >> 4;
      *(LAS u32x2*)(sm + L_H + (r >> 3) * H_BYTES + ((r >> 2) & 1) * H_COPY + (r & 3) * H_ROW + 1024 + (t & 15) * 8) = (u32x2){0, 0};
    } else if (t < 260) *(LAS u32x4*)(sm + L_X + ((t - 256) >> 1) * X_BYTES + (t & 1) * 16) = (u32x4){0, 0, 0, 0};
    else if (t < 264) *(LAS u32x4*)(sm + L_CY + (t - 260) * CY_BYTES) = (u32x4){0, 0, 0, 0};
  }

  const int c2 = w >> 2, ts = (w >> 1) & 1, par = w & 1;
  const int tl0 = ts == 0 ? 0 : 1, tl1 = ts == 0 ? 3 : 2, tl2 = ts == 0 ? 4 : 5, tl3 = ts == 0 ? 7 : 6;
  const int tf0 = par ? tl2 : tl0, tf1 = par ? tl3 : tl1, tg0 = par ? tl0 : tl2, tg1 = par ? tl1 : tl3;
  const int sa = tf0 * 256 + fr * 16 + g * 4, sb = tf1 * 256 + fr * 16 + g * 4;
  if (w == 0) wait_flags256(ws, WS_FL1, epoch, lane);
  LBAR();
  STAMP(3);
  {
    const int d = dB + c2;
    dma16<AUX_SC1>(ws + WS_KVT + ((size_t)d * S_LEN + (w & 3) * 512) * 2, lane * 16, lb + L_X + c2 * X_BYTES + 32 + (w & 3) * 1024);
    u32x2 qv0 = ld64_sc1(ws + WS_QT + (size_t)d * S_LEN * 2, sa * 2), qv1 = ld64_sc1(ws + WS_QT + (size_t)d * S_LEN * 2, sb * 2);
    u32x4 sv0 = ld128_sc1(ws + WS_STQ, sa * 4), sv1 = ld128_sc1(ws + WS_STQ, sb * 4);
    WAIT_V(4);
    RAW_BAR();
    STAMP(4); CSTAMP(1);
    f32x4 acc[4][4];
#pragma unroll
    for (int j = 0; j < 4; ++j)
#pragma unroll
      for (int pz = 0; pz < 4; ++pz) acc[j][pz] = (f32x4){0, 0, 0, 0};
    const LAS char* xa = sm + L_X + c2 * X_BYTES + 8 + 64 * g + 8 * q_ + 8 * pp + 256 * par;
    const LAS char* hb = sm + L_H + c2 * H_BYTES + ((fr & 1) ? 0 : H_COPY) + 1016 - 8 * fr + 16 * g + 64 * par - 896;
    auto job = [&](auto J) {
      constexpr int j = decltype(J)::value;
      if constexpr (j == 0) {
        int r2 = ((b * 43) >> 3) + w; r2 = r2 > F_DIM - 1 ? F_DIM - 1 : r2;
        dma16<0>(p.w2 + (size_t)r2 * D_DIM, lane * 16, lb + L_JUNKA);
      } else if constexpr (j < 7) {
        const int ch = 2 + (w >> 1);
        dma16<0>(p.w1 + (size_t)ch * 64 * F_DIM, w1o[j - 1], lb + L_W1 + ch * 12288 + ((w & 1) * 6 + j - 1) * 1024);
      } else {
        if (w < 4) {
          const int u = (w >> 1) * 3 + (j - 7);
          dma16<0>(p.w1 + (size_t)6 * 64 * F_DIM, (w & 2) ? w1o[3 + j - 7] : w1o[j - 7], lb + L_W1 + 6 * 12288 + ((w & 1) * 6 + u) * 1024);
        }
      }
    };
    if (ts == 0) { if (par == 0) conv_main<0, 0>(xa, hb, acc, job); else conv_main<0, 1>(xa, hb, acc, job); }
    else         { if (par == 0) conv_main<1, 0>(xa, hb, acc, job); else conv_main<1, 1>(xa, hb, acc, job); }
    STAMP(5); CSTAMP(2);
#pragma unroll
    for (int j = 0; j < 4; ++j) {
      acc[j][3][1] += acc[j][2][0];
      acc[j][3][2] += acc[j][2][1] + acc[j][1][0];
      acc[j][3][3] += (acc[j][2][2] + acc[j][1][1]) + acc[j][0][0];
      acc[j][0][1] += acc[j][2][3] + acc[j][1][2];
      acc[j][0][2] += acc[j][1][3];
    }
    LAS char* cy = sm + L_CY + (c2 * 2 + par) * CY_BYTES + 16 + lane * 16;
    *(LAS f32x4*)(cy + tf0 * 1024) = acc[0][0];
    *(LAS f32x4*)(cy + tf1 * 1024) = acc[1][0];
    *(LAS f32x4*)(cy + tg0 * 1024) = acc[2][0];
    *(LAS f32x4*)(cy + tg1 * 1024) = acc[3][0];
    LAS char* qd = sm + L_QD + w * 2048 + lane * 16;
    *(LAS f32x4*)(qd) = acc[2][3]; *(LAS f32x4*)(qd + 1024) = acc[3][3];
    const u32x2 xv0 = *(const LAS u32x2*)(sm + L_X + c2 * X_BYTES + 32 + sa * 2), xv1 = *(const LAS u32x2*)(sm + L_X + c2 * X_BYTES + 32 + sb * 2);
    LBAR();
    STAMP(29);
    const int prev = g > 0 ? lane - 16 : (fr > 0 ? 47 + fr : -1);
    const LAS char* cy0 = sm + L_CY + (c2 * 2) * CY_BYTES + 16 + prev * 16;
    const LAS char* qs = sm + L_QD + (w ^ 1) * 2048 + lane * 16;
    WAIT_V(7);
    asm volatile("" : "+v"(qv0), "+v"(qv1), "+v"(sv0), "+v"(sv1));
    u32x2 ov[2];
#pragma unroll
    for (int i = 0; i < 2; ++i) {
      const int tf = i ? tf1 : tf0;
      const f32x4 pq = *(const LAS f32x4*)(qs + i * 1024);
      const f32x4 ca = *(const LAS f32x4*)(cy0 + tf * 1024), cb_ = *(const LAS f32x4*)(cy0 + CY_BYTES + tf * 1024);
      f32x4 y = acc[i][3] + pq;
      y[0] += ca[1] + cb_[1]; y[1] += ca[2] + cb_[2]; y[2] += ca[3] + cb_[3];
      const u32x2 xv = i ? xv1 : xv0, qv = i ? qv1 : qv0;
      const u32x4 sv = i ? sv1 : sv0;
      const float xx[4] = {bf_lo(xv[0]), bf_hi(xv[0]), bf_lo(xv[1]), bf_hi(xv[1])};
      const float qx[4] = {bf_lo(qv[0]), bf_hi(qv[0]), bf_lo(qv[1]), bf_hi(qv[1])};
      float o[4];
#pragma unroll
      for (int e = 0; e < 4; ++e) o[e] = qx[e] + ((qx[e] - bf_lo(sv[e])) * __builtin_amdgcn_rcpf(bf_hi(sv[e]))) * (y[e] + xx[e] * Dv);
      ov[i] = (u32x2){pk_bf16(o[0], o[1]), pk_bf16(o[2], o[3])};
    }
    const u32x4 o16 = widen16(ov[0], ov[1]);
    const int so = ((g & 1) ? tf1 : tf0) * 256 + fr * 16 + (g >> 1) * 8;
    __builtin_amdgcn_raw_buffer_store_b128(o16, rs, WS_HT + (d * S_LEN + so) * 2, 0, AUX_SC1);
    STAMP(30);
  }
  WAIT_V(0);
  LBAR();
  if (t == 0) __hip_atomic_store((gu32*)(ws + WS_FL2) + b * FLAG_STRIDE, epoch, RLX_AGENT);
  RAW_BAR();
  STAMP(6);

  const int kp = w >> 2, wm = (w >> 1) & 1, wn = w & 1;
  const int mt = x * 4 + (jb >> 3), dt = jb & 7;
  const int md0 = mt * 64, d0 = dt * 64;
  const int kq = w >> 1, dh = w & 1;
  unsigned wvo[2], hvo[2];
#pragma unroll
  for (int u = 0; u < 2; ++u) {
    const int r = ((w & 1) * 2 + u) * 8 + (lane >> 3), c = ((lane & 7) ^ (2 * ((r >> 1) & 3))) * 16;
    wvo[u] = r * (D_DIM * 2) + c; hvo[u] = r * (S_LEN * 2) + c;
  }
  auto load_w2 = [&](int step, int lds) {
    int ks = kq * 11 + step; ks = ks > 42 ? 42 : ks;
#pragma unroll
    for (int u = 0; u < 2; ++u) dma16<0>(p.w2 + (size_t)ks * 32 * D_DIM + d0, wvo[u], lb + lds + kq * 4096 + ((w & 1) * 2 + u) * 1024);
  };
  const bf16x8_t ONES = __builtin_bit_cast(bf16x8_t, (u32x4){0x3f803f80u, 0x3f803f80u, 0x3f803f80u, 0x3f803f80u});
  if (c_on) {
    bf16x8_t W1[8][3];
    f32x4 csa[3];
    if (w < 2) w1_half(7, L_W1B, 1024);
#pragma unroll
    for (int i = 0; i < 3; ++i) csa[i] = (f32x4){0, 0, 0, 0};
    int wo[3];
#pragma unroll
    for (int i = 0; i < 3; ++i) wo[i] = (kp * 32 + rl) * 192 + 8 * pp + (((wn * 3 + i) ^ ((rl >> 2) & 1)) << 5);
#pragma unroll
    for (int c = 0; c < 7; ++c)
#pragma unroll
      for (int i = 0; i < 3; ++i) {
        W1[c][i] = tr_frag(sm + L_W1 + c * 12288 + wo[i], 16 * 192);
        csa[i] = mfma16(ONES, W1[c][i], csa[i]);
      }
    WAIT_V(0);
    RAW_BAR();
    STAMP(28);
#pragma unroll
    for (int i = 0; i < 3; ++i) {
      W1[7][i] = tr_frag(sm + L_W1B + wo[i], 16 * 192);
      csa[i] = mfma16(ONES, W1[7][i], csa[i]);
    }
    LBAR();
    load_w2(0, L_W2A); load_w2(1, L_W2A + 16384); load_w2(2, L_W2A + 2 * 16384);
    STAMP(7);
    if (w == 0) wait_flags256(ws, WS_FL2, epoch, lane);
    LBAR();
    STAMP(8);
    if (b == 0 && t == 0) __hip_atomic_store((gu32*)(ws + WS_EPOCH), epoch, RLX_AGENT);
    const char* hsrc = ws + WS_HT + ((size_t)(w * 8) * S_LEN + m0) * 2;
    const int ra = lane >> 4, rb = ra + 4;
    const unsigned va = ra * (S_LEN * 2) + (((lane & 15) ^ (2 * ra)) << 4), vb = rb * (S_LEN * 2) + (((lane & 15) ^ (2 * rb)) << 4);
    auto load_ht = [&](int c) {
      dma16<AUX_SC1>(hsrc + (size_t)c * (64 * S_LEN * 2), va, lb + (c & 3) * 16384 + (w * 2) * 1024);
      dma16<AUX_SC1>(hsrc + (size_t)c * (64 * S_LEN * 2), vb, lb + (c & 3) * 16384 + (w * 2 + 1) * 1024);
    };
    load_ht(0); load_ht(1);
    if (wm == 0 && g == 0) {
#pragma unroll
      for (int i = 0; i < 3; ++i) *(LAS float*)(sm + L_CS + (kp * 96 + wn * 48 + i * 16 + fr) * 4) = csa[i][0];
    }
    f32x4 acc[4][3], sta[4];
    int aoff[4];
#pragma unroll
    for (int i = 0; i < 4; ++i) {
      sta[i] = (f32x4){0, 0, 0, 0};
      aoff[i] = (kp * 32 + rl) * 256 + (((wm * 4 + (i ^ (2 * kp))) ^ (rl & 7)) << 5) + 8 * pp;
#pragma unroll
      for (int jn = 0; jn < 3; ++jn) acc[i][jn] = (f32x4){0, 0, 0, 0};
    }
    auto g1_loop = [&](auto WN) {
      bf16x8_t A[2][4];
      auto half = [&](bf16x8_t (&Af)[4], const bf16x8_t (&Wf)[3], int i0) {
#pragma unroll
        for (int i = i0; i < i0 + 2; ++i) {
#pragma unroll
          for (int jn = 0; jn < 3; ++jn) acc[i][jn] = mfma16(Af[i], Wf[jn], acc[i][jn]);
          sta[i] = mfma16(Af[i], decltype(WN)::value ? Af[i] : ONES, sta[i]);
        }
      };
      auto warm_w2 = [&](int i) {
        int ks = kq * 11 + 5 + 2 * i + (w & 1); ks = ks > 42 ? 42 : ks;
        dma16<0>(p.w2 + (size_t)(ks * 32 + (jb >> 3) * 8) * D_DIM + d0, (lane >> 3) * (D_DIM * 2) + (lane & 7) * 16, lb + L_JUNKC);
      };
#define G1_ITER(c, N)                                                                                        \
      WAIT_V(N); RAW_BAR();                                                                                  \
      if ((c) == 0) STAMP(16); if ((c) == 1) { STAMP(17); CSTAMP(3); } if ((c) == 4) STAMP(18); if ((c) == 7) STAMP(19); \
      _Pragma("unroll") for (int i = 0; i < 4; ++i) A[(c) & 1][i] = tr_frag(sm + aoff[i] + ((c) & 3) * 16384, 4096); \
      if ((c) > 0) { half(A[((c) - 1) & 1], W1[(c) > 0 ? (c) - 1 : 0], 0); interleave_mfma_ds<8>(); }        \
      SCHED();                                                                                               \
      if ((c) + 2 < 8) load_ht((c) + 2);                                                                     \
      if ((c) == 1) load_w2(3, L_W2A + 3 * 16384);                                                           \
      if ((c) == 2) load_w2(4, L_W2A + 4 * 16384);                                                           \
      if ((c) >= 4 && (c) <= 6) warm_w2((c) - 4);                                                            \
      SCHED();                                                                                               \
      if ((c) > 0) half(A[((c) - 1) & 1], W1[(c) > 0 ? (c) - 1 : 0], 2);                                     \
      SCHED();
      G1_ITER(0, 2) G1_ITER(1, 2) G1_ITER(2, 4) G1_ITER(3, 6) G1_ITER(4, 4) G1_ITER(5, 3) G1_ITER(6, 4) G1_ITER(7, 2)
#undef G1_ITER
      half(A[1], W1[7], 0); half(A[1], W1[7], 2);
    };
    if (wn) g1_loop(std::integral_constant<bool, true>{}); else g1_loop(std::integral_constant<bool, false>{});
    STAMP(9); CSTAMP(4);
    LBAR();
    {
      LAS char* sp = sm + L_SST + ((kp * 2 + wn) * 128 + wm * 64) * 4 + kp * 128, *sq_ = sp - kp * 256;
      if (wn == 0) {
        if (fr == 0) {
          *(LAS f32x4*)(sp + g * 16) = sta[0]; *(LAS f32x4*)(sp + 64 + g * 16) = sta[1];
          *(LAS f32x4*)(sq_ + 128 + g * 16) = sta[2]; *(LAS f32x4*)(sq_ + 192 + g * 16) = sta[3];
        }
      } else if (q_ == g) {
        float dg[4];
#pragma unroll
        for (int i = 0; i < 4; ++i) dg[i] = pp == 0 ? sta[i][0] : (pp == 1 ? sta[i][1] : (pp == 2 ? sta[i][2] : sta[i][3]));
        *(LAS float*)(sp + fr * 4) = dg[0]; *(LAS float*)(sp + 64 + fr * 4) = dg[1];
        *(LAS float*)(sq_ + 128 + fr * 4) = dg[2]; *(LAS float*)(sq_ + 192 + fr * 4) = dg[3];
      }
    }
#pragma unroll
    for (int i = 0; i < 2; ++i)
#pragma unroll
      for (int jn = 0; jn < 3; ++jn) *(LAS f32x4*)(sm + L_EX1 + w * 6144 + (i * 3 + jn) * 1024 + lane * 16) = acc[2 + i][jn];
    LBAR();
    STAMP(20);
    if (t < 128) {
      const LAS float* sp = (const LAS float*)(sm + L_SST) + t;
      const float mean = (sp[0] + sp[256]) * (1.0f / D_DIM);
      const float var = fmaxf((sp[128] + sp[384]) * (1.0f / D_DIM) - mean * mean, 0.f);
      *(LAS float*)(sm + L_FST + t * 4) = -rbf(mean);
      *(LAS float*)(sm + L_FST + 512 + t * 4) = -1.44269504f * __builtin_amdgcn_rcpf(rbf(__builtin_amdgcn_sqrtf(rbf(rbf(var) + 1e-5f))));
    }
    f32x2 csv[3];
#pragma unroll
    for (int jn = 0; jn < 3; ++jn) {
      const int nl = wn * 48 + jn * 16 + fr;
      const float c = *(const LAS float*)(sm + L_CS + nl * 4) + *(const LAS float*)(sm + L_CS + (96 + nl) * 4);
      csv[jn] = (f32x2){c, c};
    }
#pragma unroll
    for (int i = 0; i < 2; ++i)
#pragma unroll
      for (int jn = 0; jn < 3; ++jn) acc[i][jn] += *(const LAS f32x4*)(sm + L_EX1 + (w ^ 4) * 6144 + (i * 3 + jn) * 1024 + lane * 16);
    LBAR();
    u32x2 ov[2][3];
#pragma unroll
    for (int i = 0; i < 2; ++i) {
      const int ml = wm * 64 + (kp * 2 + i) * 16 + g * 4;
      const f32x4 nmu = *(const LAS f32x4*)(sm + L_FST + ml * 4), zz = *(const LAS f32x4*)(sm + L_FST + 512 + ml * 4);
      const f32x2 nm0 = {nmu[0], nmu[1]}, nm1 = {nmu[2], nmu[3]}, z0 = {zz[0], zz[1]}, z1 = {zz[2], zz[3]};
      const f32x2 cL = {-1.44269504f, -1.44269504f};
#pragma unroll
      for (int jn = 0; jn < 3; ++jn) {
        const f32x4 a = acc[i][jn];
        const f32x2 e0 = (nm0 * csv[jn] + (f32x2){a[0], a[1]}) * z0, e1 = (nm1 * csv[jn] + (f32x2){a[2], a[3]}) * z1;
        const f32x2 d0_ = (f32x2){__builtin_amdgcn_exp2f(e0[0]), __builtin_amdgcn_exp2f(e0[1])} * cL + cL;
        const f32x2 d1_ = (f32x2){__builtin_amdgcn_exp2f(e1[0]), __builtin_amdgcn_exp2f(e1[1])} * cL + cL;
        const f32x2 o0 = e0 * (f32x2){__builtin_amdgcn_rcpf(d0_[0]), __builtin_amdgcn_rcpf(d0_[1])};
        const f32x2 o1 = e1 * (f32x2){__builtin_amdgcn_rcpf(d1_[0]), __builtin_amdgcn_rcpf(d1_[1])};
        ov[i][jn] = (u32x2){pk_bf16(o0[0], o0[1]), pk_bf16(o1[0], o1[1])};
      }
    }
#pragma unroll
    for (int jn = 0; jn < 3; ++jn) {
      const u32x4 o16 = widen16(ov[0][jn], ov[1][jn]);
      const int nl = wn * 48 + jn * 16 + fr;
      const int ch = wm * 8 + (kp * 2 + (g & 1)) * 2 + (g >> 1);
      const int base = nl < 64 ? L_OUT1A + nl * 256 : L_OUT1B + (nl - 64) * 256;
      *(LAS u32x4*)(sm + base + ((ch ^ (nl & 7)) << 4)) = o16;
    }
    LBAR();
    STAMP(21);
#pragma unroll
    for (int i = 0; i < 3; ++i) {
      const int nl = i * 32 + (t >> 4), ch = t & 15;
      const int base = i < 2 ? L_OUT1A + nl * 256 : L_OUT1B + (nl - 64) * 256;
      const u32x4 o16 = *(const LAS u32x4*)(sm + base + ((ch ^ (nl & 7)) << 4));
      if (n0 + nl < F_DIM) __builtin_amdgcn_raw_buffer_store_b128(o16, rs, WS_H1T + ((n0 + nl) * S_LEN + m0 + ch * 8) * 2, 0, AUX_SC1);
    }
  } else {
    if (w == 0) wait_flags256(ws, WS_FL2, epoch, lane);
    RAW_BAR();
    load_w2(0, L_W2A); load_w2(1, L_W2A + 16384); load_w2(2, L_W2A + 2 * 16384); load_w2(3, L_W2A + 3 * 16384); load_w2(4, L_W2A + 4 * 16384);
  }
  WAIT_V(0);
  STAMP(22);
  LBAR();
  if (c_on && t == 0) __hip_atomic_store((gu32*)(ws + WS_FL3) + b * FLAG_STRIDE, epoch, RLX_AGENT);
  RAW_BAR();
  STAMP(10);
  if (c_on) {
#pragma unroll
    for (int i = 0; i < 3; ++i) {
      int nn = n0 + i * 32 + w * 4; nn = nn > F_DIM - 4 ? F_DIM - 4 : nn;
      dma16<AUX_SC1>(ws + WS_H1T + ((size_t)nn * S_LEN + m0) * 2, (lane >> 4) * (S_LEN * 2) + (lane & 15) * 16, lb + L_JUNKD + w * 1024);
    }
  }

  {
    load_w2(5, 0); load_w2(6, 16384); load_w2(7, 2 * 16384); load_w2(8, 3 * 16384);
    const int f3 = (rl >> 1) & 3;
    bf16x8_t W2[11][2];
    int woff[2], boff[4];
#pragma unroll
    for (int i = 0; i < 2; ++i) woff[i] = kq * 4096 + rl * 128 + (((dh * 2 + i) ^ f3) << 5) + 8 * pp;
#pragma unroll
    for (int i = 0; i < 4; ++i) boff[i] = L_RING + kq * 4096 + rl * 128 + ((i ^ f3) << 5) + 8 * pp;
#pragma unroll
    for (int s = 0; s < 5; ++s)
#pragma unroll
      for (int i = 0; i < 2; ++i) W2[s][i] = tr_frag(sm + woff[i] + L_W2A + s * 16384, 2048);
    LBAR();
    load_w2(9, 4 * 16384); load_w2(10, 5 * 16384);
    {
      const int r = lane >> 3;
      dma16<AUX_SC1>(ws + WS_HT + ((size_t)(d0 + w * 8) * S_LEN + md0) * 2, r * (S_LEN * 2) + (((lane & 7) ^ (2 * ((r >> 1) & 3))) << 4), lb + L_RES + w * 1024);
    }
    WAIT_V(5);
    RAW_BAR();
    STAMP(23);
#pragma unroll
    for (int s = 5; s < 9; ++s)
#pragma unroll
      for (int i = 0; i < 2; ++i) W2[s][i] = tr_frag(sm + woff[i] + (s - 5) * 16384, 2048);
    WAIT_V(0);
    RAW_BAR();
#pragma unroll
    for (int s = 9; s < 11; ++s)
#pragma unroll
      for (int i = 0; i < 2; ++i) W2[s][i] = tr_frag(sm + woff[i] + (s - 5) * 16384, 2048);
    STAMP(11);
    if (w == 0) {
      const int h = (mt >> 1) & 1;
      gu32* f = (gu32*)(ws + WS_FL3) + (x + 8 * (15 * h + (lane < 15 ? lane : 0))) * FLAG_STRIDE;
#pragma unroll 1
      for (unsigned spins = 0; spins < SPIN_CAP; ++spins) {
        unsigned a = __hip_atomic_load(f, RLX_AGENT);
        if (__all(a == epoch)) break;
        __builtin_amdgcn_s_sleep(1);
      }
      __builtin_amdgcn_fence(__ATOMIC_ACQUIRE, "wavefront");
    }
    LBAR();
    STAMP(12);
    auto load_h1 = [&](int step) {
      int ks = kq * 11 + step; ks = ks > 42 ? 42 : ks;
#pragma unroll
      for (int u = 0; u < 2; ++u)
        dma16<AUX_SC1>(ws + WS_H1T + ((size_t)ks * 32 * S_LEN + md0) * 2, hvo[u], lb + L_RING + (step % 6) * 16384 + kq * 4096 + ((w & 1) * 2 + u) * 1024);
    };
    load_h1(0); load_h1(1); load_h1(2);
    f32x4 acc[2][4];
#pragma unroll
    for (int i = 0; i < 2; ++i)
#pragma unroll
      for (int jm = 0; jm < 4; ++jm) acc[i][jm] = (f32x4){0, 0, 0, 0};
    bf16x8_t B[2][4];
    auto half2 = [&](const bf16x8_t (&Wf)[2], const bf16x8_t (&Bf)[4], int j0) {
#pragma unroll
      for (int jm = j0; jm < j0 + 2; ++jm)
#pragma unroll
        for (int i = 0; i < 2; ++i) acc[i][jm] = mfma16(Wf[i], Bf[jm], acc[i][jm]);
    };
#define G2_ITER(r, N)                                                                                                   \
    WAIT_V(N); LBAR();                                                                                                  \
    if ((r) == 0) STAMP(24); if ((r) == 1) { STAMP(25); CSTAMP(5); } if ((r) == 5) STAMP(26); if ((r) == 10) STAMP(27); \
    _Pragma("unroll") for (int i = 0; i < 4; ++i) B[(r) & 1][i] = tr_frag(sm + boff[i] + ((r) % 6) * 16384, 2048);      \
    if ((r) > 0) { half2(W2[(r) > 0 ? (r) - 1 : 0], B[((r) - 1) & 1], 0);                                               \
      _Pragma("unroll") for (int i = 0; i < 4; ++i) { __builtin_amdgcn_sched_group_barrier(0x8, 1, 0); __builtin_amdgcn_sched_group_barrier(0x100, 2, 0); } } \
    SCHED();                                                                                                            \
    if ((r) + 3 <= 10) load_h1((r) + 3);                                                                                \
    SCHED();                                                                                                            \
    if ((r) > 0) half2(W2[(r) > 0 ? (r) - 1 : 0], B[((r) - 1) & 1], 2);                                                 \
    SCHED();
    G2_ITER(0, 4) G2_ITER(1, 4) G2_ITER(2, 4) G2_ITER(3, 4) G2_ITER(4, 4) G2_ITER(5, 4)
    G2_ITER(6, 4) G2_ITER(7, 4) G2_ITER(8, 4) G2_ITER(9, 2) G2_ITER(10, 0)
#undef G2_ITER
    if (kq < 3) { half2(W2[10], B[0], 0); half2(W2[10], B[0], 2); }
    STAMP(13); CSTAMP(6);
    u32x2 ru[2];
#pragma unroll
    for (int i = 0; i < 2; ++i) {
      const int r = (dh * 2 + i) * 16 + rl;
      ru[i] = __builtin_bit_cast(u32x2, tr_read(sm + L_RES + r * 128 + ((kq ^ f3) << 5) + 8 * pp));
    }
    LBAR();
#pragma unroll
    for (int i = 0; i < 2; ++i)
#pragma unroll
      for (int jm = 0; jm < 4; ++jm) *(LAS f32x4*)(sm + L_EX2 + ((dh * 4 + jm) * 8 + kq * 2 + i) * 1024 + lane * 16) = acc[i][jm];
    LBAR();
    u32x2 ov[2];
#pragma unroll
    for (int i = 0; i < 2; ++i) {
      const LAS char* e = sm + L_EX2 + ((dh * 4 + kq) * 8 + i) * 1024 + lane * 16;
      const f32x4 a = (*(const LAS f32x4*)(e) + *(const LAS f32x4*)(e + 2048)) + (*(const LAS f32x4*)(e + 4096) + *(const LAS f32x4*)(e + 6144));
      ov[i] = (u32x2){pk_bf16(bf_lo(ru[i][0]) + rbf(a[0]), bf_hi(ru[i][0]) + rbf(a[1])), pk_bf16(bf_lo(ru[i][1]) + rbf(a[2]), bf_hi(ru[i][1]) + rbf(a[3]))};
    }
    const u32x4 o16 = widen16(ov[0], ov[1]);
    {
      const __amdgpu_buffer_rsrc_t ro = __builtin_amdgcn_make_buffer_rsrc(p.out, 0, S_LEN * D_DIM * 2, 0x00020000);
      __builtin_amdgcn_raw_buffer_store_b128(o16, ro, ((md0 + kq * 16 + fr) * D_DIM + d0 + (dh * 2 + (g & 1)) * 16 + (g >> 1) * 8) * 2, 0, 16);
    }
  }
  asm volatile(".Lh3_code_end:");
  STAMP(14); CSTAMP(7);
}

extern "C" void kernel_launch(void* const* d_in, const int* in_sizes, int n_in, void* d_out, int out_size, void* d_ws, size_t ws_size,
                              hipStream_t stream) {
  Params p;
  p.q = (const u16*)d_in[0];
  p.k = (const u16*)d_in[1];
  p.v = (const u16*)d_in[2];
  p.shk = (const u16*)d_in[3];
  p.ssmk = (const u16*)d_in[4];
  p.ssmD = (const u16*)d_in[5];
  p.w1 = (const u16*)d_in[7];
  p.w2 = (const u16*)d_in[8];
  p.out = (u16*)d_out;
  p.ws = (char*)d_ws;
  (void)hipFuncSetAttribute((const void*)h3_fused, hipFuncAttributeMaxDynamicSharedMemorySize, LDS_BYTES);
  h3_fused<<<256, 512, LDS_BYTES, stream>>>(p);
}
```

```cpp
#include <hip/hip_runtime.h>
#include <hip/hip_bf16.h>
#include <stdint.h>
#include <type_traits>
#include <utility>

typedef unsigned short u16;
typedef short bf16x8 __attribute__((ext_vector_type(8)));
typedef short s16x4 __attribute__((ext_vector_type(4)));
typedef float f32x4 __attribute__((ext_vector_type(4)));
typedef float f32x2 __attribute__((ext_vector_type(2)));
typedef unsigned u32x4 __attribute__((ext_vector_type(4)));
typedef unsigned u32x2 __attribute__((ext_vector_type(2)));
typedef __bf16 bf16x2_t __attribute__((ext_vector_type(2)));
typedef __bf16 bf16x8_t __attribute__((ext_vector_type(8)));
typedef short v4i16_t __attribute__((ext_vector_type(4)));
#define LAS __attribute__((address_space(3)))
#define GAS __attribute__((address_space(1)))
typedef GAS unsigned gu32;
#define RLX_AGENT __ATOMIC_RELAXED, __HIP_MEMORY_SCOPE_AGENT

constexpr int S_LEN = 2048, D_DIM = 512, F_DIM = 1376;
constexpr int AUX_SC1 = 16;

constexpr unsigned WS_KVT = 0, WS_QT = 2u << 20, WS_HT = 4u << 20, WS_H1B = 6u << 20, WS_STQ = 12u << 20;
constexpr unsigned WS_FL1 = WS_STQ + 65536, WS_FLB = WS_FL1 + 16384, WS_FLC = WS_FLB + 2 * 16384, WS_EPOCH = WS_FLC + 2 * 16384;
constexpr unsigned WS_STAMP = WS_EPOCH + 4096;
constexpr unsigned WS_BYTES = 16u << 20;
constexpr int FLAG_STRIDE = 16;
constexpr unsigned SPIN_CAP = 1u << 18;

#define WAIT_V(n) asm volatile("s_waitcnt vmcnt(%0)" ::"n"(n) : "memory")
#define WAIT_L0() asm volatile("s_waitcnt lgkmcnt(0)" ::: "memory")
#define RAW_BAR() do { asm volatile("" ::: "memory"); __builtin_amdgcn_s_barrier(); asm volatile("" ::: "memory"); } while (0)
#define LBAR() do { WAIT_L0(); RAW_BAR(); } while (0)
#define SCHED() __builtin_amdgcn_sched_barrier(0)
template <int N> __device__ __forceinline__ void interleave_mfma_ds() {
#pragma unroll
  for (int i = 0; i < N; ++i) { __builtin_amdgcn_sched_group_barrier(0x8, 1, 0); __builtin_amdgcn_sched_group_barrier(0x100, 1, 0); }
}

#define STAMP(i)
#define CSTAMP(i)

__device__ __forceinline__ float bf_lo(unsigned x) { return __uint_as_float(x << 16); }
__device__ __forceinline__ float bf_hi(unsigned x) { return __uint_as_float(x & 0xffff0000u); }
__device__ __forceinline__ unsigned pk_bf16(float lo, float hi) {
  bf16x2_t v; v.x = (__bf16)lo; v.y = (__bf16)hi;
  return __builtin_bit_cast(unsigned, v);
}
__device__ __forceinline__ float rbf(float x) { return __uint_as_float(((unsigned)__builtin_bit_cast(u16, (__bf16)x)) << 16); }
__device__ __forceinline__ s16x4 tr_read(const LAS char* p) {
  return __builtin_bit_cast(s16x4, __builtin_amdgcn_ds_read_tr16_b64_v4i16((LAS v4i16_t*)p));
}
__device__ __forceinline__ bf16x8_t cat8(s16x4 a, s16x4 b) {
  bf16x8 r = __builtin_shufflevector(a, b, 0, 1, 2, 3, 4, 5, 6, 7);
  return __builtin_bit_cast(bf16x8_t, r);
}
__device__ __forceinline__ bf16x8_t tr_frag(const LAS char* p, int second) { return cat8(tr_read(p), tr_read(p + second)); }
__device__ __forceinline__ f32x4 mfma16(bf16x8_t a, bf16x8_t b, f32x4 c) {
  return __builtin_amdgcn_mfma_f32_16x16x32_bf16(a, b, c, 0, 0, 0);
}
template <int AUX>
__device__ __forceinline__ void dma16(const void* sbase, unsigned voff, unsigned lds) {
  if constexpr (AUX == AUX_SC1)
    asm volatile("s_mov_b32 m0, %2\n\ts_nop 0\n\tglobal_load_lds_dwordx4 %0, %1 sc1" ::"v"(voff), "s"(sbase), "s"(lds) : "memory");
  else
    asm volatile("s_mov_b32 m0, %2\n\ts_nop 0\n\tglobal_load_lds_dwordx4 %0, %1" ::"v"(voff), "s"(sbase), "s"(lds) : "memory");
}
__device__ __forceinline__ void dma4(const void* sbase, unsigned voff, unsigned lds) {
  asm volatile("s_mov_b32 m0, %2\n\ts_nop 0\n\tglobal_load_lds_dword %0, %1" ::"v"(voff), "s"(sbase), "s"(lds) : "memory");
}
__device__ __forceinline__ void dma4v(const void* vaddr, unsigned lds) {
  asm volatile("s_mov_b32 m0, %1\n\ts_nop 0\n\tglobal_load_lds_dword %0, off" ::"v"(vaddr), "s"(lds) : "memory");
}
__device__ __forceinline__ u32x4 ld128_asm(const void* sbase, unsigned voff) {
  u32x4 r; asm volatile("global_load_dwordx4 %0, %1, %2" : "=v"(r) : "v"(voff), "s"(sbase) : "memory"); return r;
}
__device__ __forceinline__ u32x4 ld128_sc1(const void* sbase, unsigned voff) {
  u32x4 r; asm volatile("global_load_dwordx4 %0, %1, %2 sc1" : "=v"(r) : "v"(voff), "s"(sbase) : "memory"); return r;
}
__device__ __forceinline__ unsigned ld32_sc1(const void* sbase, unsigned voff) {
  unsigned r; asm volatile("global_load_dword %0, %1, %2 sc1" : "=v"(r) : "v"(voff), "s"(sbase) : "memory"); return r;
}
__device__ __forceinline__ unsigned ld16_asm(const void* sbase, unsigned voff) {
  unsigned r; asm volatile("global_load_ushort %0, %1, %2" : "=v"(r) : "v"(voff), "s"(sbase) : "memory"); return r;
}
__device__ __forceinline__ u32x2 ld64_asm(const void* sbase, unsigned voff) {
  u32x2 r; asm volatile("global_load_dwordx2 %0, %1, %2" : "=v"(r) : "v"(voff), "s"(sbase) : "memory"); return r;
}
__device__ __forceinline__ u32x2 ld64_sc1(const void* sbase, unsigned voff) {
  u32x2 r; asm volatile("global_load_dwordx2 %0, %1, %2 sc1" : "=v"(r) : "v"(voff), "s"(sbase) : "memory"); return r;
}
template <int CTRL>
__device__ __forceinline__ float dpp_add(float x) {
  return x + __builtin_bit_cast(float, __builtin_amdgcn_mov_dpp(__builtin_bit_cast(int, x), CTRL, 0xf, 0xf, true));
}
__device__ __forceinline__ float wave_sum(float x) {
  x = dpp_add<0xB1>(x); x = dpp_add<0x4E>(x); x = dpp_add<0x141>(x); x = dpp_add<0x140>(x);
  auto s = __builtin_amdgcn_permlane16_swap(__float_as_uint(x), __float_as_uint(x), false, false);
  x = __uint_as_float(s[0]) + __uint_as_float(s[1]);
  auto u = __builtin_amdgcn_permlane32_swap(__float_as_uint(x), __float_as_uint(x), false, false);
  return __uint_as_float(u[0]) + __uint_as_float(u[1]);
}
__device__ __forceinline__ u32x4 widen16(u32x2 X, u32x2 Y) {
  auto a = __builtin_amdgcn_permlane16_swap(X[0], Y[0], false, false);
  auto b = __builtin_amdgcn_permlane16_swap(X[1], Y[1], false, false);
  return (u32x4){a[0], b[0], a[1], b[1]};
}

struct Params {
  const u16 *q, *k, *v, *shk, *ssmk, *ssmD, *w1, *w2;
  u16* out;
  char* ws;
};

__device__ __forceinline__ void wait_flags256(const char* ws, unsigned off, unsigned epoch, int lane) {
  gu32* f = (gu32*)(ws + off) + lane * FLAG_STRIDE;
#pragma unroll 1
  for (unsigned spins = 0; spins < SPIN_CAP; ++spins) {
    unsigned a = __hip_atomic_load(f, RLX_AGENT), b = __hip_atomic_load(f + 64 * FLAG_STRIDE, RLX_AGENT);
    unsigned c = __hip_atomic_load(f + 128 * FLAG_STRIDE, RLX_AGENT), d = __hip_atomic_load(f + 192 * FLAG_STRIDE, RLX_AGENT);
    if (__all((a == epoch) & (b == epoch) & (c == epoch) & (d == epoch))) break;
    __builtin_amdgcn_s_sleep(1);
  }
  __builtin_amdgcn_fence(__ATOMIC_ACQUIRE, "wavefront");
}

constexpr int L_KT = 32768, L_VT = L_KT + 72 * 128, L_QT = L_VT + 8192, L_OKV = L_QT + 8192, L_OQ = L_OKV + 8192, L_SS = L_OQ + 8192;
constexpr int H_ROW = 1152, H_COPY = 4728, H_BYTES = 9344, X_BYTES = 4160;
constexpr int L_H = 0, L_X = L_H + 2 * H_BYTES, L_QD = L_X + 2 * X_BYTES;
constexpr int CY_BYTES = 257 * 16;
constexpr int L_CY = L_QD + 8192;
constexpr int L_JUNKA = 76800;
constexpr int L_W1 = 77824;
constexpr int L_W1B = 51712;
static_assert(L_CY + 4 * CY_BYTES <= L_W1B && L_W1B + 12288 <= L_JUNKA, "lds");
static_assert(L_CY + 4 * CY_BYTES <= L_JUNKA && L_JUNKA + 1024 <= L_W1 && L_W1 + 7 * 12288 <= 163840 && L_SS + 64 <= L_JUNKA, "lds");
static_assert(L_X + 2 * X_BYTES <= L_KT, "lds A/B overlap");
constexpr int L_W2A = 4 * 16384;
constexpr int L_CS = 147456;
constexpr int L_SST = L_CS + 1024;
constexpr int L_FST = L_SST + 1024;
constexpr int L_JUNKC = 159744;
constexpr int L_EX1 = 0;
constexpr int L_OUT1 = 2 * 16384;
static_assert(L_JUNKC + 1024 <= 163840, "lds");
constexpr int L_W2X = 9 * 16384;
constexpr int L_RES = 4 * 16384;
constexpr int L_EX2 = 5 * 16384;
constexpr int LDS_BYTES = 163840;

template <class F, int... I>
__device__ __forceinline__ void static_for_impl(F&& f, std::integer_sequence<int, I...>) { (f(std::integral_constant<int, I>{}), ...); }
template <int N, class F>
__device__ __forceinline__ void static_for(F&& f) { static_for_impl(f, std::make_integer_sequence<int, N>{}); }

template <int TS, int PAR, int H, class F>
__device__ __forceinline__ void conv_pass(const LAS char* xa, const LAS char* hb, bf16x8_t (&A)[8], f32x4 (&acc)[2][4], F&& job) {
  constexpr int U0 = H ? (TS ? 5 : 4) : (TS ? 1 : 0), U1 = H ? (TS ? 6 : 7) : (TS ? 2 : 3);
  constexpr int T[2] = {PAR ? U1 : U0, PAR ? U0 : U1};
  constexpr int NA = U1 + 1, A0 = H ? (TS ? 3 : 4) : 0;
  bf16x8_t B[3][4];
#pragma unroll
  for (int p = 0; p < 4; ++p) B[0][p] = *(const LAS bf16x8_t*)(hb + p * H_ROW + 896);
#pragma unroll
  for (int a = NA - 1; a >= A0; --a) A[a] = tr_frag(xa + 512 * a, 32);
#pragma unroll
  for (int p = 0; p < 4; ++p) B[1][p] = *(const LAS bf16x8_t*)(hb + p * H_ROW + 896 - 128);
#pragma unroll
  for (int j = 0; j < 2; ++j)
#pragma unroll
    for (int p = 0; p < 4; ++p) acc[j][p] = (f32x4){0, 0, 0, 0};
  SCHED();
  static_for<NA>([&](auto BB) {
    constexpr int bb = decltype(BB)::value;
    if (bb + 2 < NA) {
#pragma unroll
      for (int p = 0; p < 4; ++p) B[(bb + 2) % 3][p] = *(const LAS bf16x8_t*)(hb + p * H_ROW + 896 - 128 * (bb + 2));
    }
#pragma unroll
    for (int p = 0; p < 4; ++p)
#pragma unroll
      for (int j = 0; j < 2; ++j)
        if (T[j] >= bb) acc[j][p] = mfma16(A[T[j] - bb], B[bb % 3][p], acc[j][p]);
    if (bb + 2 < NA) interleave_mfma_ds<4>();
    SCHED();
    job(std::integral_constant<int, H>{}, BB);
    SCHED();
  });
}

__global__ __launch_bounds__(512, 2) void h3_fused(Params p) {
  extern __shared__ __attribute__((aligned(16))) char smem_[];
  LAS char* sm = (LAS char*)smem_;
  const unsigned lb = (unsigned)(size_t)sm;
  const int b = blockIdx.x, t = threadIdx.x, lane = t & 63;
  const int w = __builtin_amdgcn_readfirstlane(t >> 6);
  const int x = b & 7, jb = b >> 3;
  const int g = lane >> 4, fr = lane & 15, q_ = fr >> 2, pp = lane & 3;
  const int rl = 4 * g + q_;
  char* ws = p.ws;
  const __amdgpu_buffer_rsrc_t rs = __builtin_amdgcn_make_buffer_rsrc(ws, 0, WS_BYTES, 0x00020000);
  STAMP(0); CSTAMP(0);

  const int dB = x * 64 + jb * 2;
  const int cb = w & 3, sl = 8 * (4 * (w >> 2) + g);
  const unsigned tvo = (lane >> 3) * (D_DIM * 2) + (lane & 7) * 16;
  const size_t tso = ((size_t)(jb * 64 + w * 8) * D_DIM + x * 64);
  dma16<0>(p.k + tso, tvo, lb + L_KT + 1024 + w * 1024);
  dma16<0>(p.v + tso, tvo, lb + L_VT + w * 1024);
  if (w == 0) {
    if (jb > 0) dma16<0>(p.k + tso - 8 * D_DIM, tvo, lb + L_KT);
    else *(LAS u32x4*)(sm + L_KT + lane * 16) = (u32x4){0, 0, 0, 0};
  }
  u32x2 wv = ld64_asm(p.shk + (size_t)(x * 64 + cb * 16) * 4, fr * 8);
  STAMP(32);
  const bool c_on = jb < 30;
  const int panel = x * 2 + (jb >= 15 ? 1 : 0), ct = jb >= 15 ? jb - 15 : jb;
  const int m0 = panel * 128, n0 = ct * 96;
  unsigned w1o[6];
#pragma unroll
  for (int u = 0; u < 6; ++u) {
    const int idx = ((w & 1) * 6 + u) * 64 + lane, r = (idx * 43691) >> 19, cp = idx - r * 12;
    int col = n0 + (cp ^ (2 * ((r >> 2) & 1))) * 8;
    col = col > F_DIM - 8 ? F_DIM - 8 : col;
    w1o[u] = (r * F_DIM + col) * 2;
  }
  auto w1_half = [&](int chunk, int ldst, int step) {
#pragma unroll
    for (int u = 0; u < 6; ++u) dma16<0>(p.w1 + (size_t)chunk * 64 * F_DIM, w1o[u], lb + ldst + ((w & 1) * 6 + u) * step);
  };
  STAMP(33);
  WAIT_V(0);
  asm volatile("" : "+v"(wv));
  dma16<0>(p.q + tso, tvo, lb + L_QT + w * 1024);
  u32x4 sq = ld128_asm(p.q + (size_t)(b * 8 + w) * D_DIM, lane * 16);
  unsigned ep0 = ld32_sc1(ws + WS_EPOCH, 0);
  unsigned dv0 = ld16_asm(p.ssmD + dB + (w >> 2), 0);
  if (w == 7) {
    unsigned long long pc; unsigned len;
    asm volatile("s_getpc_b64 %0\n.Lh3_ref_%=:\n\ts_mov_b32 %1, .Lh3_code_end-.Lh3_ref_%=" : "=s"(pc), "=s"(len));
    const unsigned nl = len >> 7;
    unsigned line = (unsigned)(jb * 8 + lane); line = line >= nl ? line - nl : line; line = line >= nl ? 0 : line;
    dma4((const void*)pc, line << 7, lb + L_JUNKA);
  }
  if (w == 6) {
    const char* a = ws + (size_t)(jb * 8 + (lane & 7)) * 65536;
    if (lane >= 8) a = (const char*)p.w1 + jb * 44032;
    if (lane >= 16) a = (const char*)p.w2 + jb * 44032;
    if (lane >= 24) a = (const char*)p.out + jb * 65536;
    if (lane >= 32) a = (const char*)p.ssmk + jb * 65536;
    dma4v(a, lb + L_JUNKA + 256);
  }
  LBAR();
  STAMP(1);
  const int tro = q_ * 128 + cb * 32 + 8 * pp + sl * 128;
  const int oo = (cb * 16 + fr) * 128 + (((sl >> 3) ^ (fr & 7)) << 4);
  {
    const u32x2 k0 = __builtin_bit_cast(u32x2, tr_read(sm + L_KT + 4 * 128 + tro));
    const u32x2 k1 = __builtin_bit_cast(u32x2, tr_read(sm + L_KT + 8 * 128 + tro));
    const u32x2 k2 = __builtin_bit_cast(u32x2, tr_read(sm + L_KT + 12 * 128 + tro));
    const u32x2 v0 = __builtin_bit_cast(u32x2, tr_read(sm + L_VT + tro));
    const u32x2 v1 = __builtin_bit_cast(u32x2, tr_read(sm + L_VT + 4 * 128 + tro));
    const float w0 = bf_lo(wv[0]), w1 = bf_hi(wv[0]), w2 = bf_lo(wv[1]), w3 = bf_hi(wv[1]);
    const float kx[11] = {bf_hi(k0[0]), bf_lo(k0[1]), bf_hi(k0[1]), bf_lo(k1[0]), bf_hi(k1[0]), bf_lo(k1[1]), bf_hi(k1[1]),
                          bf_lo(k2[0]), bf_hi(k2[0]), bf_lo(k2[1]), bf_hi(k2[1])};
    const float vx[8] = {bf_lo(v0[0]), bf_hi(v0[0]), bf_lo(v0[1]), bf_hi(v0[1]), bf_lo(v1[0]), bf_hi(v1[0]), bf_lo(v1[1]), bf_hi(v1[1])};
    float o[8];
#pragma unroll
    for (int e = 0; e < 8; ++e) o[e] = (kx[e] * w3 + kx[e + 1] * w2 + kx[e + 2] * w1 + kx[e + 3] * w0) * vx[e];
    *(LAS u32x4*)(sm + L_OKV + oo) = (u32x4){pk_bf16(o[0], o[1]), pk_bf16(o[2], o[3]), pk_bf16(o[4], o[5]), pk_bf16(o[6], o[7])};
  }
  LBAR();
  STAMP(15);
  const int toff = (t >> 3) * 128 + (((t & 7) ^ ((t >> 3) & 7)) << 4);
  const unsigned tgo = ((x * 64 + (t >> 3)) * S_LEN + jb * 64 + (t & 7) * 8) * 2;
  __builtin_amdgcn_raw_buffer_store_b128(*(const LAS u32x4*)(sm + L_OKV + toff), rs, WS_KVT + tgo, 0, AUX_SC1);
  WAIT_V(1);
  asm volatile("" : "+v"(sq), "+v"(ep0), "+v"(dv0));
  RAW_BAR();
  const unsigned epoch = (unsigned)__builtin_amdgcn_readfirstlane(ep0) + 1u;
  const float Dv = bf_lo(dv0);
  {
    const u32x2 q0 = __builtin_bit_cast(u32x2, tr_read(sm + L_QT + tro));
    const u32x2 q1 = __builtin_bit_cast(u32x2, tr_read(sm + L_QT + 4 * 128 + tro));
    f32x2 xs[4];
#pragma unroll
    for (int i = 0; i < 4; ++i) xs[i] = (f32x2){bf_lo(sq[i]), bf_hi(sq[i])};
    const f32x2 s2 = (xs[0] + xs[1]) + (xs[2] + xs[3]);
    const float mean = wave_sum(s2[0] + s2[1]) * (1.0f / D_DIM);
    *(LAS u32x4*)(sm + L_OQ + oo) = (u32x4){q0[0], q0[1], q1[0], q1[1]};
    f32x2 vs = {0.f, 0.f};
#pragma unroll
    for (int i = 0; i < 4; ++i) { const f32x2 dd = xs[i] - mean; vs += dd * dd; }
    const float var = rbf(wave_sum(vs[0] + vs[1]) * (1.0f / D_DIM));
    if (lane == 0) *(LAS unsigned*)(sm + L_SS + w * 4) = pk_bf16(mean, __builtin_amdgcn_sqrtf(rbf(var + 1e-5f)));
  }
  LBAR();
  __builtin_amdgcn_raw_buffer_store_b128(*(const LAS u32x4*)(sm + L_OQ + toff), rs, WS_QT + tgo, 0, AUX_SC1);
  if (t < 2) __builtin_amdgcn_raw_buffer_store_b128(*(const LAS u32x4*)(sm + L_SS + t * 16), rs, WS_STQ + b * 32 + t * 16, 0, AUX_SC1);
  STAMP(31);
  WAIT_V(0);
  LBAR();
  if (t == 0) __hip_atomic_store((gu32*)(ws + WS_FL1) + b * FLAG_STRIDE, epoch, RLX_AGENT);
  RAW_BAR();
  STAMP(2);
  {
    const int hc = t >> 8, hw = t & 255;
    u32x4 hv = ld128_asm(p.ssmk + (size_t)dB * S_LEN, (hc * S_LEN + hw * 8) * 2);
    const unsigned o1 = min((unsigned)(b * 5504 + (w < 6 ? w : 5) * 1024), (unsigned)(D_DIM * F_DIM * 2 - 1024));
    dma16<0>((const char*)p.w1 + o1, lane * 16, lb + L_JUNKA);
    WAIT_V(1);
    asm volatile("" : "+v"(hv));
    LAS char* hb = sm + L_H + hc * H_BYTES + 1020 - 4 * hw;
    const unsigned h0 = __builtin_amdgcn_perm(hv[1], hv[3], 0x07060302u), h1 = __builtin_amdgcn_perm(hv[1], hv[3], 0x05040100u);
    const unsigned h2 = __builtin_amdgcn_perm(hv[0], hv[2], 0x07060302u), h3 = __builtin_amdgcn_perm(hv[0], hv[2], 0x05040100u);
    *(LAS unsigned*)(hb + 0 * H_ROW) = h0; *(LAS unsigned*)(hb + H_COPY + 0 * H_ROW) = h0;
    *(LAS unsigned*)(hb + 1 * H_ROW) = h1; *(LAS unsigned*)(hb + H_COPY + 1 * H_ROW) = h1;
    *(LAS unsigned*)(hb + 2 * H_ROW) = h2; *(LAS unsigned*)(hb + H_COPY + 2 * H_ROW) = h2;
    *(LAS unsigned*)(hb + 3 * H_ROW) = h3; *(LAS unsigned*)(hb + H_COPY + 3 * H_ROW) = h3;
    if (t < 256) {
      const int r = t >> 4;
      *(LAS u32x2*)(sm + L_H + (r >> 3) * H_BYTES + ((r >> 2) & 1) * H_COPY + (r & 3) * H_ROW + 1024 + (t & 15) * 8) = (u32x2){0, 0};
    } else if (t < 260) *(LAS u32x4*)(sm + L_X + ((t - 256) >> 1) * X_BYTES + (t & 1) * 16) = (u32x4){0, 0, 0, 0};
    else if (t < 264) *(LAS u32x4*)(sm + L_CY + (t - 260) * CY_BYTES) = (u32x4){0, 0, 0, 0};
  }

  const int c2 = w >> 2, ts = (w >> 1) & 1, par = w & 1;
  const int u00 = ts ? 1 : 0, u01 = ts ? 2 : 3, u10 = ts ? 5 : 4, u11 = ts ? 6 : 7;
  const int tf0 = par ? u01 : u00, tg0 = par ? u00 : u01, tf1 = par ? u11 : u10, tg1 = par ? u10 : u11;
  const int sa = tf0 * 256 + fr * 16 + g * 4, sb = tf1 * 256 + fr * 16 + g * 4;
  const int kp = w >> 2, wm = (w >> 1) & 1, wn = w & 1;
  const bf16x8_t ONES = __builtin_bit_cast(bf16x8_t, (u32x4){0x3f803f80u, 0x3f803f80u, 0x3f803f80u, 0x3f803f80u});
  bf16x8_t W1[8][3];
  int wo[3];
#pragma unroll
  for (int i = 0; i < 3; ++i) wo[i] = (kp * 32 + rl) * 192 + 8 * pp + (((wn * 3 + i) ^ ((rl >> 2) & 1)) << 5);
  if (w == 0) wait_flags256(ws, WS_FL1, epoch, lane);
  LBAR();
  STAMP(3);
  {
    const int d = dB + c2;
    dma16<AUX_SC1>(ws + WS_KVT + ((size_t)d * S_LEN + (w & 3) * 512) * 2, lane * 16, lb + L_X + c2 * X_BYTES + 32 + (w & 3) * 1024);
    u32x2 qv0 = ld64_sc1(ws + WS_QT + (size_t)d * S_LEN * 2, sa * 2), qv1 = ld64_sc1(ws + WS_QT + (size_t)d * S_LEN * 2, sb * 2);
    u32x4 sv0 = ld128_sc1(ws + WS_STQ, sa * 4), sv1 = ld128_sc1(ws + WS_STQ, sb * 4);
    WAIT_V(4);
    RAW_BAR();
    STAMP(4); CSTAMP(1);
    f32x4 acc[2][4];
    bf16x8_t A[8];
    const LAS char* xa = sm + L_X + c2 * X_BYTES + 8 + 64 * g + 8 * q_ + 8 * pp + 256 * par;
    const LAS char* hb = sm + L_H + c2 * H_BYTES + ((fr & 1) ? 0 : H_COPY) + 1016 - 8 * fr + 16 * g + 64 * par - 896;
    const int prev = g > 0 ? lane - 16 : (fr > 0 ? 47 + fr : -1);
    unsigned fl[4];
    auto w1_read = [&](auto CC) {
      constexpr int c = decltype(CC)::value;
#pragma unroll
      for (int i = 0; i < 3; ++i) W1[c][i] = tr_frag(sm + (c < 7 ? L_W1 + c * 12288 : L_W1B) + wo[i], 16 * 192);
    };
    auto w1_third = [&](int ch, int j3) {
      const int c = ch + (w >> 2), u = ((w >> 1) & 1) * 3 + j3;
      dma16<0>(p.w1 + (size_t)c * 64 * F_DIM, (w & 2) ? w1o[3 + j3] : w1o[j3], lb + (c < 7 ? L_W1 + c * 12288 : L_W1B) + ((w & 1) * 6 + u) * 1024);
    };
    auto job = [&](auto HH, auto BB) {
      constexpr int h = decltype(HH)::value, bb = decltype(BB)::value;
      if constexpr (h == 0) {
        if constexpr (bb < 3) w1_third(0, bb);
      } else {
        if constexpr (bb == 0) {
          int r2 = ((b * 43) >> 3) + w; r2 = r2 > F_DIM - 1 ? F_DIM - 1 : r2;
          dma16<0>(p.w2 + (size_t)r2 * D_DIM, lane * 16, lb + L_JUNKA);
        }
        if constexpr (bb == 2) {
          WAIT_V(5);
          RAW_BAR();
          if (t == 0) __hip_atomic_store((gu32*)(ws + WS_FLB) + b * FLAG_STRIDE, epoch, RLX_AGENT);
          if (t >= 64 && t < 68) {
            LAS char* c = sm + L_CY + (t - 64) * CY_BYTES;
            *(LAS f32x4*)c = *(const LAS f32x4*)(c + 256 * 16);
          }
        }
        if constexpr (bb < 3) {
          const int ch = 2 + (w >> 1);
#pragma unroll
          for (int u = 2 * bb; u < 2 * bb + 2; ++u) dma16<0>(p.w1 + (size_t)ch * 64 * F_DIM, w1o[u], lb + L_W1 + ch * 12288 + ((w & 1) * 6 + u) * 1024);
        }
        if constexpr (bb == 3) { w1_third(6, 0); w1_third(6, 1); w1_read(std::integral_constant<int, 0>{}); }
        if constexpr (bb == 4) { w1_third(6, 2); w1_read(std::integral_constant<int, 1>{}); }
        if constexpr (bb == 5) {
          WAIT_V(3);
          RAW_BAR();
          if (w == 0) {
#pragma unroll
            for (int i = 0; i < 4; ++i) fl[i] = ld32_sc1(ws + WS_FLB, (lane + 64 * i) * (FLAG_STRIDE * 4));
          }
          w1_read(std::integral_constant<int, 2>{}); w1_read(std::integral_constant<int, 3>{});
        }
        if constexpr (bb == 6) { w1_read(std::integral_constant<int, 4>{}); w1_read(std::integral_constant<int, 5>{}); }
      }
    };
    auto epilogue = [&](int tf, int tg, int s0, u32x2 qv, u32x4 sv, auto LAST) {
#pragma unroll
      for (int j = 0; j < 2; ++j) {
        acc[j][3][1] += acc[j][2][0];
        acc[j][3][2] += acc[j][2][1] + acc[j][1][0];
        acc[j][3][3] += (acc[j][2][2] + acc[j][1][1]) + acc[j][0][0];
        acc[j][0][1] += acc[j][2][3] + acc[j][1][2];
        acc[j][0][2] += acc[j][1][3];
      }
      LAS char* cy = sm + L_CY + (c2 * 2 + par) * CY_BYTES + 16 + lane * 16;
      *(LAS f32x4*)(cy + (tf & 3) * 1024) = acc[0][0];
      *(LAS f32x4*)(cy + (tg & 3) * 1024) = acc[1][0];
      *(LAS f32x4*)(sm + L_QD + w * 1024 + lane * 16) = acc[1][3];
      const u32x2 xv = *(const LAS u32x2*)(sm + L_X + c2 * X_BYTES + 32 + s0 * 2);
      if constexpr (decltype(LAST)::value) {
        WAIT_V(0);
        if (w == 0) {
          asm volatile("" : "+v"(fl[0]), "+v"(fl[1]), "+v"(fl[2]), "+v"(fl[3]));
          if (!__all((fl[0] == epoch) & (fl[1] == epoch) & (fl[2] == epoch) & (fl[3] == epoch))) wait_flags256(ws, WS_FLB, epoch, lane);
        }
      }
      LBAR();
      const LAS char* cy0 = sm + L_CY + (c2 * 2) * CY_BYTES + 16 + prev * 16 + (tf & 3) * 1024;
      const f32x4 pq = *(const LAS f32x4*)(sm + L_QD + (w ^ 1) * 1024 + lane * 16);
      const f32x4 ca = *(const LAS f32x4*)(cy0), cb_ = *(const LAS f32x4*)(cy0 + CY_BYTES);
      f32x4 y = acc[0][3] + pq;
      y[0] += ca[1] + cb_[1]; y[1] += ca[2] + cb_[2]; y[2] += ca[3] + cb_[3];
      const float xx[4] = {bf_lo(xv[0]), bf_hi(xv[0]), bf_lo(xv[1]), bf_hi(xv[1])};
      const float qx[4] = {bf_lo(qv[0]), bf_hi(qv[0]), bf_lo(qv[1]), bf_hi(qv[1])};
      float o[4];
#pragma unroll
      for (int e = 0; e < 4; ++e) o[e] = qx[e] + ((qx[e] - bf_lo(sv[e])) * __builtin_amdgcn_rcpf(bf_hi(sv[e]))) * (y[e] + xx[e] * Dv);
      __builtin_amdgcn_raw_buffer_store_b64((u32x2){pk_bf16(o[0], o[1]), pk_bf16(o[2], o[3])}, rs, WS_HT + (d * S_LEN + s0) * 2, 0, AUX_SC1);
    };
    if (ts == 0) { if (par == 0) conv_pass<0, 0, 0>(xa, hb, A, acc, job); else conv_pass<0, 1, 0>(xa, hb, A, acc, job); }
    else         { if (par == 0) conv_pass<1, 0, 0>(xa, hb, A, acc, job); else conv_pass<1, 1, 0>(xa, hb, A, acc, job); }
    STAMP(34);
    WAIT_V(3);
    asm volatile("" : "+v"(qv0), "+v"(qv1), "+v"(sv0), "+v"(sv1));
    epilogue(tf0, tg0, sa, qv0, sv0, std::false_type{});
    STAMP(35);
    if (ts == 0) { if (par == 0) conv_pass<0, 0, 1>(xa, hb, A, acc, job); else conv_pass<0, 1, 1>(xa, hb, A, acc, job); }
    else         { if (par == 0) conv_pass<1, 0, 1>(xa, hb, A, acc, job); else conv_pass<1, 1, 1>(xa, hb, A, acc, job); }
    STAMP(5); CSTAMP(2);
    epilogue(tf1, tg1, sb, qv1, sv1, std::true_type{});
    STAMP(30);
  }
#pragma unroll
  for (int c = 6; c < 8; ++c)
#pragma unroll
    for (int i = 0; i < 3; ++i) W1[c][i] = tr_frag(sm + (c < 7 ? L_W1 + c * 12288 : L_W1B) + wo[i], 16 * 192);
  LBAR();
  STAMP(6);

  const int rt = x * 2 + (jb >= 15 ? 1 : 0);
  const int mt = x * 4 + (jb >> 3), dt = jb & 7;
  const int d0 = dt * 64;
  const int kq = w >> 1, dh = w & 1;
  const int f3 = (rl >> 1) & 3;
  unsigned wvo[2], avo[2];
#pragma unroll
  for (int u = 0; u < 2; ++u) {
    const int r = ((w & 1) * 2 + u) * 8 + (lane >> 3), c = ((lane & 7) ^ (2 * ((r >> 1) & 3))) * 16;
    wvo[u] = r * (D_DIM * 2) + c;
    const int ra = u * 8 + (lane >> 3);
    avo[u] = ra * (S_LEN * 2) + (((lane & 7) ^ (2 * ((ra >> 1) & 3))) << 4);
  }
  auto load_w2 = [&](int step, int lds) {
    int ks = kq * 11 + step; ks = ks > 42 ? 42 : ks;
#pragma unroll
    for (int u = 0; u < 2; ++u) dma16<0>(p.w2 + (size_t)ks * 32 * D_DIM + d0, wvo[u], lb + lds + kq * 4096 + ((w & 1) * 2 + u) * 1024);
  };
  bf16x8_t W2[11][2];
  const unsigned flc_off = (x + 8 * (15 * ((mt >> 1) & 1) + (lane < 15 ? lane : 0))) * (FLAG_STRIDE * 4);
  unsigned flc = 0;
  int woff[2];
#pragma unroll
  for (int i = 0; i < 2; ++i) woff[i] = kq * 4096 + rl * 128 + (((dh * 2 + i) ^ f3) << 5) + 8 * pp;
  if (c_on) {
    const char* hsrc = ws + WS_HT + ((size_t)(w * 16) * S_LEN + rt * 64) * 2;
    auto load_ht = [&](int h, int j) {
      const int slot = h ? (j + 3) & 3 : j;
      dma16<AUX_SC1>(hsrc + (size_t)j * (128 * S_LEN * 2) + h * 2048, avo[0], lb + slot * 16384 + (w * 2) * 1024);
      dma16<AUX_SC1>(hsrc + (size_t)j * (128 * S_LEN * 2) + h * 2048 + 8 * S_LEN * 2, avo[0], lb + slot * 16384 + (w * 2 + 1) * 1024);
    };
    load_ht(0, 0); load_ht(0, 1);
    f32x4 csa[3];
#pragma unroll
    for (int i = 0; i < 3; ++i) csa[i] = (f32x4){0, 0, 0, 0};
#pragma unroll
    for (int c = 0; c < 8; ++c)
#pragma unroll
      for (int i = 0; i < 3; ++i) csa[i] = mfma16(ONES, W1[c][i], csa[i]);
    if (wm == 0 && g == 0) {
#pragma unroll
      for (int i = 0; i < 3; ++i) *(LAS float*)(sm + L_CS + (kp * 96 + wn * 48 + i * 16 + fr) * 4) = csa[i][0];
    }
    STAMP(7);
    int aoff[2];
#pragma unroll
    for (int i = 0; i < 2; ++i) aoff[i] = (kp * 32 + rl) * 128 + (((wm * 2 + (i ^ kp)) ^ f3) << 5) + 8 * pp;
    static_for<2>([&](auto HH) {
      constexpr int h = decltype(HH)::value;
      unsigned fl[4];
      f32x4 acc[2][3], sta[2];
#pragma unroll
      for (int i = 0; i < 2; ++i) {
        sta[i] = (f32x4){0, 0, 0, 0};
#pragma unroll
        for (int jn = 0; jn < 3; ++jn) acc[i][jn] = (f32x4){0, 0, 0, 0};
      }
      auto g1_loop = [&](auto WN) {
        bf16x8_t A[2][4];
        auto half = [&](bf16x8_t (&Af)[4], const bf16x8_t (&Wf)[3], int u) {
#pragma unroll
          for (int i = 0; i < 2; ++i) {
#pragma unroll
            for (int jn = 0; jn < 3; ++jn) acc[i][jn] = mfma16(Af[2 * u + i], Wf[jn], acc[i][jn]);
            sta[i] = mfma16(Af[2 * u + i], decltype(WN)::value ? Af[2 * u + i] : ONES, sta[i]);
          }
        };
#define G1_ITER(j, N)                                                                                        \
        WAIT_V(N); RAW_BAR();                                                                                \
               \
        if ((j) == h && t == 0) __hip_atomic_store((gu32*)(ws + (h == 0 ? WS_FLB + 16384 : WS_FLC)) + b * FLAG_STRIDE, epoch, RLX_AGENT); \
        if ((j) == 0) { if (h == 0) STAMP(16); else STAMP(37); }                                             \
        _Pragma("unroll") for (int u = 0; u < 2; ++u)                                                        \
          _Pragma("unroll") for (int i = 0; i < 2; ++i)                                                      \
            A[(j) & 1][2 * u + i] = tr_frag(sm + aoff[i] + (h ? ((j) + 3) & 3 : (j)) * 16384 + u * 8192, 2048); \
        if ((j) > 0) { half(A[((j) - 1) & 1], W1[(j) > 0 ? 2 * (j) - 2 : 0], 0); interleave_mfma_ds<8>(); }  \
        SCHED();                                                                                             \
        if ((j) + 2 < 4) { load_ht(h, (j) + 2); load_w2(3 * h + (j), L_W2A + (3 * h + (j)) * 16384); }       \
        if ((j) == 2) {                                                                                      \
          if (h == 0) {                                                                                      \
            if (w == 0) { _Pragma("unroll") for (int i = 0; i < 4; ++i) fl[i] = ld32_sc1(ws + WS_FLB + 16384, (lane + 64 * i) * (FLAG_STRIDE * 4)); } \
          } else {                                                                                           \
              \
            _Pragma("unroll") for (int v = 0; v < 2; ++v) {                                                  \
              int ks = kq * 11 + 5 + 3 * (w & 1) + v; ks = ks > 42 ? 42 : ks;                                \
              dma16<0>(p.w2 + (size_t)(ks * 32 + (jb >> 3) * 8) * D_DIM + d0, (lane >> 3) * (D_DIM * 2) + (lane & 7) * 16, lb + L_JUNKC); \
            }                                                                                                \
          }                                                                                                  \
        }                                                                                                    \
        SCHED();                                                                                             \
        if ((j) > 0) half(A[((j) - 1) & 1], W1[(j) > 0 ? 2 * (j) - 1 : 0], 1);                               \
        SCHED();
        if constexpr (h == 0) {
          G1_ITER(0, 2) G1_ITER(1, 4) G1_ITER(2, 6)
          if (w == 0) { WAIT_V(6); } else { WAIT_V(2); }
          G1_ITER(3, 63)
        } else {
          G1_ITER(0, 4) G1_ITER(1, 4) G1_ITER(2, 6) G1_ITER(3, 4)
        }
#undef G1_ITER
        half(A[1], W1[6], 0); half(A[1], W1[7], 1);
      };
      if (wn) g1_loop(std::integral_constant<bool, true>{}); else g1_loop(std::integral_constant<bool, false>{});
      if (h == 0) STAMP(9); else STAMP(38);
      constexpr int EX = h == 0 ? 7 * 16384 : L_EX1;
      if constexpr (h == 1) LBAR();
      if (h == 0) STAMP(25); else STAMP(28);
      {
        LAS char* sp = sm + L_SST + ((kp * 2 + wn) * 64 + wm * 32) * 4;
        LAS char *s0 = sp + kp * 64, *s1 = sp + (kp ^ 1) * 64;
        if (wn == 0) {
          if (fr == 0) { *(LAS f32x4*)(s0 + g * 16) = sta[0]; *(LAS f32x4*)(s1 + g * 16) = sta[1]; }
        } else if (q_ == g) {
          *(LAS float*)(s0 + fr * 4) = pp == 0 ? sta[0][0] : (pp == 1 ? sta[0][1] : (pp == 2 ? sta[0][2] : sta[0][3]));
          *(LAS float*)(s1 + fr * 4) = pp == 0 ? sta[1][0] : (pp == 1 ? sta[1][1] : (pp == 2 ? sta[1][2] : sta[1][3]));
        }
      }
#pragma unroll
      for (int jn = 0; jn < 3; ++jn) *(LAS f32x4*)(sm + EX + w * 3072 + jn * 1024 + lane * 16) = acc[1][jn];
      if constexpr (h == 0) {
        if (w == 0) {
          WAIT_V(0);
          asm volatile("" : "+v"(fl[0]), "+v"(fl[1]), "+v"(fl[2]), "+v"(fl[3]));
          if (!__all((fl[0] == epoch) & (fl[1] == epoch) & (fl[2] == epoch) & (fl[3] == epoch))) wait_flags256(ws, WS_FLB + 16384, epoch, lane);
        }
      }
      if constexpr (h == 1) {
        WAIT_V(2);
        RAW_BAR();
#pragma unroll
        for (int s = 0; s < 5; ++s)
#pragma unroll
          for (int i = 0; i < 2; ++i) W2[s][i] = tr_frag(sm + L_W2A + woff[i] + s * 16384, 2048);
      }
      LBAR();
      if (h == 0) STAMP(26); else STAMP(29);
      if constexpr (h == 0) {
        if (b == 0 && t == 0) __hip_atomic_store((gu32*)(ws + WS_EPOCH), epoch, RLX_AGENT);
        load_ht(1, 0);
        load_ht(1, 1);
        load_w2(2, L_W2A + 2 * 16384);
      }
      if constexpr (h == 1) { load_w2(5, L_W2A); load_w2(6, L_W2A + 16384); }
      if (t < 64) {
        const LAS float* sp = (const LAS float*)(sm + L_SST) + t;
        const float mean = (sp[0] + sp[128]) * (1.0f / D_DIM);
        const float var = fmaxf((sp[64] + sp[192]) * (1.0f / D_DIM) - mean * mean, 0.f);
        *(LAS float*)(sm + L_FST + t * 4) = -rbf(mean);
        *(LAS float*)(sm + L_FST + 256 + t * 4) = -1.44269504f * __builtin_amdgcn_rcpf(rbf(__builtin_amdgcn_sqrtf(rbf(rbf(var) + 1e-5f))));
      }
      f32x2 csv[3];
#pragma unroll
      for (int jn = 0; jn < 3; ++jn) {
        const int nl = wn * 48 + jn * 16 + fr;
        const float c = *(const LAS float*)(sm + L_CS + nl * 4) + *(const LAS float*)(sm + L_CS + (96 + nl) * 4);
        csv[jn] = (f32x2){c, c};
      }
#pragma unroll
      for (int jn = 0; jn < 3; ++jn) acc[0][jn] += *(const LAS f32x4*)(sm + EX + (w ^ 4) * 3072 + jn * 1024 + lane * 16);
      LBAR();
      if (h == 0) STAMP(27); else STAMP(31 + 5);
      if constexpr (h == 1) {
        if (w == 0) flc = ld32_sc1(ws + WS_FLC, flc_off);
        load_w2(7, L_W2A + 2 * 16384); load_w2(8, L_W2A + 3 * 16384);
      }
      {
        const int ms = wm * 2 + kp;
        const int ml = ms * 16 + g * 4;
        const f32x4 nmu = *(const LAS f32x4*)(sm + L_FST + ml * 4), zz = *(const LAS f32x4*)(sm + L_FST + 256 + ml * 4);
        const f32x2 nm0 = {nmu[0], nmu[1]}, nm1 = {nmu[2], nmu[3]}, z0 = {zz[0], zz[1]}, z1 = {zz[2], zz[3]};
        const f32x2 cL = {-1.44269504f, -1.44269504f};
#pragma unroll
        for (int jn = 0; jn < 3; ++jn) {
          const f32x4 a = acc[0][jn];
          const f32x2 e0 = (nm0 * csv[jn] + (f32x2){a[0], a[1]}) * z0, e1 = (nm1 * csv[jn] + (f32x2){a[2], a[3]}) * z1;
          const f32x2 d0_ = (f32x2){__builtin_amdgcn_exp2f(e0[0]), __builtin_amdgcn_exp2f(e0[1])} * cL + cL;
          const f32x2 d1_ = (f32x2){__builtin_amdgcn_exp2f(e1[0]), __builtin_amdgcn_exp2f(e1[1])} * cL + cL;
          const f32x2 o0 = e0 * (f32x2){__builtin_amdgcn_rcpf(d0_[0]), __builtin_amdgcn_rcpf(d0_[1])};
          const f32x2 o1 = e1 * (f32x2){__builtin_amdgcn_rcpf(d1_[0]), __builtin_amdgcn_rcpf(d1_[1])};
          const int nl = wn * 48 + jn * 16 + fr;
          *(LAS u32x2*)(sm + L_OUT1 + nl * 128 + (((ms * 2 + (g >> 1)) ^ (nl & 7)) << 4) + (g & 1) * 8) = (u32x2){pk_bf16(o0[0], o0[1]), pk_bf16(o1[0], o1[1])};
        }
      }
      LBAR();
      if (h == 0) STAMP(21); else STAMP(39);
      if constexpr (h == 1) load_w2(9, L_W2A + 4 * 16384);
#pragma unroll
      for (int i = 0; i < 2; ++i) {
        const int pc = t + 512 * i;
        if (i == 0 || t < 256) {
          const int sbk = pc >= 384 ? 1 : 0, rem = pc - sbk * 384, nl = rem >> 2, c4 = rem & 3;
          const u32x4 o16 = *(const LAS u32x4*)(sm + L_OUT1 + nl * 128 + (((sbk * 4 + c4) ^ (nl & 7)) << 4));
          if (n0 + nl < F_DIM)
            __builtin_amdgcn_raw_buffer_store_b128(o16, rs, WS_H1B + (((h * 32 + rt * 2 + sbk) * F_DIM + n0 + nl) * 32 + c4 * 8) * 2, 0, AUX_SC1);
        }
      }
      WAIT_L0();
    });
  } else {
    WAIT_V(0);
    RAW_BAR();
    if (t == 0) __hip_atomic_store((gu32*)(ws + WS_FLB + 16384) + b * FLAG_STRIDE, epoch, RLX_AGENT);
    if (w == 0) wait_flags256(ws, WS_FLB + 16384, epoch, lane);
    RAW_BAR();
#pragma unroll
    for (int s = 0; s < 5; ++s) load_w2(s, L_W2A + s * 16384);
    WAIT_V(0);
    RAW_BAR();
#pragma unroll
    for (int s = 0; s < 5; ++s)
#pragma unroll
      for (int i = 0; i < 2; ++i) W2[s][i] = tr_frag(sm + L_W2A + woff[i] + s * 16384, 2048);
    LBAR();
    load_w2(5, L_W2A); load_w2(6, L_W2A + 16384);
    if (w == 0) flc = ld32_sc1(ws + WS_FLC, flc_off);
    load_w2(7, L_W2A + 2 * 16384); load_w2(8, L_W2A + 3 * 16384); load_w2(9, L_W2A + 4 * 16384);
  }
  STAMP(22);
  {
    auto poll_flc = [&](int h) {
      gu32* f = (gu32*)(ws + WS_FLC + h * 16384 + flc_off);
#pragma unroll 1
      for (unsigned spins = 0; spins < SPIN_CAP; ++spins) {
        unsigned a = __hip_atomic_load(f, RLX_AGENT);
        if (__all(a == epoch)) break;
        __builtin_amdgcn_s_sleep(1);
      }
      __builtin_amdgcn_fence(__ATOMIC_ACQUIRE, "wavefront");
    };
    if (w == 0) {
      WAIT_V(6);
      asm volatile("" : "+v"(flc));
      if (!__all(flc == epoch)) poll_flc(0);
    }
    LBAR();
    STAMP(12);
    unsigned hvo[2];
#pragma unroll
    for (int u = 0; u < 2; ++u) {
      const int r = dh * 32 + u * 16 + (lane >> 2);
      hvo[u] = r * 64 + (((lane & 3) ^ (2 * ((r >> 2) & 1))) << 4);
    }
    int boff[2];
#pragma unroll
    for (int i = 0; i < 2; ++i) boff[i] = kq * 4096 + rl * 64 + ((i ^ ((rl >> 2) & 1)) << 5) + 8 * pp;
    auto load_h1 = [&](int h, int r) {
      const char* hsrc = ws + WS_H1B + ((size_t)(h * 32 + mt) * F_DIM + kq * 11 * 32) * 64;
#pragma unroll
      for (int u = 0; u < 2; ++u) dma16<AUX_SC1>(hsrc + r * 4096, hvo[u], lb + (r & 3) * 16384 + kq * 4096 + (dh * 2 + u) * 1024);
    };
    load_h1(0, 0); load_h1(0, 1); load_h1(0, 2);
    load_w2(10, L_W2X);
    static_for<2>([&](auto HH) {
      constexpr int h = decltype(HH)::value;
      f32x4 acc[2][2];
#pragma unroll
      for (int i = 0; i < 2; ++i)
#pragma unroll
        for (int jm = 0; jm < 2; ++jm) acc[i][jm] = (f32x4){0, 0, 0, 0};
      bf16x8_t B[2][4];
      auto step2 = [&](const bf16x8_t (&Wf)[2], const bf16x8_t (&Bf)[4], int u) {
#pragma unroll
        for (int jm = 0; jm < 2; ++jm)
#pragma unroll
          for (int i = 0; i < 2; ++i) acc[i][jm] = mfma16(Wf[i], Bf[2 * u + jm], acc[i][jm]);
      };
#define G2_ITER(r, N)                                                                                                   \
      WAIT_V(N); LBAR();                                                                                                \
      if ((r) == 0) {                                                                                                   \
        if (h == 0) { if (c_on && t == 0) __hip_atomic_store((gu32*)(ws + WS_FLC + 16384) + b * FLAG_STRIDE, epoch, RLX_AGENT); STAMP(24); } \
        else STAMP(19);                                                                                                 \
      }                                                                                                                 \
      _Pragma("unroll") for (int u = 0; u < 2; ++u)                                                                     \
        _Pragma("unroll") for (int i = 0; i < 2; ++i) B[(r) & 1][2 * u + i] = tr_frag(sm + boff[i] + ((r) & 3) * 16384 + u * 2048, 1024); \
      if ((r) == 0 && h == 0) {                                                                                         \
        _Pragma("unroll") for (int s = 5; s < 10; ++s)                                                                  \
          _Pragma("unroll") for (int i = 0; i < 2; ++i) W2[s][i] = tr_frag(sm + L_W2A + woff[i] + (s - 5) * 16384, 2048); \
      }                                                                                                                 \
      if ((r) == 3 && h == 0) { _Pragma("unroll") for (int i = 0; i < 2; ++i) W2[10][i] = tr_frag(sm + L_W2X + woff[i], 2048); } \
      if ((r) > 0) { step2(W2[(r) > 0 ? 2 * (r) - 2 : 0], B[((r) - 1) & 1], 0);                                         \
        _Pragma("unroll") for (int i = 0; i < 4; ++i) { __builtin_amdgcn_sched_group_barrier(0x8, 1, 0); __builtin_amdgcn_sched_group_barrier(0x100, 2, 0); } } \
      SCHED();                                                                                                          \
      if ((r) + 3 <= 5) load_h1(h, (r) + 3);                                                                            \
      if ((r) == 1 && h == 0) {                                                                                         \
                          \
        const int rr_ = (w & 3) * 16 + (lane >> 2);                                                                     \
        dma16<AUX_SC1>(ws + WS_HT + ((size_t)(d0 + (w & 3) * 16) * S_LEN + (w >> 2) * 1024 + mt * 32) * 2,              \
                       (lane >> 2) * (S_LEN * 2) + (((lane & 3) ^ (2 * ((rr_ >> 2) & 1))) << 4), lb + L_RES + w * 1024); \
      }                                                                                                                 \
      if ((r) == 5 && h == 0) { if (w == 0) flc = ld32_sc1(ws + WS_FLC + 16384, flc_off); }                             \
      SCHED();                                                                                                          \
      if ((r) > 0) step2(W2[(r) > 0 ? 2 * (r) - 1 : 0], B[((r) - 1) & 1], 1);                                           \
      SCHED();
      if constexpr (h == 0) {
        G2_ITER(0, 6) G2_ITER(1, 6) G2_ITER(2, 7) G2_ITER(3, 5) G2_ITER(4, 3) G2_ITER(5, 0)
      } else {
        G2_ITER(0, 4) G2_ITER(1, 4) G2_ITER(2, 4) G2_ITER(3, 4) G2_ITER(4, 2) G2_ITER(5, 0)
      }
#undef G2_ITER
      if (kq < 3) step2(W2[10], B[1], 0);
      if (h == 0) STAMP(13); else STAMP(20);
      const int ds = dh * 2 + (kq >> 1), ms = kq & 1;
      const int rr = ds * 16 + rl;
      const u32x2 ru = __builtin_bit_cast(u32x2, tr_read(sm + L_RES + h * 4096 + rr * 64 + ((ms ^ ((rl >> 2) & 1)) << 5) + 8 * pp));
#pragma unroll
      for (int i = 0; i < 2; ++i)
#pragma unroll
        for (int jm = 0; jm < 2; ++jm) *(LAS f32x4*)(sm + L_EX2 + ((dh * 4 + i * 2 + jm) * 4 + kq) * 1024 + lane * 16) = acc[i][jm];
      if constexpr (h == 0) {
        if (w == 0) {
          WAIT_V(0);
          asm volatile("" : "+v"(flc));
          if (!__all(flc == epoch)) poll_flc(1);
        }
      }
      LBAR();
      if constexpr (h == 0) { load_h1(1, 0); load_h1(1, 1); load_h1(1, 2); STAMP(18); }
      const LAS char* e = sm + L_EX2 + ((dh * 4 + (kq >> 1) * 2 + ms) * 4) * 1024 + lane * 16;
      const f32x4 a = (*(const LAS f32x4*)(e) + *(const LAS f32x4*)(e + 1024)) + (*(const LAS f32x4*)(e + 2048) + *(const LAS f32x4*)(e + 3072));
      const u32x2 ov = {pk_bf16(bf_lo(ru[0]) + rbf(a[0]), bf_hi(ru[0]) + rbf(a[1])), pk_bf16(bf_lo(ru[1]) + rbf(a[2]), bf_hi(ru[1]) + rbf(a[3]))};
      *(u32x2*)(p.out + (size_t)(h * 1024 + mt * 32 + ms * 16 + fr) * D_DIM + d0 + ds * 16 + g * 4) = ov;
      if (h == 0) STAMP(14);
    });
  }
  asm volatile(".Lh3_code_end:");
  STAMP(23); CSTAMP(7);
}

extern "C" void kernel_launch(void* const* d_in, const int* in_sizes, int n_in, void* d_out, int out_size, void* d_ws, size_t ws_size,
                              hipStream_t stream) {
  Params p;
  p.q = (const u16*)d_in[0];
  p.k = (const u16*)d_in[1];
  p.v = (const u16*)d_in[2];
  p.shk = (const u16*)d_in[3];
  p.ssmk = (const u16*)d_in[4];
  p.ssmD = (const u16*)d_in[5];
  p.w1 = (const u16*)d_in[7];
  p.w2 = (const u16*)d_in[8];
  p.out = (u16*)d_out;
  p.ws = (char*)d_ws;
  (void)hipFuncSetAttribute((const void*)h3_fused, hipFuncAttributeMaxDynamicSharedMemorySize, LDS_BYTES);
  h3_fused<<<256, 512, LDS_BYTES, stream>>>(p);
}
```

```cpp
#include <hip/hip_runtime.h>
#include <hip/hip_bf16.h>
#include <stdint.h>
#include <type_traits>
#include <utility>

typedef unsigned short u16;
typedef short bf16x8 __attribute__((ext_vector_type(8)));
typedef short s16x4 __attribute__((ext_vector_type(4)));
typedef float f32x4 __attribute__((ext_vector_type(4)));
typedef float f32x2 __attribute__((ext_vector_type(2)));
typedef unsigned u32x4 __attribute__((ext_vector_type(4)));
typedef unsigned u32x2 __attribute__((ext_vector_type(2)));
typedef __bf16 bf16x2_t __attribute__((ext_vector_type(2)));
typedef __bf16 bf16x8_t __attribute__((ext_vector_type(8)));
typedef short v4i16_t __attribute__((ext_vector_type(4)));
#define LAS __attribute__((address_space(3)))
#define GAS __attribute__((address_space(1)))
typedef GAS unsigned gu32;
#define RLX_AGENT __ATOMIC_RELAXED, __HIP_MEMORY_SCOPE_AGENT

constexpr int S_LEN = 2048, D_DIM = 512, F_DIM = 1376;
constexpr int AUX_SC1 = 16;

constexpr unsigned WS_KVT = 0, WS_QT = 2u << 20, WS_HT = 4u << 20, WS_H1T = 6u << 20, WS_STQ = 12u << 20;
constexpr unsigned WS_FL1 = WS_STQ + 65536, WS_FL2 = WS_FL1 + 32768, WS_FL3 = WS_FL2 + 32768, WS_EPOCH = WS_FL3 + 32768;
constexpr unsigned WS_STAMP = WS_EPOCH + 4096;
constexpr unsigned WS_BYTES = 16u << 20;
constexpr int FLAG_STRIDE = 16;
constexpr unsigned SPIN_CAP = 1u << 18;

#define WAIT_V(n) asm volatile("s_waitcnt vmcnt(%0)" ::"n"(n) : "memory")
#define WAIT_L0() asm volatile("s_waitcnt lgkmcnt(0)" ::: "memory")
#define RAW_BAR() do { asm volatile("" ::: "memory"); __builtin_amdgcn_s_barrier(); asm volatile("" ::: "memory"); } while (0)
#define LBAR() do { WAIT_L0(); RAW_BAR(); } while (0)
#define SCHED() __builtin_amdgcn_sched_barrier(0)
template <int N> __device__ __forceinline__ void interleave_mfma_ds() {
#pragma unroll
  for (int i = 0; i < N; ++i) { __builtin_amdgcn_sched_group_barrier(0x8, 1, 0); __builtin_amdgcn_sched_group_barrier(0x100, 1, 0); }
}

#define STAMP(i)
#define CSTAMP(i)

__device__ __forceinline__ float bf_lo(unsigned x) { return __uint_as_float(x << 16); }
__device__ __forceinline__ float bf_hi(unsigned x) { return __uint_as_float(x & 0xffff0000u); }
__device__ __forceinline__ unsigned pk_bf16(float lo, float hi) {
  bf16x2_t v; v.x = (__bf16)lo; v.y = (__bf16)hi;
  return __builtin_bit_cast(unsigned, v);
}
__device__ __forceinline__ float rbf(float x) { return __uint_as_float(((unsigned)__builtin_bit_cast(u16, (__bf16)x)) << 16); }
__device__ __forceinline__ s16x4 tr_read(const LAS char* p) {
  return __builtin_bit_cast(s16x4, __builtin_amdgcn_ds_read_tr16_b64_v4i16((LAS v4i16_t*)p));
}
__device__ __forceinline__ bf16x8_t cat8(s16x4 a, s16x4 b) {
  bf16x8 r = __builtin_shufflevector(a, b, 0, 1, 2, 3, 4, 5, 6, 7);
  return __builtin_bit_cast(bf16x8_t, r);
}
__device__ __forceinline__ bf16x8_t tr_frag(const LAS char* p, int second) { return cat8(tr_read(p), tr_read(p + second)); }
__device__ __forceinline__ f32x4 mfma16(bf16x8_t a, bf16x8_t b, f32x4 c) {
  return __builtin_amdgcn_mfma_f32_16x16x32_bf16(a, b, c, 0, 0, 0);
}
template <int AUX>
__device__ __forceinline__ void dma16(const void* sbase, unsigned voff, unsigned lds) {
  if constexpr (AUX == AUX_SC1)
    asm volatile("s_mov_b32 m0, %2\n\ts_nop 0\n\tglobal_load_lds_dwordx4 %0, %1 sc1" ::"v"(voff), "s"(sbase), "s"(lds) : "memory");
  else
    asm volatile("s_mov_b32 m0, %2\n\ts_nop 0\n\tglobal_load_lds_dwordx4 %0, %1" ::"v"(voff), "s"(sbase), "s"(lds) : "memory");
}
__device__ __forceinline__ void dma4(const void* sbase, unsigned voff, unsigned lds) {
  asm volatile("s_mov_b32 m0, %2\n\ts_nop 0\n\tglobal_load_lds_dword %0, %1" ::"v"(voff), "s"(sbase), "s"(lds) : "memory");
}
__device__ __forceinline__ void dma4v(const void* vaddr, unsigned lds) {
  asm volatile("s_mov_b32 m0, %1\n\ts_nop 0\n\tglobal_load_lds_dword %0, off" ::"v"(vaddr), "s"(lds) : "memory");
}
__device__ __forceinline__ u32x4 ld128_asm(const void* sbase, unsigned voff) {
  u32x4 r; asm volatile("global_load_dwordx4 %0, %1, %2" : "=v"(r) : "v"(voff), "s"(sbase) : "memory"); return r;
}
__device__ __forceinline__ u32x4 ld128_sc1(const void* sbase, unsigned voff) {
  u32x4 r; asm volatile("global_load_dwordx4 %0, %1, %2 sc1" : "=v"(r) : "v"(voff), "s"(sbase) : "memory"); return r;
}
__device__ __forceinline__ unsigned ld32_sc1(const void* sbase, unsigned voff) {
  unsigned r; asm volatile("global_load_dword %0, %1, %2 sc1" : "=v"(r) : "v"(voff), "s"(sbase) : "memory"); return r;
}
__device__ __forceinline__ unsigned ld16_asm(const void* sbase, unsigned voff) {
  unsigned r; asm volatile("global_load_ushort %0, %1, %2" : "=v"(r) : "v"(voff), "s"(sbase) : "memory"); return r;
}
__device__ __forceinline__ u32x2 ld64_asm(const void* sbase, unsigned voff) {
  u32x2 r; asm volatile("global_load_dwordx2 %0, %1, %2" : "=v"(r) : "v"(voff), "s"(sbase) : "memory"); return r;
}
__device__ __forceinline__ u32x2 ld64_sc1(const void* sbase, unsigned voff) {
  u32x2 r; asm volatile("global_load_dwordx2 %0, %1, %2 sc1" : "=v"(r) : "v"(voff), "s"(sbase) : "memory"); return r;
}
template <int CTRL>
__device__ __forceinline__ float dpp_add(float x) {
  return x + __builtin_bit_cast(float, __builtin_amdgcn_mov_dpp(__builtin_bit_cast(int, x), CTRL, 0xf, 0xf, true));
}
__device__ __forceinline__ float wave_sum(float x) {
  x = dpp_add<0xB1>(x); x = dpp_add<0x4E>(x); x = dpp_add<0x141>(x); x = dpp_add<0x140>(x);
  auto s = __builtin_amdgcn_permlane16_swap(__float_as_uint(x), __float_as_uint(x), false, false);
  x = __uint_as_float(s[0]) + __uint_as_float(s[1]);
  auto u = __builtin_amdgcn_permlane32_swap(__float_as_uint(x), __float_as_uint(x), false, false);
  return __uint_as_float(u[0]) + __uint_as_float(u[1]);
}
__device__ __forceinline__ u32x4 widen16(u32x2 X, u32x2 Y) {
  auto a = __builtin_amdgcn_permlane16_swap(X[0], Y[0], false, false);
  auto b = __builtin_amdgcn_permlane16_swap(X[1], Y[1], false, false);
  return (u32x4){a[0], b[0], a[1], b[1]};
}

struct Params {
  const u16 *q, *k, *v, *shk, *ssmk, *ssmD, *w1, *w2;
  u16* out;
  char* ws;
};

__device__ __forceinline__ void wait_flags256(const char* ws, unsigned off, unsigned epoch, int lane) {
  gu32* f = (gu32*)(ws + off) + lane * FLAG_STRIDE;
#pragma unroll 1
  for (unsigned spins = 0; spins < SPIN_CAP; ++spins) {
    unsigned a = __hip_atomic_load(f, RLX_AGENT), b = __hip_atomic_load(f + 64 * FLAG_STRIDE, RLX_AGENT);
    unsigned c = __hip_atomic_load(f + 128 * FLAG_STRIDE, RLX_AGENT), d = __hip_atomic_load(f + 192 * FLAG_STRIDE, RLX_AGENT);
    if (__all((a == epoch) & (b == epoch) & (c == epoch) & (d == epoch))) break;
    __builtin_amdgcn_s_sleep(1);
  }
  __builtin_amdgcn_fence(__ATOMIC_ACQUIRE, "wavefront");
}

constexpr int L_KT = 32768, L_VT = L_KT + 72 * 128, L_QT = L_VT + 8192, L_OKV = L_QT + 8192, L_OQ = L_OKV + 8192, L_SS = L_OQ + 8192;
constexpr int H_ROW = 1152, H_COPY = 4728, H_BYTES = 9344, X_BYTES = 4160;
constexpr int L_H = 0, L_X = L_H + 2 * H_BYTES, L_QD = L_X + 2 * X_BYTES;
constexpr int CY_BYTES = 513 * 16;
constexpr int L_CY = L_QD + 16384;
constexpr int L_JUNKA = 76800;
constexpr int L_W1 = 77824;
constexpr int L_W1B = 0;
static_assert(L_CY + 4 * CY_BYTES <= L_JUNKA && L_JUNKA + 1024 <= L_W1 && L_W1 + 7 * 12288 <= 163840 && L_SS + 64 <= L_JUNKA, "lds");
static_assert(L_X + 2 * X_BYTES <= L_KT, "lds A/B overlap");
constexpr int L_W2A = 4 * 16384;
constexpr int L_CS = 147456;
constexpr int L_SST = L_CS + 1024;
constexpr int L_FST = L_SST + 2048;
constexpr int L_OUT1B = L_FST + 1024;
constexpr int L_JUNKC = L_OUT1B + 8192;
constexpr int L_EX1 = 0;
constexpr int L_OUT1A = 49152;
static_assert(L_JUNKC + 1024 <= 163840, "lds");
constexpr int L_RES = 155648;
constexpr int L_RING = 0;
constexpr int L_JUNKD = 147456;
constexpr int L_EX2 = 0;
constexpr int LDS_BYTES = 163840;

template <class F, int... I>
__device__ __forceinline__ void static_for_impl(F&& f, std::integer_sequence<int, I...>) { (f(std::integral_constant<int, I>{}), ...); }
template <int N, class F>
__device__ __forceinline__ void static_for(F&& f) { static_for_impl(f, std::make_integer_sequence<int, N>{}); }

template <int TS, int PAR, class F>
__device__ __forceinline__ void conv_main(const LAS char* xa, const LAS char* hb, f32x4 (&acc)[4][4], F&& job) {
  constexpr int U[4] = {TS == 0 ? 0 : 1, TS == 0 ? 3 : 2, TS == 0 ? 4 : 5, TS == 0 ? 7 : 6};
  constexpr int T[4] = {U[PAR ? 2 : 0], U[PAR ? 3 : 1], U[PAR ? 0 : 2], U[PAR ? 1 : 3]};
  constexpr int NA = U[3] + 1;
  bf16x8_t A[8], B[3][4];
#pragma unroll
  for (int p = 0; p < 4; ++p) B[0][p] = *(const LAS bf16x8_t*)(hb + p * H_ROW + 896);
#pragma unroll
  for (int a = NA - 1; a >= 0; --a) A[a] = tr_frag(xa + 512 * a, 32);
#pragma unroll
  for (int p = 0; p < 4; ++p) B[1][p] = *(const LAS bf16x8_t*)(hb + p * H_ROW + 896 - 128);
  SCHED();
  static_for<NA>([&](auto BB) {
    constexpr int bb = decltype(BB)::value;
    if (bb + 2 < NA) {
#pragma unroll
      for (int p = 0; p < 4; ++p) B[(bb + 2) % 3][p] = *(const LAS bf16x8_t*)(hb + p * H_ROW + 896 - 128 * (bb + 2));
    }
#pragma unroll
    for (int p = 0; p < 4; ++p)
#pragma unroll
      for (int j = 3; j >= 0; --j)
        if (T[j] >= bb) acc[j][p] = mfma16(A[T[j] - bb], B[bb % 3][p], acc[j][p]);
    if (bb + 2 < NA) interleave_mfma_ds<4>();
    SCHED();
    if constexpr (bb < 5) { job(std::integral_constant<int, 2 * bb>{}); job(std::integral_constant<int, 2 * bb + 1>{}); }
    SCHED();
  });
}

__global__ __launch_bounds__(512, 2) void h3_fused(Params p) {
  extern __shared__ __attribute__((aligned(16))) char smem_[];
  LAS char* sm = (LAS char*)smem_;
  const unsigned lb = (unsigned)(size_t)sm;
  const int b = blockIdx.x, t = threadIdx.x, lane = t & 63;
  const int w = __builtin_amdgcn_readfirstlane(t >> 6);
  const int x = b & 7, jb = b >> 3;
  const int g = lane >> 4, fr = lane & 15, q_ = fr >> 2, pp = lane & 3;
  const int rl = 4 * g + q_;
  char* ws = p.ws;
  const __amdgpu_buffer_rsrc_t rs = __builtin_amdgcn_make_buffer_rsrc(ws, 0, WS_BYTES, 0x00020000);
  STAMP(0); CSTAMP(0);
  const unsigned long long dbg_t0 = __builtin_amdgcn_s_memrealtime();

  const int dB = x * 64 + jb * 2;
  const int cb = w & 3, sl = 8 * (4 * (w >> 2) + g);
  const unsigned tvo = (lane >> 3) * (D_DIM * 2) + (lane & 7) * 16;
  const size_t tso = ((size_t)(jb * 64 + w * 8) * D_DIM + x * 64);
  dma16<0>(p.k + tso, tvo, lb + L_KT + 1024 + w * 1024);
  dma16<0>(p.v + tso, tvo, lb + L_VT + w * 1024);
  if (w == 0) {
    if (jb > 0) dma16<0>(p.k + tso - 8 * D_DIM, tvo, lb + L_KT);
    else *(LAS u32x4*)(sm + L_KT + lane * 16) = (u32x4){0, 0, 0, 0};
  }
  u32x2 wv = ld64_asm(p.shk + (size_t)(x * 64 + cb * 16) * 4, fr * 8);
  STAMP(32);
  const bool c_on = jb < 30;
  const int panel = x * 2 + (jb >= 15 ? 1 : 0), ct = jb >= 15 ? jb - 15 : jb;
  const int m0 = panel * 128, n0 = ct * 96;
  unsigned w1o[6];
#pragma unroll
  for (int u = 0; u < 6; ++u) {
    const int idx = ((w & 1) * 6 + u) * 64 + lane, r = (idx * 43691) >> 19, cp = idx - r * 12;
    int col = n0 + (cp ^ (2 * ((r >> 2) & 1))) * 8;
    col = col > F_DIM - 8 ? F_DIM - 8 : col;
    w1o[u] = (r * F_DIM + col) * 2;
  }
  auto w1_half = [&](int chunk, int ldst, int step) {
#pragma unroll
    for (int u = 0; u < 6; ++u) dma16<0>(p.w1 + (size_t)chunk * 64 * F_DIM, w1o[u], lb + ldst + ((w & 1) * 6 + u) * step);
  };
  STAMP(33);
  WAIT_V(0);
  asm volatile("" : "+v"(wv));
  dma16<0>(p.q + tso, tvo, lb + L_QT + w * 1024);
  u32x4 sq = ld128_asm(p.q + (size_t)(b * 8 + w) * D_DIM, lane * 16);
  unsigned ep0 = ld32_sc1(ws + WS_EPOCH, 0);
  unsigned dv0 = ld16_asm(p.ssmD + dB + (w >> 2), 0);
  if (w == 7) {
    unsigned long long pc; unsigned len;
    asm volatile("s_getpc_b64 %0\n.Lh3_ref_%=:\n\ts_mov_b32 %1, .Lh3_code_end-.Lh3_ref_%=" : "=s"(pc), "=s"(len));
    const unsigned nl = len >> 7;
    unsigned line = (unsigned)(jb * 8 + lane); line = line >= nl ? line - nl : line; line = line >= nl ? 0 : line;
    dma4((const void*)pc, line << 7, lb + L_JUNKA);
  }
  if (w == 6) {
    const char* a = ws + (size_t)(jb * 8 + (lane & 7)) * 65536;
    if (lane >= 8) a = (const char*)p.w1 + jb * 44032;
    if (lane >= 16) a = (const char*)p.w2 + jb * 44032;
    if (lane >= 24) a = (const char*)p.out + jb * 65536;
    if (lane >= 32) a = (const char*)p.ssmk + jb * 65536;
    dma4v(a, lb + L_JUNKA + 256);
  }
  LBAR();
  STAMP(1);
  const int tro = q_ * 128 + cb * 32 + 8 * pp + sl * 128;
  const int oo = (cb * 16 + fr) * 128 + (((sl >> 3) ^ (fr & 7)) << 4);
  {
    const u32x2 k0 = __builtin_bit_cast(u32x2, tr_read(sm + L_KT + 4 * 128 + tro));
    const u32x2 k1 = __builtin_bit_cast(u32x2, tr_read(sm + L_KT + 8 * 128 + tro));
    const u32x2 k2 = __builtin_bit_cast(u32x2, tr_read(sm + L_KT + 12 * 128 + tro));
    const u32x2 v0 = __builtin_bit_cast(u32x2, tr_read(sm + L_VT + tro));
    const u32x2 v1 = __builtin_bit_cast(u32x2, tr_read(sm + L_VT + 4 * 128 + tro));
    const float w0 = bf_lo(wv[0]), w1 = bf_hi(wv[0]), w2 = bf_lo(wv[1]), w3 = bf_hi(wv[1]);
    const float kx[11] = {bf_hi(k0[0]), bf_lo(k0[1]), bf_hi(k0[1]), bf_lo(k1[0]), bf_hi(k1[0]), bf_lo(k1[1]), bf_hi(k1[1]),
                          bf_lo(k2[0]), bf_hi(k2[0]), bf_lo(k2[1]), bf_hi(k2[1])};
    const float vx[8] = {bf_lo(v0[0]), bf_hi(v0[0]), bf_lo(v0[1]), bf_hi(v0[1]), bf_lo(v1[0]), bf_hi(v1[0]), bf_lo(v1[1]), bf_hi(v1[1])};
    float o[8];
#pragma unroll
    for (int e = 0; e < 8; ++e) o[e] = (kx[e] * w3 + kx[e + 1] * w2 + kx[e + 2] * w1 + kx[e + 3] * w0) * vx[e];
    *(LAS u32x4*)(sm + L_OKV + oo) = (u32x4){pk_bf16(o[0], o[1]), pk_bf16(o[2], o[3]), pk_bf16(o[4], o[5]), pk_bf16(o[6], o[7])};
  }
  LBAR();
  STAMP(15);
  const int toff = (t >> 3) * 128 + (((t & 7) ^ ((t >> 3) & 7)) << 4);
  const unsigned tgo = ((x * 64 + (t >> 3)) * S_LEN + jb * 64 + (t & 7) * 8) * 2;
  __builtin_amdgcn_raw_buffer_store_b128(*(const LAS u32x4*)(sm + L_OKV + toff), rs, WS_KVT + tgo, 0, AUX_SC1);
  WAIT_V(1);
  asm volatile("" : "+v"(sq), "+v"(ep0), "+v"(dv0));
  RAW_BAR();
  const unsigned epoch = (unsigned)__builtin_amdgcn_readfirstlane(ep0) + 1u;
  const float Dv = bf_lo(dv0);
  {
    const u32x2 q0 = __builtin_bit_cast(u32x2, tr_read(sm + L_QT + tro));
    const u32x2 q1 = __builtin_bit_cast(u32x2, tr_read(sm + L_QT + 4 * 128 + tro));
    f32x2 xs[4];
#pragma unroll
    for (int i = 0; i < 4; ++i) xs[i] = (f32x2){bf_lo(sq[i]), bf_hi(sq[i])};
    const f32x2 s2 = (xs[0] + xs[1]) + (xs[2] + xs[3]);
    const float mean = wave_sum(s2[0] + s2[1]) * (1.0f / D_DIM);
    *(LAS u32x4*)(sm + L_OQ + oo) = (u32x4){q0[0], q0[1], q1[0], q1[1]};
    f32x2 vs = {0.f, 0.f};
#pragma unroll
    for (int i = 0; i < 4; ++i) { const f32x2 dd = xs[i] - mean; vs += dd * dd; }
    const float var = rbf(wave_sum(vs[0] + vs[1]) * (1.0f / D_DIM));
    if (lane == 0) *(LAS unsigned*)(sm + L_SS + w * 4) = pk_bf16(mean, __builtin_amdgcn_sqrtf(rbf(var + 1e-5f)));
  }
  LBAR();
  __builtin_amdgcn_raw_buffer_store_b128(*(const LAS u32x4*)(sm + L_OQ + toff), rs, WS_QT + tgo, 0, AUX_SC1);
  if (t < 2) __builtin_amdgcn_raw_buffer_store_b128(*(const LAS u32x4*)(sm + L_SS + t * 16), rs, WS_STQ + b * 32 + t * 16, 0, AUX_SC1);
  STAMP(31);
  WAIT_V(0);
  LBAR();
  if (t == 0) __hip_atomic_store((gu32*)(ws + WS_FL1) + b * FLAG_STRIDE, epoch, RLX_AGENT);
  RAW_BAR();
  STAMP(2);
  {
    const int hc = t >> 8, hw = t & 255;
    u32x4 hv = ld128_asm(p.ssmk + (size_t)dB * S_LEN, (hc * S_LEN + hw * 8) * 2);
    if (w < 4) { w1_half(w >> 1, L_W1 + (w >> 1) * 12288, 1024); WAIT_V(6); } else { WAIT_V(0); }
    asm volatile("" : "+v"(hv));
    LAS char* hb = sm + L_H + hc * H_BYTES + 1020 - 4 * hw;
    const unsigned h0 = __builtin_amdgcn_perm(hv[1], hv[3], 0x07060302u), h1 = __builtin_amdgcn_perm(hv[1], hv[3], 0x05040100u);
    const unsigned h2 = __builtin_amdgcn_perm(hv[0], hv[2], 0x07060302u), h3 = __builtin_amdgcn_perm(hv[0], hv[2], 0x05040100u);
    *(LAS unsigned*)(hb + 0 * H_ROW) = h0; *(LAS unsigned*)(hb + H_COPY + 0 * H_ROW) = h0;
    *(LAS unsigned*)(hb + 1 * H_ROW) = h1; *(LAS unsigned*)(hb + H_COPY + 1 * H_ROW) = h1;
    *(LAS unsigned*)(hb + 2 * H_ROW) = h2; *(LAS unsigned*)(hb + H_COPY + 2 * H_ROW) = h2;
    *(LAS unsigned*)(hb + 3 * H_ROW) = h3; *(LAS unsigned*)(hb + H_COPY + 3 * H_ROW) = h3;
    if (t < 256) {
      const int r = t >> 4;
      *(LAS u32x2*)(sm + L_H + (r >> 3) * H_BYTES + ((r >> 2) & 1) * H_COPY + (r & 3) * H_ROW + 1024 + (t & 15) * 8) = (u32x2){0, 0};
    } else if (t < 260) *(LAS u32x4*)(sm + L_X + ((t - 256) >> 1) * X_BYTES + (t & 1) * 16) = (u32x4){0, 0, 0, 0};
    else if (t < 264) *(LAS u32x4*)(sm + L_CY + (t - 260) * CY_BYTES) = (u32x4){0, 0, 0, 0};
  }

  const int c2 = w >> 2, ts = (w >> 1) & 1, par = w & 1;
  const int tl0 = ts == 0 ? 0 : 1, tl1 = ts == 0 ? 3 : 2, tl2 = ts == 0 ? 4 : 5, tl3 = ts == 0 ? 7 : 6;
  const int tf0 = par ? tl2 : tl0, tf1 = par ? tl3 : tl1, tg0 = par ? tl0 : tl2, tg1 = par ? tl1 : tl3;
  const int sa = tf0 * 256 + fr * 16 + g * 4, sb = tf1 * 256 + fr * 16 + g * 4;
  if (w == 0) wait_flags256(ws, WS_FL1, epoch, lane);
  LBAR();
  STAMP(3);
  {
    const int d = dB + c2;
    dma16<AUX_SC1>(ws + WS_KVT + ((size_t)d * S_LEN + (w & 3) * 512) * 2, lane * 16, lb + L_X + c2 * X_BYTES + 32 + (w & 3) * 1024);
    u32x2 qv0 = ld64_sc1(ws + WS_QT + (size_t)d * S_LEN * 2, sa * 2), qv1 = ld64_sc1(ws + WS_QT + (size_t)d * S_LEN * 2, sb * 2);
    u32x4 sv0 = ld128_sc1(ws + WS_STQ, sa * 4), sv1 = ld128_sc1(ws + WS_STQ, sb * 4);
    WAIT_V(4);
    RAW_BAR();
    STAMP(4); CSTAMP(1);
    f32x4 acc[4][4];
#pragma unroll
    for (int j = 0; j < 4; ++j)
#pragma unroll
      for (int pz = 0; pz < 4; ++pz) acc[j][pz] = (f32x4){0, 0, 0, 0};
    const LAS char* xa = sm + L_X + c2 * X_BYTES + 8 + 64 * g + 8 * q_ + 8 * pp + 256 * par;
    const LAS char* hb = sm + L_H + c2 * H_BYTES + ((fr & 1) ? 0 : H_COPY) + 1016 - 8 * fr + 16 * g + 64 * par - 896;
    auto job = [&](auto J) {
      constexpr int j = decltype(J)::value;
      if constexpr (j == 0) {
        int r2 = ((b * 43) >> 3) + w; r2 = r2 > F_DIM - 1 ? F_DIM - 1 : r2;
        dma16<0>(p.w2 + (size_t)r2 * D_DIM, lane * 16, lb + L_JUNKA);
      } else if constexpr (j < 7) {
        const int ch = 2 + (w >> 1);
        dma16<0>(p.w1 + (size_t)ch * 64 * F_DIM, w1o[j - 1], lb + L_W1 + ch * 12288 + ((w & 1) * 6 + j - 1) * 1024);
      } else {
        if (w < 4) {
          const int u = (w >> 1) * 3 + (j - 7);
          dma16<0>(p.w1 + (size_t)6 * 64 * F_DIM, (w & 2) ? w1o[3 + j - 7] : w1o[j - 7], lb + L_W1 + 6 * 12288 + ((w & 1) * 6 + u) * 1024);
        }
      }
    };
    if (ts == 0) { if (par == 0) conv_main<0, 0>(xa, hb, acc, job); else conv_main<0, 1>(xa, hb, acc, job); }
    else         { if (par == 0) conv_main<1, 0>(xa, hb, acc, job); else conv_main<1, 1>(xa, hb, acc, job); }
    STAMP(5); CSTAMP(2);
#pragma unroll
    for (int j = 0; j < 4; ++j) {
      acc[j][3][1] += acc[j][2][0];
      acc[j][3][2] += acc[j][2][1] + acc[j][1][0];
      acc[j][3][3] += (acc[j][2][2] + acc[j][1][1]) + acc[j][0][0];
      acc[j][0][1] += acc[j][2][3] + acc[j][1][2];
      acc[j][0][2] += acc[j][1][3];
    }
    LAS char* cy = sm + L_CY + (c2 * 2 + par) * CY_BYTES + 16 + lane * 16;
    *(LAS f32x4*)(cy + tf0 * 1024) = acc[0][0];
    *(LAS f32x4*)(cy + tf1 * 1024) = acc[1][0];
    *(LAS f32x4*)(cy + tg0 * 1024) = acc[2][0];
    *(LAS f32x4*)(cy + tg1 * 1024) = acc[3][0];
    LAS char* qd = sm + L_QD + w * 2048 + lane * 16;
    *(LAS f32x4*)(qd) = acc[2][3]; *(LAS f32x4*)(qd + 1024) = acc[3][3];
    const u32x2 xv0 = *(const LAS u32x2*)(sm + L_X + c2 * X_BYTES + 32 + sa * 2), xv1 = *(const LAS u32x2*)(sm + L_X + c2 * X_BYTES + 32 + sb * 2);
    LBAR();
    STAMP(29);
    const int prev = g > 0 ? lane - 16 : (fr > 0 ? 47 + fr : -1);
    const LAS char* cy0 = sm + L_CY + (c2 * 2) * CY_BYTES + 16 + prev * 16;
    const LAS char* qs = sm + L_QD + (w ^ 1) * 2048 + lane * 16;
    WAIT_V(7);
    asm volatile("" : "+v"(qv0), "+v"(qv1), "+v"(sv0), "+v"(sv1));
    u32x2 ov[2];
#pragma unroll
    for (int i = 0; i < 2; ++i) {
      const int tf = i ? tf1 : tf0;
      const f32x4 pq = *(const LAS f32x4*)(qs + i * 1024);
      const f32x4 ca = *(const LAS f32x4*)(cy0 + tf * 1024), cb_ = *(const LAS f32x4*)(cy0 + CY_BYTES + tf * 1024);
      f32x4 y = acc[i][3] + pq;
      y[0] += ca[1] + cb_[1]; y[1] += ca[2] + cb_[2]; y[2] += ca[3] + cb_[3];
      const u32x2 xv = i ? xv1 : xv0, qv = i ? qv1 : qv0;
      const u32x4 sv = i ? sv1 : sv0;
      const float xx[4] = {bf_lo(xv[0]), bf_hi(xv[0]), bf_lo(xv[1]), bf_hi(xv[1])};
      const float qx[4] = {bf_lo(qv[0]), bf_hi(qv[0]), bf_lo(qv[1]), bf_hi(qv[1])};
      float o[4];
#pragma unroll
      for (int e = 0; e < 4; ++e) o[e] = qx[e] + ((qx[e] - bf_lo(sv[e])) * __builtin_amdgcn_rcpf(bf_hi(sv[e]))) * (y[e] + xx[e] * Dv);
      ov[i] = (u32x2){pk_bf16(o[0], o[1]), pk_bf16(o[2], o[3])};
    }
    const u32x4 o16 = widen16(ov[0], ov[1]);
    const int so = ((g & 1) ? tf1 : tf0) * 256 + fr * 16 + (g >> 1) * 8;
    __builtin_amdgcn_raw_buffer_store_b128(o16, rs, WS_HT + (d * S_LEN + so) * 2, 0, AUX_SC1);
    STAMP(30);
  }
  WAIT_V(0);
  LBAR();
  if (t == 0) __hip_atomic_store((gu32*)(ws + WS_FL2) + b * FLAG_STRIDE, epoch, RLX_AGENT);
  RAW_BAR();
  STAMP(6);

  const int kp = w >> 2, wm = (w >> 1) & 1, wn = w & 1;
  const int mt = x * 4 + (jb >> 3), dt = jb & 7;
  const int md0 = mt * 64, d0 = dt * 64;
  const int kq = w >> 1, dh = w & 1;
  unsigned wvo[2], hvo[2];
#pragma unroll
  for (int u = 0; u < 2; ++u) {
    const int r = ((w & 1) * 2 + u) * 8 + (lane >> 3), c = ((lane & 7) ^ (2 * ((r >> 1) & 3))) * 16;
    wvo[u] = r * (D_DIM * 2) + c; hvo[u] = r * (S_LEN * 2) + c;
  }
  auto load_w2 = [&](int step, int lds) {
    int ks = kq * 11 + step; ks = ks > 42 ? 42 : ks;
#pragma unroll
    for (int u = 0; u < 2; ++u) dma16<0>(p.w2 + (size_t)ks * 32 * D_DIM + d0, wvo[u], lb + lds + kq * 4096 + ((w & 1) * 2 + u) * 1024);
  };
  const bf16x8_t ONES = __builtin_bit_cast(bf16x8_t, (u32x4){0x3f803f80u, 0x3f803f80u, 0x3f803f80u, 0x3f803f80u});
  if (c_on) {
    bf16x8_t W1[8][3];
    f32x4 csa[3];
    if (w < 2) w1_half(7, L_W1B, 1024);
#pragma unroll
    for (int i = 0; i < 3; ++i) csa[i] = (f32x4){0, 0, 0, 0};
    int wo[3];
#pragma unroll
    for (int i = 0; i < 3; ++i) wo[i] = (kp * 32 + rl) * 192 + 8 * pp + (((wn * 3 + i) ^ ((rl >> 2) & 1)) << 5);
#pragma unroll
    for (int c = 0; c < 7; ++c)
#pragma unroll
      for (int i = 0; i < 3; ++i) {
        W1[c][i] = tr_frag(sm + L_W1 + c * 12288 + wo[i], 16 * 192);
        csa[i] = mfma16(ONES, W1[c][i], csa[i]);
      }
    WAIT_V(0);
    RAW_BAR();
    STAMP(28);
#pragma unroll
    for (int i = 0; i < 3; ++i) {
      W1[7][i] = tr_frag(sm + L_W1B + wo[i], 16 * 192);
      csa[i] = mfma16(ONES, W1[7][i], csa[i]);
    }
    LBAR();
    load_w2(0, L_W2A); load_w2(1, L_W2A + 16384); load_w2(2, L_W2A + 2 * 16384);
    STAMP(7);
    if (w == 0) wait_flags256(ws, WS_FL2, epoch, lane);
    LBAR();
    STAMP(8);
    if (b == 0 && t == 0) __hip_atomic_store((gu32*)(ws + WS_EPOCH), epoch, RLX_AGENT);
    const char* hsrc = ws + WS_HT + ((size_t)(w * 8) * S_LEN + m0) * 2;
    const int ra = lane >> 4, rb = ra + 4;
    const unsigned va = ra * (S_LEN * 2) + (((lane & 15) ^ (2 * ra)) << 4), vb = rb * (S_LEN * 2) + (((lane & 15) ^ (2 * rb)) << 4);
    auto load_ht = [&](int c) {
      dma16<AUX_SC1>(hsrc + (size_t)c * (64 * S_LEN * 2), va, lb + (c & 3) * 16384 + (w * 2) * 1024);
      dma16<AUX_SC1>(hsrc + (size_t)c * (64 * S_LEN * 2), vb, lb + (c & 3) * 16384 + (w * 2 + 1) * 1024);
    };
    load_ht(0); load_ht(1);
    if (wm == 0 && g == 0) {
#pragma unroll
      for (int i = 0; i < 3; ++i) *(LAS float*)(sm + L_CS + (kp * 96 + wn * 48 + i * 16 + fr) * 4) = csa[i][0];
    }
    f32x4 acc[4][3], sta[4];
    int aoff[4];
#pragma unroll
    for (int i = 0; i < 4; ++i) {
      sta[i] = (f32x4){0, 0, 0, 0};
      aoff[i] = (kp * 32 + rl) * 256 + (((wm * 4 + (i ^ (2 * kp))) ^ (rl & 7)) << 5) + 8 * pp;
#pragma unroll
      for (int jn = 0; jn < 3; ++jn) acc[i][jn] = (f32x4){0, 0, 0, 0};
    }
    auto g1_loop = [&](auto WN) {
      bf16x8_t A[2][4];
      auto half = [&](bf16x8_t (&Af)[4], const bf16x8_t (&Wf)[3], int i0) {
#pragma unroll
        for (int i = i0; i < i0 + 2; ++i) {
#pragma unroll
          for (int jn = 0; jn < 3; ++jn) acc[i][jn] = mfma16(Af[i], Wf[jn], acc[i][jn]);
          sta[i] = mfma16(Af[i], decltype(WN)::value ? Af[i] : ONES, sta[i]);
        }
      };
      auto warm_w2 = [&](int i) {
        int ks = kq * 11 + 5 + 2 * i + (w & 1); ks = ks > 42 ? 42 : ks;
        dma16<0>(p.w2 + (size_t)(ks * 32 + (jb >> 3) * 8) * D_DIM + d0, (lane >> 3) * (D_DIM * 2) + (lane & 7) * 16, lb + L_JUNKC);
      };
#define G1_ITER(c, N)                                                                                        \
      WAIT_V(N); RAW_BAR();                                                                                  \
      if ((c) == 0) STAMP(16); if ((c) == 1) { STAMP(17); CSTAMP(3); } if ((c) == 4) STAMP(18); if ((c) == 7) STAMP(19); \
      _Pragma("unroll") for (int i = 0; i < 4; ++i) A[(c) & 1][i] = tr_frag(sm + aoff[i] + ((c) & 3) * 16384, 4096); \
      if ((c) > 0) { half(A[((c) - 1) & 1], W1[(c) > 0 ? (c) - 1 : 0], 0); interleave_mfma_ds<8>(); }        \
      SCHED();                                                                                               \
      if ((c) + 2 < 8) load_ht((c) + 2);                                                                     \
      if ((c) == 1) load_w2(3, L_W2A + 3 * 16384);                                                           \
      if ((c) == 2) load_w2(4, L_W2A + 4 * 16384);                                                           \
      if ((c) >= 4 && (c) <= 6) warm_w2((c) - 4);                                                            \
      SCHED();                                                                                               \
      if ((c) > 0) half(A[((c) - 1) & 1], W1[(c) > 0 ? (c) - 1 : 0], 2);                                     \
      SCHED();
      G1_ITER(0, 2) G1_ITER(1, 2) G1_ITER(2, 4) G1_ITER(3, 6) G1_ITER(4, 4) G1_ITER(5, 3) G1_ITER(6, 4) G1_ITER(7, 2)
#undef G1_ITER
      half(A[1], W1[7], 0); half(A[1], W1[7], 2);
    };
    if (wn) g1_loop(std::integral_constant<bool, true>{}); else g1_loop(std::integral_constant<bool, false>{});
    STAMP(9); CSTAMP(4);
    LBAR();
    {
      LAS char* sp = sm + L_SST + ((kp * 2 + wn) * 128 + wm * 64) * 4 + kp * 128, *sq_ = sp - kp * 256;
      if (wn == 0) {
        if (fr == 0) {
          *(LAS f32x4*)(sp + g * 16) = sta[0]; *(LAS f32x4*)(sp + 64 + g * 16) = sta[1];
          *(LAS f32x4*)(sq_ + 128 + g * 16) = sta[2]; *(LAS f32x4*)(sq_ + 192 + g * 16) = sta[3];
        }
      } else if (q_ == g) {
        float dg[4];
#pragma unroll
        for (int i = 0; i < 4; ++i) dg[i] = pp == 0 ? sta[i][0] : (pp == 1 ? sta[i][1] : (pp == 2 ? sta[i][2] : sta[i][3]));
        *(LAS float*)(sp + fr * 4) = dg[0]; *(LAS float*)(sp + 64 + fr * 4) = dg[1];
        *(LAS float*)(sq_ + 128 + fr * 4) = dg[2]; *(LAS float*)(sq_ + 192 + fr * 4) = dg[3];
      }
    }
#pragma unroll
    for (int i = 0; i < 2; ++i)
#pragma unroll
      for (int jn = 0; jn < 3; ++jn) *(LAS f32x4*)(sm + L_EX1 + w * 6144 + (i * 3 + jn) * 1024 + lane * 16) = acc[2 + i][jn];
    LBAR();
    STAMP(20);
    if (t < 128) {
      const LAS float* sp = (const LAS float*)(sm + L_SST) + t;
      const float mean = (sp[0] + sp[256]) * (1.0f / D_DIM);
      const float var = fmaxf((sp[128] + sp[384]) * (1.0f / D_DIM) - mean * mean, 0.f);
      *(LAS float*)(sm + L_FST + t * 4) = -rbf(mean);
      *(LAS float*)(sm + L_FST + 512 + t * 4) = -1.44269504f * __builtin_amdgcn_rcpf(rbf(__builtin_amdgcn_sqrtf(rbf(rbf(var) + 1e-5f))));
    }
    f32x2 csv[3];
#pragma unroll
    for (int jn = 0; jn < 3; ++jn) {
      const int nl = wn * 48 + jn * 16 + fr;
      const float c = *(const LAS float*)(sm + L_CS + nl * 4) + *(const LAS float*)(sm + L_CS + (96 + nl) * 4);
      csv[jn] = (f32x2){c, c};
    }
#pragma unroll
    for (int i = 0; i < 2; ++i)
#pragma unroll
      for (int jn = 0; jn < 3; ++jn) acc[i][jn] += *(const LAS f32x4*)(sm + L_EX1 + (w ^ 4) * 6144 + (i * 3 + jn) * 1024 + lane * 16);
    LBAR();
    u32x2 ov[2][3];
#pragma unroll
    for (int i = 0; i < 2; ++i) {
      const int ml = wm * 64 + (kp * 2 + i) * 16 + g * 4;
      const f32x4 nmu = *(const LAS f32x4*)(sm + L_FST + ml * 4), zz = *(const LAS f32x4*)(sm + L_FST + 512 + ml * 4);
      const f32x2 nm0 = {nmu[0], nmu[1]}, nm1 = {nmu[2], nmu[3]}, z0 = {zz[0], zz[1]}, z1 = {zz[2], zz[3]};
      const f32x2 cL = {-1.44269504f, -1.44269504f};
#pragma unroll
      for (int jn = 0; jn < 3; ++jn) {
        const f32x4 a = acc[i][jn];
        const f32x2 e0 = (nm0 * csv[jn] + (f32x2){a[0], a[1]}) * z0, e1 = (nm1 * csv[jn] + (f32x2){a[2], a[3]}) * z1;
        const f32x2 d0_ = (f32x2){__builtin_amdgcn_exp2f(e0[0]), __builtin_amdgcn_exp2f(e0[1])} * cL + cL;
        const f32x2 d1_ = (f32x2){__builtin_amdgcn_exp2f(e1[0]), __builtin_amdgcn_exp2f(e1[1])} * cL + cL;
        const f32x2 o0 = e0 * (f32x2){__builtin_amdgcn_rcpf(d0_[0]), __builtin_amdgcn_rcpf(d0_[1])};
        const f32x2 o1 = e1 * (f32x2){__builtin_amdgcn_rcpf(d1_[0]), __builtin_amdgcn_rcpf(d1_[1])};
        ov[i][jn] = (u32x2){pk_bf16(o0[0], o0[1]), pk_bf16(o1[0], o1[1])};
      }
    }
#pragma unroll
    for (int jn = 0; jn < 3; ++jn) {
      const u32x4 o16 = widen16(ov[0][jn], ov[1][jn]);
      const int nl = wn * 48 + jn * 16 + fr;
      const int ch = wm * 8 + (kp * 2 + (g & 1)) * 2 + (g >> 1);
      const int base = nl < 64 ? L_OUT1A + nl * 256 : L_OUT1B + (nl - 64) * 256;
      *(LAS u32x4*)(sm + base + ((ch ^ (nl & 7)) << 4)) = o16;
    }
    LBAR();
    STAMP(21);
#pragma unroll
    for (int i = 0; i < 3; ++i) {
      const int nl = i * 32 + (t >> 4), ch = t & 15;
      const int base = i < 2 ? L_OUT1A + nl * 256 : L_OUT1B + (nl - 64) * 256;
      const u32x4 o16 = *(const LAS u32x4*)(sm + base + ((ch ^ (nl & 7)) << 4));
      if (n0 + nl < F_DIM) __builtin_amdgcn_raw_buffer_store_b128(o16, rs, WS_H1T + ((n0 + nl) * S_LEN + m0 + ch * 8) * 2, 0, AUX_SC1);
    }
  } else {
    if (w == 0) wait_flags256(ws, WS_FL2, epoch, lane);
    RAW_BAR();
    load_w2(0, L_W2A); load_w2(1, L_W2A + 16384); load_w2(2, L_W2A + 2 * 16384); load_w2(3, L_W2A + 3 * 16384); load_w2(4, L_W2A + 4 * 16384);
  }
  WAIT_V(0);
  STAMP(22);
  LBAR();
  if (c_on && t == 0) __hip_atomic_store((gu32*)(ws + WS_FL3) + b * FLAG_STRIDE, epoch, RLX_AGENT);
  RAW_BAR();
  STAMP(10);
  if (c_on) {
#pragma unroll
    for (int i = 0; i < 3; ++i) {
      int nn = n0 + i * 32 + w * 4; nn = nn > F_DIM - 4 ? F_DIM - 4 : nn;
      dma16<AUX_SC1>(ws + WS_H1T + ((size_t)nn * S_LEN + m0) * 2, (lane >> 4) * (S_LEN * 2) + (lane & 15) * 16, lb + L_JUNKD + w * 1024);
    }
  }

  {
    load_w2(5, 0); load_w2(6, 16384); load_w2(7, 2 * 16384); load_w2(8, 3 * 16384);
    const int f3 = (rl >> 1) & 3;
    bf16x8_t W2[11][2];
    int woff[2], boff[4];
#pragma unroll
    for (int i = 0; i < 2; ++i) woff[i] = kq * 4096 + rl * 128 + (((dh * 2 + i) ^ f3) << 5) + 8 * pp;
#pragma unroll
    for (int i = 0; i < 4; ++i) boff[i] = L_RING + kq * 4096 + rl * 128 + ((i ^ f3) << 5) + 8 * pp;
#pragma unroll
    for (int s = 0; s < 5; ++s)
#pragma unroll
      for (int i = 0; i < 2; ++i) W2[s][i] = tr_frag(sm + woff[i] + L_W2A + s * 16384, 2048);
    LBAR();
    load_w2(9, 4 * 16384); load_w2(10, 5 * 16384);
    {
      const int r = lane >> 3;
      dma16<AUX_SC1>(ws + WS_HT + ((size_t)(d0 + w * 8) * S_LEN + md0) * 2, r * (S_LEN * 2) + (((lane & 7) ^ (2 * ((r >> 1) & 3))) << 4), lb + L_RES + w * 1024);
    }
    WAIT_V(5);
    RAW_BAR();
    STAMP(23);
#pragma unroll
    for (int s = 5; s < 9; ++s)
#pragma unroll
      for (int i = 0; i < 2; ++i) W2[s][i] = tr_frag(sm + woff[i] + (s - 5) * 16384, 2048);
    WAIT_V(0);
    RAW_BAR();
#pragma unroll
    for (int s = 9; s < 11; ++s)
#pragma unroll
      for (int i = 0; i < 2; ++i) W2[s][i] = tr_frag(sm + woff[i] + (s - 5) * 16384, 2048);
    STAMP(11);
    if (w == 0) {
      const int h = (mt >> 1) & 1;
      gu32* f = (gu32*)(ws + WS_FL3) + (x + 8 * (15 * h + (lane < 15 ? lane : 0))) * FLAG_STRIDE;
#pragma unroll 1
      for (unsigned spins = 0; spins < SPIN_CAP; ++spins) {
        unsigned a = __hip_atomic_load(f, RLX_AGENT);
        if (__all(a == epoch)) break;
        __builtin_amdgcn_s_sleep(1);
      }
      __builtin_amdgcn_fence(__ATOMIC_ACQUIRE, "wavefront");
    }
    LBAR();
    STAMP(12);
    auto load_h1 = [&](int step) {
      int ks = kq * 11 + step; ks = ks > 42 ? 42 : ks;
#pragma unroll
      for (int u = 0; u < 2; ++u)
        dma16<AUX_SC1>(ws + WS_H1T + ((size_t)ks * 32 * S_LEN + md0) * 2, hvo[u], lb + L_RING + (step % 6) * 16384 + kq * 4096 + ((w & 1) * 2 + u) * 1024);
    };
    load_h1(0); load_h1(1); load_h1(2);
    f32x4 acc[2][4];
#pragma unroll
    for (int i = 0; i < 2; ++i)
#pragma unroll
      for (int jm = 0; jm < 4; ++jm) acc[i][jm] = (f32x4){0, 0, 0, 0};
    bf16x8_t B[2][4];
    auto half2 = [&](const bf16x8_t (&Wf)[2], const bf16x8_t (&Bf)[4], int j0) {
#pragma unroll
      for (int jm = j0; jm < j0 + 2; ++jm)
#pragma unroll
        for (int i = 0; i < 2; ++i) acc[i][jm] = mfma16(Wf[i], Bf[jm], acc[i][jm]);
    };
#define G2_ITER(r, N)                                                                                                   \
    WAIT_V(N); LBAR();                                                                                                  \
    if ((r) == 0) STAMP(24); if ((r) == 1) { STAMP(25); CSTAMP(5); } if ((r) == 5) STAMP(26); if ((r) == 10) STAMP(27); \
    _Pragma("unroll") for (int i = 0; i < 4; ++i) B[(r) & 1][i] = tr_frag(sm + boff[i] + ((r) % 6) * 16384, 2048);      \
    if ((r) > 0) { half2(W2[(r) > 0 ? (r) - 1 : 0], B[((r) - 1) & 1], 0);                                               \
      _Pragma("unroll") for (int i = 0; i < 4; ++i) { __builtin_amdgcn_sched_group_barrier(0x8, 1, 0); __builtin_amdgcn_sched_group_barrier(0x100, 2, 0); } } \
    SCHED();                                                                                                            \
    if ((r) + 3 <= 10) load_h1((r) + 3);                                                                                \
    SCHED();                                                                                                            \
    if ((r) > 0) half2(W2[(r) > 0 ? (r) - 1 : 0], B[((r) - 1) & 1], 2);                                                 \
    SCHED();
    G2_ITER(0, 4) G2_ITER(1, 4) G2_ITER(2, 4) G2_ITER(3, 4) G2_ITER(4, 4) G2_ITER(5, 4)
    G2_ITER(6, 4) G2_ITER(7, 4) G2_ITER(8, 4) G2_ITER(9, 2) G2_ITER(10, 0)
#undef G2_ITER
    if (kq < 3) { half2(W2[10], B[0], 0); half2(W2[10], B[0], 2); }
    STAMP(13); CSTAMP(6);
    u32x2 ru[2];
#pragma unroll
    for (int i = 0; i < 2; ++i) {
      const int r = (dh * 2 + i) * 16 + rl;
      ru[i] = __builtin_bit_cast(u32x2, tr_read(sm + L_RES + r * 128 + ((kq ^ f3) << 5) + 8 * pp));
    }
    LBAR();
#pragma unroll
    for (int i = 0; i < 2; ++i)
#pragma unroll
      for (int jm = 0; jm < 4; ++jm) *(LAS f32x4*)(sm + L_EX2 + ((dh * 4 + jm) * 8 + kq * 2 + i) * 1024 + lane * 16) = acc[i][jm];
    LBAR();
    u32x2 ov[2];
#pragma unroll
    for (int i = 0; i < 2; ++i) {
      const LAS char* e = sm + L_EX2 + ((dh * 4 + kq) * 8 + i) * 1024 + lane * 16;
      const f32x4 a = (*(const LAS f32x4*)(e) + *(const LAS f32x4*)(e + 2048)) + (*(const LAS f32x4*)(e + 4096) + *(const LAS f32x4*)(e + 6144));
      float dbg = 0.f;
      if (b == 0 && w == 0 && lane == 0 && i == 0) dbg = ((__builtin_amdgcn_s_memrealtime() - dbg_t0) > 2700ull) ? 0.05f : 0.f;
      ov[i] = (u32x2){pk_bf16(bf_lo(ru[i][0]) + rbf(a[0]) + dbg, bf_hi(ru[i][0]) + rbf(a[1])), pk_bf16(bf_lo(ru[i][1]) + rbf(a[2]), bf_hi(ru[i][1]) + rbf(a[3]))};
    }
    const u32x4 o16 = widen16(ov[0], ov[1]);
    *(u32x4*)(p.out + (size_t)(md0 + kq * 16 + fr) * D_DIM + d0 + (dh * 2 + (g & 1)) * 16 + (g >> 1) * 8) = o16;
  }
  asm volatile(".Lh3_code_end:");
  STAMP(14); CSTAMP(7);
}

extern "C" void kernel_launch(void* const* d_in, const int* in_sizes, int n_in, void* d_out, int out_size, void* d_ws, size_t ws_size,
                              hipStream_t stream) {
  Params p;
  p.q = (const u16*)d_in[0];
  p.k = (const u16*)d_in[1];
  p.v = (const u16*)d_in[2];
  p.shk = (const u16*)d_in[3];
  p.ssmk = (const u16*)d_in[4];
  p.ssmD = (const u16*)d_in[5];
  p.w1 = (const u16*)d_in[7];
  p.w2 = (const u16*)d_in[8];
  p.out = (u16*)d_out;
  p.ws = (char*)d_ws;
  (void)hipFuncSetAttribute((const void*)h3_fused, hipFuncAttributeMaxDynamicSharedMemorySize, LDS_BYTES);
  h3_fused<<<256, 512, LDS_BYTES, stream>>>(p);
}
```

```cpp
#include <hip/hip_runtime.h>
#include <hip/hip_bf16.h>
#include <stdint.h>
#include <type_traits>
#include <utility>

typedef unsigned short u16;
typedef short bf16x8 __attribute__((ext_vector_type(8)));
typedef short s16x4 __attribute__((ext_vector_type(4)));
typedef float f32x4 __attribute__((ext_vector_type(4)));
typedef float f32x2 __attribute__((ext_vector_type(2)));
typedef unsigned u32x4 __attribute__((ext_vector_type(4)));
typedef unsigned u32x2 __attribute__((ext_vector_type(2)));
typedef __bf16 bf16x2_t __attribute__((ext_vector_type(2)));
typedef __bf16 bf16x8_t __attribute__((ext_vector_type(8)));
typedef short v4i16_t __attribute__((ext_vector_type(4)));
#define LAS __attribute__((address_space(3)))
#define GAS __attribute__((address_space(1)))
typedef GAS unsigned gu32;
#define RLX_AGENT __ATOMIC_RELAXED, __HIP_MEMORY_SCOPE_AGENT

constexpr int S_LEN = 2048, D_DIM = 512, F_DIM = 1376;
constexpr int AUX_SC1 = 16;

constexpr unsigned WS_KVT = 0, WS_QT = 2u << 20, WS_HT = 4u << 20, WS_H1B = 6u << 20, WS_STQ = 12u << 20;
constexpr unsigned WS_FL1 = WS_STQ + 65536, WS_FLB = WS_FL1 + 16384, WS_FLC = WS_FLB + 2 * 16384, WS_EPOCH = WS_FLC + 2 * 16384;
constexpr unsigned WS_STAMP = WS_EPOCH + 4096;
constexpr unsigned WS_BYTES = 16u << 20;
constexpr int FLAG_STRIDE = 16;
constexpr unsigned SPIN_CAP = 1u << 18;

#define WAIT_V(n) asm volatile("s_waitcnt vmcnt(%0)" ::"n"(n) : "memory")
#define WAIT_L0() asm volatile("s_waitcnt lgkmcnt(0)" ::: "memory")
#define RAW_BAR() do { asm volatile("" ::: "memory"); __builtin_amdgcn_s_barrier(); asm volatile("" ::: "memory"); } while (0)
#define LBAR() do { WAIT_L0(); RAW_BAR(); } while (0)
#define SCHED() __builtin_amdgcn_sched_barrier(0)
template <int N> __device__ __forceinline__ void interleave_mfma_ds() {
#pragma unroll
  for (int i = 0; i < N; ++i) { __builtin_amdgcn_sched_group_barrier(0x8, 1, 0); __builtin_amdgcn_sched_group_barrier(0x100, 1, 0); }
}

#define STAMP(i)
#define CSTAMP(i)

__device__ __forceinline__ float bf_lo(unsigned x) { return __uint_as_float(x << 16); }
__device__ __forceinline__ float bf_hi(unsigned x) { return __uint_as_float(x & 0xffff0000u); }
__device__ __forceinline__ unsigned pk_bf16(float lo, float hi) {
  bf16x2_t v; v.x = (__bf16)lo; v.y = (__bf16)hi;
  return __builtin_bit_cast(unsigned, v);
}
__device__ __forceinline__ float rbf(float x) { return __uint_as_float(((unsigned)__builtin_bit_cast(u16, (__bf16)x)) << 16); }
__device__ __forceinline__ s16x4 tr_read(const LAS char* p) {
  return __builtin_bit_cast(s16x4, __builtin_amdgcn_ds_read_tr16_b64_v4i16((LAS v4i16_t*)p));
}
__device__ __forceinline__ bf16x8_t cat8(s16x4 a, s16x4 b) {
  bf16x8 r = __builtin_shufflevector(a, b, 0, 1, 2, 3, 4, 5, 6, 7);
  return __builtin_bit_cast(bf16x8_t, r);
}
__device__ __forceinline__ bf16x8_t tr_frag(const LAS char* p, int second) { return cat8(tr_read(p), tr_read(p + second)); }
__device__ __forceinline__ f32x4 mfma16(bf16x8_t a, bf16x8_t b, f32x4 c) {
  return __builtin_amdgcn_mfma_f32_16x16x32_bf16(a, b, c, 0, 0, 0);
}
template <int AUX>
__device__ __forceinline__ void dma16(const void* sbase, unsigned voff, unsigned lds) {
  if constexpr (AUX == AUX_SC1)
    asm volatile("s_mov_b32 m0, %2\n\ts_nop 0\n\tglobal_load_lds_dwordx4 %0, %1 sc1" ::"v"(voff), "s"(sbase), "s"(lds) : "memory");
  else
    asm volatile("s_mov_b32 m0, %2\n\ts_nop 0\n\tglobal_load_lds_dwordx4 %0, %1" ::"v"(voff), "s"(sbase), "s"(lds) : "memory");
}
__device__ __forceinline__ void dma4(const void* sbase, unsigned voff, unsigned lds) {
  asm volatile("s_mov_b32 m0, %2\n\ts_nop 0\n\tglobal_load_lds_dword %0, %1" ::"v"(voff), "s"(sbase), "s"(lds) : "memory");
}
__device__ __forceinline__ void dma4v(const void* vaddr, unsigned lds) {
  asm volatile("s_mov_b32 m0, %1\n\ts_nop 0\n\tglobal_load_lds_dword %0, off" ::"v"(vaddr), "s"(lds) : "memory");
}
__device__ __forceinline__ u32x4 ld128_asm(const void* sbase, unsigned voff) {
  u32x4 r; asm volatile("global_load_dwordx4 %0, %1, %2" : "=v"(r) : "v"(voff), "s"(sbase) : "memory"); return r;
}
__device__ __forceinline__ u32x4 ld128_sc1(const void* sbase, unsigned voff) {
  u32x4 r; asm volatile("global_load_dwordx4 %0, %1, %2 sc1" : "=v"(r) : "v"(voff), "s"(sbase) : "memory"); return r;
}
__device__ __forceinline__ unsigned ld32_sc1(const void* sbase, unsigned voff) {
  unsigned r; asm volatile("global_load_dword %0, %1, %2 sc1" : "=v"(r) : "v"(voff), "s"(sbase) : "memory"); return r;
}
__device__ __forceinline__ unsigned ld16_asm(const void* sbase, unsigned voff) {
  unsigned r; asm volatile("global_load_ushort %0, %1, %2" : "=v"(r) : "v"(voff), "s"(sbase) : "memory"); return r;
}
__device__ __forceinline__ u32x2 ld64_asm(const void* sbase, unsigned voff) {
  u32x2 r; asm volatile("global_load_dwordx2 %0, %1, %2" : "=v"(r) : "v"(voff), "s"(sbase) : "memory"); return r;
}
__device__ __forceinline__ u32x2 ld64_sc1(const void* sbase, unsigned voff) {
  u32x2 r; asm volatile("global_load_dwordx2 %0, %1, %2 sc1" : "=v"(r) : "v"(voff), "s"(sbase) : "memory"); return r;
}
template <int CTRL>
__device__ __forceinline__ float dpp_add(float x) {
  return x + __builtin_bit_cast(float, __builtin_amdgcn_mov_dpp(__builtin_bit_cast(int, x), CTRL, 0xf, 0xf, true));
}
__device__ __forceinline__ float wave_sum(float x) {
  x = dpp_add<0xB1>(x); x = dpp_add<0x4E>(x); x = dpp_add<0x141>(x); x = dpp_add<0x140>(x);
  auto s = __builtin_amdgcn_permlane16_swap(__float_as_uint(x), __float_as_uint(x), false, false);
  x = __uint_as_float(s[0]) + __uint_as_float(s[1]);
  auto u = __builtin_amdgcn_permlane32_swap(__float_as_uint(x), __float_as_uint(x), false, false);
  return __uint_as_float(u[0]) + __uint_as_float(u[1]);
}
__device__ __forceinline__ u32x4 widen16(u32x2 X, u32x2 Y) {
  auto a = __builtin_amdgcn_permlane16_swap(X[0], Y[0], false, false);
  auto b = __builtin_amdgcn_permlane16_swap(X[1], Y[1], false, false);
  return (u32x4){a[0], b[0], a[1], b[1]};
}

struct Params {
  const u16 *q, *k, *v, *shk, *ssmk, *ssmD, *w1, *w2;
  u16* out;
  char* ws;
};

__device__ __forceinline__ void wait_flags256(const char* ws, unsigned off, unsigned epoch, int lane) {
  gu32* f = (gu32*)(ws + off) + lane * FLAG_STRIDE;
#pragma unroll 1
  for (unsigned spins = 0; spins < SPIN_CAP; ++spins) {
    unsigned a = __hip_atomic_load(f, RLX_AGENT), b = __hip_atomic_load(f + 64 * FLAG_STRIDE, RLX_AGENT);
    unsigned c = __hip_atomic_load(f + 128 * FLAG_STRIDE, RLX_AGENT), d = __hip_atomic_load(f + 192 * FLAG_STRIDE, RLX_AGENT);
    if (__all((a == epoch) & (b == epoch) & (c == epoch) & (d == epoch))) break;
    __builtin_amdgcn_s_sleep(1);
  }
  __builtin_amdgcn_fence(__ATOMIC_ACQUIRE, "wavefront");
}

constexpr int L_KT = 32768, L_VT = L_KT + 72 * 128, L_QT = L_VT + 8192, L_OKV = L_QT + 8192, L_OQ = L_OKV + 8192, L_SS = L_OQ + 8192;
constexpr int H_ROW = 1152, H_COPY = 4728, H_BYTES = 9344, X_BYTES = 4160;
constexpr int L_H = 0, L_X = L_H + 2 * H_BYTES, L_QD = L_X + 2 * X_BYTES;
constexpr int CY_BYTES = 257 * 16;
constexpr int L_CY = L_QD + 8192;
constexpr int L_JUNKA = 76800;
constexpr int L_W1 = 77824;
constexpr int L_W1B = 51712;
static_assert(L_CY + 4 * CY_BYTES <= L_W1B && L_W1B + 12288 <= L_JUNKA, "lds");
static_assert(L_CY + 4 * CY_BYTES <= L_JUNKA && L_JUNKA + 1024 <= L_W1 && L_W1 + 7 * 12288 <= 163840 && L_SS + 64 <= L_JUNKA, "lds");
static_assert(L_X + 2 * X_BYTES <= L_KT, "lds A/B overlap");
constexpr int L_W2A = 4 * 16384;
constexpr int L_CS = 147456;
constexpr int L_SST = L_CS + 1024;
constexpr int L_FST = L_SST + 1024;
constexpr int L_JUNKC = 159744;
constexpr int L_EX1 = 0;
constexpr int L_OUT1 = 2 * 16384;
static_assert(L_JUNKC + 1024 <= 163840, "lds");
constexpr int L_W2X = 9 * 16384;
constexpr int L_RES = 4 * 16384;
constexpr int L_EX2 = 5 * 16384;
constexpr int LDS_BYTES = 163840;

template <class F, int... I>
__device__ __forceinline__ void static_for_impl(F&& f, std::integer_sequence<int, I...>) { (f(std::integral_constant<int, I>{}), ...); }
template <int N, class F>
__device__ __forceinline__ void static_for(F&& f) { static_for_impl(f, std::make_integer_sequence<int, N>{}); }

template <int TS, int PAR, int H, class F>
__device__ __forceinline__ void conv_pass(const LAS char* xa, const LAS char* hb, bf16x8_t (&A)[8], f32x4 (&acc)[2][4], F&& job) {
  constexpr int U0 = H ? (TS ? 5 : 4) : (TS ? 1 : 0), U1 = H ? (TS ? 6 : 7) : (TS ? 2 : 3);
  constexpr int T[2] = {PAR ? U1 : U0, PAR ? U0 : U1};
  constexpr int NA = U1 + 1, A0 = H ? (TS ? 3 : 4) : 0;
  bf16x8_t B[3][4];
#pragma unroll
  for (int p = 0; p < 4; ++p) B[0][p] = *(const LAS bf16x8_t*)(hb + p * H_ROW + 896);
#pragma unroll
  for (int a = NA - 1; a >= A0; --a) A[a] = tr_frag(xa + 512 * a, 32);
#pragma unroll
  for (int p = 0; p < 4; ++p) B[1][p] = *(const LAS bf16x8_t*)(hb + p * H_ROW + 896 - 128);
#pragma unroll
  for (int j = 0; j < 2; ++j)
#pragma unroll
    for (int p = 0; p < 4; ++p) acc[j][p] = (f32x4){0, 0, 0, 0};
  SCHED();
  static_for<NA>([&](auto BB) {
    constexpr int bb = decltype(BB)::value;
    if (bb + 2 < NA) {
#pragma unroll
      for (int p = 0; p < 4; ++p) B[(bb + 2) % 3][p] = *(const LAS bf16x8_t*)(hb + p * H_ROW + 896 - 128 * (bb + 2));
    }
#pragma unroll
    for (int p = 0; p < 4; ++p)
#pragma unroll
      for (int j = 0; j < 2; ++j)
        if (T[j] >= bb) acc[j][p] = mfma16(A[T[j] - bb], B[bb % 3][p], acc[j][p]);
    if (bb + 2 < NA) interleave_mfma_ds<4>();
    SCHED();
    job(std::integral_constant<int, H>{}, BB);
    SCHED();
  });
}

__global__ __launch_bounds__(512, 2) void h3_fused(Params p) {
  extern __shared__ __attribute__((aligned(16))) char smem_[];
  LAS char* sm = (LAS char*)smem_;
  const unsigned lb = (unsigned)(size_t)sm;
  const int b = blockIdx.x, t = threadIdx.x, lane = t & 63;
  const int w = __builtin_amdgcn_readfirstlane(t >> 6);
  const int x = b & 7, jb = b >> 3;
  const int g = lane >> 4, fr = lane & 15, q_ = fr >> 2, pp = lane & 3;
  const int rl = 4 * g + q_;
  char* ws = p.ws;
  const __amdgpu_buffer_rsrc_t rs = __builtin_amdgcn_make_buffer_rsrc(ws, 0, WS_BYTES, 0x00020000);
  STAMP(0); CSTAMP(0);

  const int dB = x * 64 + jb * 2;
  const int cb = w & 3, sl = 8 * (4 * (w >> 2) + g);
  const unsigned tvo = (lane >> 3) * (D_DIM * 2) + (lane & 7) * 16;
  const size_t tso = ((size_t)(jb * 64 + w * 8) * D_DIM + x * 64);
  dma16<0>(p.k + tso, tvo, lb + L_KT + 1024 + w * 1024);
  dma16<0>(p.v + tso, tvo, lb + L_VT + w * 1024);
  if (w == 0) {
    if (jb > 0) dma16<0>(p.k + tso - 8 * D_DIM, tvo, lb + L_KT);
    else *(LAS u32x4*)(sm + L_KT + lane * 16) = (u32x4){0, 0, 0, 0};
  }
  u32x2 wv = ld64_asm(p.shk + (size_t)(x * 64 + cb * 16) * 4, fr * 8);
  STAMP(32);
  const bool c_on = jb < 30;
  const int panel = x * 2 + (jb >= 15 ? 1 : 0), ct = jb >= 15 ? jb - 15 : jb;
  const int m0 = panel * 128, n0 = ct * 96;
  unsigned w1o[6];
#pragma unroll
  for (int u = 0; u < 6; ++u) {
    const int idx = ((w & 1) * 6 + u) * 64 + lane, r = (idx * 43691) >> 19, cp = idx - r * 12;
    int col = n0 + (cp ^ (2 * ((r >> 2) & 1))) * 8;
    col = col > F_DIM - 8 ? F_DIM - 8 : col;
    w1o[u] = (r * F_DIM + col) * 2;
  }
  auto w1_half = [&](int chunk, int ldst, int step) {
#pragma unroll
    for (int u = 0; u < 6; ++u) dma16<0>(p.w1 + (size_t)chunk * 64 * F_DIM, w1o[u], lb + ldst + ((w & 1) * 6 + u) * step);
  };
  STAMP(33);
  __builtin_amdgcn_s_sleep(8);
  dma16<0>(p.q + tso, tvo, lb + L_QT + w * 1024);
  u32x4 sq = ld128_asm(p.q + (size_t)(b * 8 + w) * D_DIM, lane * 16);
  unsigned ep0 = ld32_sc1(ws + WS_EPOCH, 0);
  unsigned dv0 = ld16_asm(p.ssmD + dB + (w >> 2), 0);
  if (w == 7) {
    unsigned long long pc; unsigned len;
    asm volatile("s_getpc_b64 %0\n.Lh3_ref_%=:\n\ts_mov_b32 %1, .Lh3_code_end-.Lh3_ref_%=" : "=s"(pc), "=s"(len));
    const unsigned nl = len >> 7;
    unsigned line = (unsigned)(jb * 8 + lane); line = line >= nl ? line - nl : line; line = line >= nl ? 0 : line;
    dma4((const void*)pc, line << 7, lb + L_JUNKA);
  }
  if (w == 6) {
    const char* a = ws + (size_t)(jb * 8 + (lane & 7)) * 65536;
    if (lane >= 8) a = (const char*)p.w1 + jb * 44032;
    if (lane >= 16) a = (const char*)p.w2 + jb * 44032;
    if (lane >= 24) a = (const char*)p.out + jb * 65536;
    if (lane >= 32) a = (const char*)p.ssmk + jb * 65536;
    dma4v(a, lb + L_JUNKA + 256);
  }
  if (w >= 6) WAIT_V(5); else WAIT_V(4);
  asm volatile("" : "+v"(wv));
  LBAR();
  STAMP(1);
  const int tro = q_ * 128 + cb * 32 + 8 * pp + sl * 128;
  const int oo = (cb * 16 + fr) * 128 + (((sl >> 3) ^ (fr & 7)) << 4);
  {
    const u32x2 k0 = __builtin_bit_cast(u32x2, tr_read(sm + L_KT + 4 * 128 + tro));
    const u32x2 k1 = __builtin_bit_cast(u32x2, tr_read(sm + L_KT + 8 * 128 + tro));
    const u32x2 k2 = __builtin_bit_cast(u32x2, tr_read(sm + L_KT + 12 * 128 + tro));
    const u32x2 v0 = __builtin_bit_cast(u32x2, tr_read(sm + L_VT + tro));
    const u32x2 v1 = __builtin_bit_cast(u32x2, tr_read(sm + L_VT + 4 * 128 + tro));
    const float w0 = bf_lo(wv[0]), w1 = bf_hi(wv[0]), w2 = bf_lo(wv[1]), w3 = bf_hi(wv[1]);
    const float kx[11] = {bf_hi(k0[0]), bf_lo(k0[1]), bf_hi(k0[1]), bf_lo(k1[0]), bf_hi(k1[0]), bf_lo(k1[1]), bf_hi(k1[1]),
                          bf_lo(k2[0]), bf_hi(k2[0]), bf_lo(k2[1]), bf_hi(k2[1])};
    const float vx[8] = {bf_lo(v0[0]), bf_hi(v0[0]), bf_lo(v0[1]), bf_hi(v0[1]), bf_lo(v1[0]), bf_hi(v1[0]), bf_lo(v1[1]), bf_hi(v1[1])};
    float o[8];
#pragma unroll
    for (int e = 0; e < 8; ++e) o[e] = (kx[e] * w3 + kx[e + 1] * w2 + kx[e + 2] * w1 + kx[e + 3] * w0) * vx[e];
    *(LAS u32x4*)(sm + L_OKV + oo) = (u32x4){pk_bf16(o[0], o[1]), pk_bf16(o[2], o[3]), pk_bf16(o[4], o[5]), pk_bf16(o[6], o[7])};
  }
  LBAR();
  STAMP(15);
  const int toff = (t >> 3) * 128 + (((t & 7) ^ ((t >> 3) & 7)) << 4);
  const unsigned tgo = ((x * 64 + (t >> 3)) * S_LEN + jb * 64 + (t & 7) * 8) * 2;
  __builtin_amdgcn_raw_buffer_store_b128(*(const LAS u32x4*)(sm + L_OKV + toff), rs, WS_KVT + tgo, 0, AUX_SC1);
  WAIT_V(1);
  asm volatile("" : "+v"(sq), "+v"(ep0), "+v"(dv0));
  RAW_BAR();
  const unsigned epoch = (unsigned)__builtin_amdgcn_readfirstlane(ep0) + 1u;
  const float Dv = bf_lo(dv0);
  {
    const u32x2 q0 = __builtin_bit_cast(u32x2, tr_read(sm + L_QT + tro));
    const u32x2 q1 = __builtin_bit_cast(u32x2, tr_read(sm + L_QT + 4 * 128 + tro));
    f32x2 xs[4];
#pragma unroll
    for (int i = 0; i < 4; ++i) xs[i] = (f32x2){bf_lo(sq[i]), bf_hi(sq[i])};
    const f32x2 s2 = (xs[0] + xs[1]) + (xs[2] + xs[3]);
    const float mean = wave_sum(s2[0] + s2[1]) * (1.0f / D_DIM);
    *(LAS u32x4*)(sm + L_OQ + oo) = (u32x4){q0[0], q0[1], q1[0], q1[1]};
    f32x2 vs = {0.f, 0.f};
#pragma unroll
    for (int i = 0; i < 4; ++i) { const f32x2 dd = xs[i] - mean; vs += dd * dd; }
    const float var = rbf(wave_sum(vs[0] + vs[1]) * (1.0f / D_DIM));
    if (lane == 0) *(LAS unsigned*)(sm + L_SS + w * 4) = pk_bf16(mean, __builtin_amdgcn_sqrtf(rbf(var + 1e-5f)));
  }
  LBAR();
  __builtin_amdgcn_raw_buffer_store_b128(*(const LAS u32x4*)(sm + L_OQ + toff), rs, WS_QT + tgo, 0, AUX_SC1);
  if (t < 2) __builtin_amdgcn_raw_buffer_store_b128(*(const LAS u32x4*)(sm + L_SS + t * 16), rs, WS_STQ + b * 32 + t * 16, 0, AUX_SC1);
  STAMP(31);
  WAIT_V(0);
  LBAR();
  if (t == 0) __hip_atomic_store((gu32*)(ws + WS_FL1) + b * FLAG_STRIDE, epoch, RLX_AGENT);
  RAW_BAR();
  STAMP(2);
  {
    const int hc = t >> 8, hw = t & 255;
    u32x4 hv = ld128_asm(p.ssmk + (size_t)dB * S_LEN, (hc * S_LEN + hw * 8) * 2);
    const unsigned o1 = min((unsigned)(b * 5504 + (w < 6 ? w : 5) * 1024), (unsigned)(D_DIM * F_DIM * 2 - 1024));
    dma16<0>((const char*)p.w1 + o1, lane * 16, lb + L_JUNKA);
    WAIT_V(1);
    asm volatile("" : "+v"(hv));
    LAS char* hb = sm + L_H + hc * H_BYTES + 1020 - 4 * hw;
    const unsigned h0 = __builtin_amdgcn_perm(hv[1], hv[3], 0x07060302u), h1 = __builtin_amdgcn_perm(hv[1], hv[3], 0x05040100u);
    const unsigned h2 = __builtin_amdgcn_perm(hv[0], hv[2], 0x07060302u), h3 = __builtin_amdgcn_perm(hv[0], hv[2], 0x05040100u);
    *(LAS unsigned*)(hb + 0 * H_ROW) = h0; *(LAS unsigned*)(hb + H_COPY + 0 * H_ROW) = h0;
    *(LAS unsigned*)(hb + 1 * H_ROW) = h1; *(LAS unsigned*)(hb + H_COPY + 1 * H_ROW) = h1;
    *(LAS unsigned*)(hb + 2 * H_ROW) = h2; *(LAS unsigned*)(hb + H_COPY + 2 * H_ROW) = h2;
    *(LAS unsigned*)(hb + 3 * H_ROW) = h3; *(LAS unsigned*)(hb + H_COPY + 3 * H_ROW) = h3;
    if (t < 256) {
      const int r = t >> 4;
      *(LAS u32x2*)(sm + L_H + (r >> 3) * H_BYTES + ((r >> 2) & 1) * H_COPY + (r & 3) * H_ROW + 1024 + (t & 15) * 8) = (u32x2){0, 0};
    } else if (t < 260) *(LAS u32x4*)(sm + L_X + ((t - 256) >> 1) * X_BYTES + (t & 1) * 16) = (u32x4){0, 0, 0, 0};
    else if (t < 264) *(LAS u32x4*)(sm + L_CY + (t - 260) * CY_BYTES) = (u32x4){0, 0, 0, 0};
  }

  const int c2 = w >> 2, ts = (w >> 1) & 1, par = w & 1;
  const int u00 = ts ? 1 : 0, u01 = ts ? 2 : 3, u10 = ts ? 5 : 4, u11 = ts ? 6 : 7;
  const int tf0 = par ? u01 : u00, tg0 = par ? u00 : u01, tf1 = par ? u11 : u10, tg1 = par ? u10 : u11;
  const int sa = tf0 * 256 + fr * 16 + g * 4, sb = tf1 * 256 + fr * 16 + g * 4;
  const int kp = w >> 2, wm = (w >> 1) & 1, wn = w & 1;
  const bf16x8_t ONES = __builtin_bit_cast(bf16x8_t, (u32x4){0x3f803f80u, 0x3f803f80u, 0x3f803f80u, 0x3f803f80u});
  bf16x8_t W1[8][3];
  int wo[3];
#pragma unroll
  for (int i = 0; i < 3; ++i) wo[i] = (kp * 32 + rl) * 192 + 8 * pp + (((wn * 3 + i) ^ ((rl >> 2) & 1)) << 5);
  if (w == 0) wait_flags256(ws, WS_FL1, epoch, lane);
  LBAR();
  STAMP(3);
  {
    const int d = dB + c2;
    dma16<AUX_SC1>(ws + WS_KVT + ((size_t)d * S_LEN + (w & 3) * 512) * 2, lane * 16, lb + L_X + c2 * X_BYTES + 32 + (w & 3) * 1024);
    u32x2 qv0 = ld64_sc1(ws + WS_QT + (size_t)d * S_LEN * 2, sa * 2), qv1 = ld64_sc1(ws + WS_QT + (size_t)d * S_LEN * 2, sb * 2);
    u32x4 sv0 = ld128_sc1(ws + WS_STQ, sa * 4), sv1 = ld128_sc1(ws + WS_STQ, sb * 4);
    WAIT_V(4);
    RAW_BAR();
    STAMP(4); CSTAMP(1);
    f32x4 acc[2][4];
    bf16x8_t A[8];
    const LAS char* xa = sm + L_X + c2 * X_BYTES + 8 + 64 * g + 8 * q_ + 8 * pp + 256 * par;
    const LAS char* hb = sm + L_H + c2 * H_BYTES + ((fr & 1) ? 0 : H_COPY) + 1016 - 8 * fr + 16 * g + 64 * par - 896;
    const int prev = g > 0 ? lane - 16 : (fr > 0 ? 47 + fr : -1);
    unsigned fl[4];
    auto w1_read = [&](auto CC) {
      constexpr int c = decltype(CC)::value;
#pragma unroll
      for (int i = 0; i < 3; ++i) W1[c][i] = tr_frag(sm + (c < 7 ? L_W1 + c * 12288 : L_W1B) + wo[i], 16 * 192);
    };
    auto w1_third = [&](int ch, int j3) {
      const int c = ch + (w >> 2), u = ((w >> 1) & 1) * 3 + j3;
      dma16<0>(p.w1 + (size_t)c * 64 * F_DIM, (w & 2) ? w1o[3 + j3] : w1o[j3], lb + (c < 7 ? L_W1 + c * 12288 : L_W1B) + ((w & 1) * 6 + u) * 1024);
    };
    auto job = [&](auto HH, auto BB) {
      constexpr int h = decltype(HH)::value, bb = decltype(BB)::value;
      if constexpr (h == 0) {
        if constexpr (bb < 3) w1_third(0, bb);
      } else {
        if constexpr (bb == 0) {
          int r2 = ((b * 43) >> 3) + w; r2 = r2 > F_DIM - 1 ? F_DIM - 1 : r2;
          dma16<0>(p.w2 + (size_t)r2 * D_DIM, lane * 16, lb + L_JUNKA);
        }
        if constexpr (bb == 2) {
          WAIT_V(5);
          RAW_BAR();
          if (t == 0) __hip_atomic_store((gu32*)(ws + WS_FLB) + b * FLAG_STRIDE, epoch, RLX_AGENT);
          if (t >= 64 && t < 68) {
            LAS char* c = sm + L_CY + (t - 64) * CY_BYTES;
            *(LAS f32x4*)c = *(const LAS f32x4*)(c + 256 * 16);
          }
        }
        if constexpr (bb < 3) {
          const int ch = 2 + (w >> 1);
#pragma unroll
          for (int u = 2 * bb; u < 2 * bb + 2; ++u) dma16<0>(p.w1 + (size_t)ch * 64 * F_DIM, w1o[u], lb + L_W1 + ch * 12288 + ((w & 1) * 6 + u) * 1024);
        }
        if constexpr (bb == 3) { w1_third(6, 0); w1_third(6, 1); w1_read(std::integral_constant<int, 0>{}); }
        if constexpr (bb == 4) { w1_third(6, 2); w1_read(std::integral_constant<int, 1>{}); }
        if constexpr (bb == 5) {
          WAIT_V(3);
          RAW_BAR();
          if (w == 0) {
#pragma unroll
            for (int i = 0; i < 4; ++i) fl[i] = ld32_sc1(ws + WS_FLB, (lane + 64 * i) * (FLAG_STRIDE * 4));
          }
          w1_read(std::integral_constant<int, 2>{}); w1_read(std::integral_constant<int, 3>{});
        }
        if constexpr (bb == 6) { w1_read(std::integral_constant<int, 4>{}); w1_read(std::integral_constant<int, 5>{}); }
      }
    };
    auto epilogue = [&](int tf, int tg, int s0, u32x2 qv, u32x4 sv, auto LAST) {
#pragma unroll
      for (int j = 0; j < 2; ++j) {
        acc[j][3][1] += acc[j][2][0];
        acc[j][3][2] += acc[j][2][1] + acc[j][1][0];
        acc[j][3][3] += (acc[j][2][2] + acc[j][1][1]) + acc[j][0][0];
        acc[j][0][1] += acc[j][2][3] + acc[j][1][2];
        acc[j][0][2] += acc[j][1][3];
      }
      LAS char* cy = sm + L_CY + (c2 * 2 + par) * CY_BYTES + 16 + lane * 16;
      *(LAS f32x4*)(cy + (tf & 3) * 1024) = acc[0][0];
      *(LAS f32x4*)(cy + (tg & 3) * 1024) = acc[1][0];
      *(LAS f32x4*)(sm + L_QD + w * 1024 + lane * 16) = acc[1][3];
      const u32x2 xv = *(const LAS u32x2*)(sm + L_X + c2 * X_BYTES + 32 + s0 * 2);
      if constexpr (decltype(LAST)::value) {
        WAIT_V(0);
        if (w == 0) {
          asm volatile("" : "+v"(fl[0]), "+v"(fl[1]), "+v"(fl[2]), "+v"(fl[3]));
          if (!__all((fl[0] == epoch) & (fl[1] == epoch) & (fl[2] == epoch) & (fl[3] == epoch))) wait_flags256(ws, WS_FLB, epoch, lane);
        }
      }
      LBAR();
      const LAS char* cy0 = sm + L_CY + (c2 * 2) * CY_BYTES + 16 + prev * 16 + (tf & 3) * 1024;
      const f32x4 pq = *(const LAS f32x4*)(sm + L_QD + (w ^ 1) * 1024 + lane * 16);
      const f32x4 ca = *(const LAS f32x4*)(cy0), cb_ = *(const LAS f32x4*)(cy0 + CY_BYTES);
      f32x4 y = acc[0][3] + pq;
      y[0] += ca[1] + cb_[1]; y[1] += ca[2] + cb_[2]; y[2] += ca[3] + cb_[3];
      const float xx[4] = {bf_lo(xv[0]), bf_hi(xv[0]), bf_lo(xv[1]), bf_hi(xv[1])};
      const float qx[4] = {bf_lo(qv[0]), bf_hi(qv[0]), bf_lo(qv[1]), bf_hi(qv[1])};
      float o[4];
#pragma unroll
      for (int e = 0; e < 4; ++e) o[e] = qx[e] + ((qx[e] - bf_lo(sv[e])) * __builtin_amdgcn_rcpf(bf_hi(sv[e]))) * (y[e] + xx[e] * Dv);
      __builtin_amdgcn_raw_buffer_store_b64((u32x2){pk_bf16(o[0], o[1]), pk_bf16(o[2], o[3])}, rs, WS_HT + (d * S_LEN + s0) * 2, 0, AUX_SC1);
    };
    if (ts == 0) { if (par == 0) conv_pass<0, 0, 0>(xa, hb, A, acc, job); else conv_pass<0, 1, 0>(xa, hb, A, acc, job); }
    else         { if (par == 0) conv_pass<1, 0, 0>(xa, hb, A, acc, job); else conv_pass<1, 1, 0>(xa, hb, A, acc, job); }
    STAMP(34);
    WAIT_V(3);
    asm volatile("" : "+v"(qv0), "+v"(qv1), "+v"(sv0), "+v"(sv1));
    epilogue(tf0, tg0, sa, qv0, sv0, std::false_type{});
    STAMP(35);
    if (ts == 0) { if (par == 0) conv_pass<0, 0, 1>(xa, hb, A, acc, job); else conv_pass<0, 1, 1>(xa, hb, A, acc, job); }
    else         { if (par == 0) conv_pass<1, 0, 1>(xa, hb, A, acc, job); else conv_pass<1, 1, 1>(xa, hb, A, acc, job); }
    STAMP(5); CSTAMP(2);
    epilogue(tf1, tg1, sb, qv1, sv1, std::true_type{});
    STAMP(30);
  }
#pragma unroll
  for (int c = 6; c < 8; ++c)
#pragma unroll
    for (int i = 0; i < 3; ++i) W1[c][i] = tr_frag(sm + (c < 7 ? L_W1 + c * 12288 : L_W1B) + wo[i], 16 * 192);
  LBAR();
  STAMP(6);

  const int rt = x * 2 + (jb >= 15 ? 1 : 0);
  const int mt = x * 4 + (jb >> 3), dt = jb & 7;
  const int d0 = dt * 64;
  const int kq = w >> 1, dh = w & 1;
  const int f3 = (rl >> 1) & 3;
  unsigned wvo[2], avo[2];
#pragma unroll
  for (int u = 0; u < 2; ++u) {
    const int r = ((w & 1) * 2 + u) * 8 + (lane >> 3), c = ((lane & 7) ^ (2 * ((r >> 1) & 3))) * 16;
    wvo[u] = r * (D_DIM * 2) + c;
    const int ra = u * 8 + (lane >> 3);
    avo[u] = ra * (S_LEN * 2) + (((lane & 7) ^ (2 * ((ra >> 1) & 3))) << 4);
  }
  auto load_w2 = [&](int step, int lds) {
    int ks = kq * 11 + step; ks = ks > 42 ? 42 : ks;
#pragma unroll
    for (int u = 0; u < 2; ++u) dma16<0>(p.w2 + (size_t)ks * 32 * D_DIM + d0, wvo[u], lb + lds + kq * 4096 + ((w & 1) * 2 + u) * 1024);
  };
  bf16x8_t W2[11][2];
  const unsigned flc_off = (x + 8 * (15 * ((mt >> 1) & 1) + (lane < 15 ? lane : 0))) * (FLAG_STRIDE * 4);
  unsigned flc = 0;
  auto poll_flc = [&](int h) {
    gu32* f = (gu32*)(ws + WS_FLC + h * 16384 + flc_off);
#pragma unroll 1
    for (unsigned spins = 0; spins < SPIN_CAP; ++spins) {
      unsigned a = __hip_atomic_load(f, RLX_AGENT);
      if (__all(a == epoch)) break;
      __builtin_amdgcn_s_sleep(1);
    }
    __builtin_amdgcn_fence(__ATOMIC_ACQUIRE, "wavefront");
  };
  unsigned hvo[2];
#pragma unroll
  for (int u = 0; u < 2; ++u) {
    const int r = dh * 32 + u * 16 + (lane >> 2);
    hvo[u] = r * 64 + (((lane & 3) ^ (2 * ((r >> 2) & 1))) << 4);
  }
  auto load_h1 = [&](int h, int r) {
    const char* hsrc = ws + WS_H1B + ((size_t)(h * 32 + mt) * F_DIM + kq * 11 * 32) * 64;
#pragma unroll
    for (int u = 0; u < 2; ++u) dma16<AUX_SC1>(hsrc + r * 4096, hvo[u], lb + ((r + (h ? 0 : 3)) & 3) * 16384 + kq * 4096 + (dh * 2 + u) * 1024);
  };
  int woff[2];
#pragma unroll
  for (int i = 0; i < 2; ++i) woff[i] = kq * 4096 + rl * 128 + (((dh * 2 + i) ^ f3) << 5) + 8 * pp;
  auto w2_read = [&](int s, int lds) {
#pragma unroll
    for (int i = 0; i < 2; ++i) W2[s][i] = tr_frag(sm + lds + woff[i], 2048);
  };
  if (c_on) {
    const char* hsrc = ws + WS_HT + ((size_t)(w * 16) * S_LEN + rt * 64) * 2;
    auto load_ht = [&](int h, int j) {
      const int slot = h ? (j + 3) & 3 : j;
      dma16<AUX_SC1>(hsrc + (size_t)j * (128 * S_LEN * 2) + h * 2048, avo[0], lb + slot * 16384 + (w * 2) * 1024);
      dma16<AUX_SC1>(hsrc + (size_t)j * (128 * S_LEN * 2) + h * 2048 + 8 * S_LEN * 2, avo[0], lb + slot * 16384 + (w * 2 + 1) * 1024);
    };
    load_ht(0, 0); load_ht(0, 1);
    f32x4 csa[3];
#pragma unroll
    for (int i = 0; i < 3; ++i) csa[i] = (f32x4){0, 0, 0, 0};
#pragma unroll
    for (int c = 0; c < 8; ++c)
#pragma unroll
      for (int i = 0; i < 3; ++i) csa[i] = mfma16(ONES, W1[c][i], csa[i]);
    if (wm == 0 && g == 0) {
#pragma unroll
      for (int i = 0; i < 3; ++i) *(LAS float*)(sm + L_CS + (kp * 96 + wn * 48 + i * 16 + fr) * 4) = csa[i][0];
    }
    STAMP(7);
    int aoff[2];
#pragma unroll
    for (int i = 0; i < 2; ++i) aoff[i] = (kp * 32 + rl) * 128 + (((wm * 2 + (i ^ kp)) ^ f3) << 5) + 8 * pp;
    static_for<2>([&](auto HH) {
      constexpr int h = decltype(HH)::value;
      unsigned fl[4];
      f32x4 acc[2][3], sta[2];
#pragma unroll
      for (int i = 0; i < 2; ++i) {
        sta[i] = (f32x4){0, 0, 0, 0};
#pragma unroll
        for (int jn = 0; jn < 3; ++jn) acc[i][jn] = (f32x4){0, 0, 0, 0};
      }
      auto g1_loop = [&](auto WN) {
        bf16x8_t A[2][4];
        auto half = [&](bf16x8_t (&Af)[4], const bf16x8_t (&Wf)[3], int u) {
#pragma unroll
          for (int i = 0; i < 2; ++i) {
#pragma unroll
            for (int jn = 0; jn < 3; ++jn) acc[i][jn] = mfma16(Af[2 * u + i], Wf[jn], acc[i][jn]);
            sta[i] = mfma16(Af[2 * u + i], decltype(WN)::value ? Af[2 * u + i] : ONES, sta[i]);
          }
        };
#define G1_ITER(j, N, N0)                                                                                    \
        if ((N0) != (N) && w == 0) WAIT_V(N0); else WAIT_V(N);                                               \
        if (h == 1 && (j) == 3) LBAR(); else RAW_BAR();                                                      \
               \
        if ((j) == h && t == 0) __hip_atomic_store((gu32*)(ws + (h == 0 ? WS_FLB + 16384 : WS_FLC)) + b * FLAG_STRIDE, epoch, RLX_AGENT); \
        if ((j) == 0) { if (h == 0) STAMP(16); else STAMP(37); }                                             \
        _Pragma("unroll") for (int u = 0; u < 2; ++u)                                                        \
          _Pragma("unroll") for (int i = 0; i < 2; ++i)                                                      \
            A[(j) & 1][2 * u + i] = tr_frag(sm + aoff[i] + (h ? ((j) + 3) & 3 : (j)) * 16384 + u * 8192, 2048); \
        if (h == 1 && (j) >= 2) { w2_read(2 * (j) - 4, L_W2A + (2 * (j) - 4) * 16384); w2_read(2 * (j) - 3, L_W2A + (2 * (j) - 3) * 16384); } \
        if ((j) > 0) { half(A[((j) - 1) & 1], W1[(j) > 0 ? 2 * (j) - 2 : 0], 0); interleave_mfma_ds<8>(); }  \
        SCHED();                                                                                             \
        if (h == 1 && (j) == 3) { load_w2(5, L_W2A); load_w2(6, L_W2A + 16384); }                            \
        if ((j) + 2 < 4) { load_ht(h, (j) + 2); load_w2(3 * h + (j), L_W2A + (3 * h + (j)) * 16384); }       \
        if ((j) == 2) {                                                                                      \
          if (h == 0) {                                                                                      \
            if (w == 0) { _Pragma("unroll") for (int i = 0; i < 4; ++i) fl[i] = ld32_sc1(ws + WS_FLB + 16384, (lane + 64 * i) * (FLAG_STRIDE * 4)); } \
          } else {                                                                                           \
              \
            _Pragma("unroll") for (int v = 0; v < 2; ++v) {                                                  \
              int ks = kq * 11 + 5 + 3 * (w & 1) + v; ks = ks > 42 ? 42 : ks;                                \
              dma16<0>(p.w2 + (size_t)(ks * 32 + (jb >> 3) * 8) * D_DIM + d0, (lane >> 3) * (D_DIM * 2) + (lane & 7) * 16, lb + L_JUNKC); \
            }                                                                                                \
          }                                                                                                  \
        }                                                                                                    \
        SCHED();                                                                                             \
        if ((j) > 0) half(A[((j) - 1) & 1], W1[(j) > 0 ? 2 * (j) - 1 : 0], 1);                               \
        SCHED();
        if constexpr (h == 0) {
          G1_ITER(0, 2, 2) G1_ITER(1, 4, 5) G1_ITER(2, 6, 6) G1_ITER(3, 2, 6)
        } else {
          G1_ITER(0, 4, 4) G1_ITER(1, 4, 4) G1_ITER(2, 6, 7) G1_ITER(3, 4, 4)
        }
#undef G1_ITER
        half(A[1], W1[6], 0); half(A[1], W1[7], 1);
      };
      if (wn) g1_loop(std::integral_constant<bool, true>{}); else g1_loop(std::integral_constant<bool, false>{});
      if (h == 0) STAMP(9); else STAMP(38);
      constexpr int EX = h == 0 ? 7 * 16384 : L_EX1;
      if constexpr (h == 1) {
        WAIT_V(6);
        LBAR();
        if (w == 0) flc = ld32_sc1(ws + WS_FLC, flc_off);
        load_w2(7, L_W2A + 2 * 16384); load_w2(8, L_W2A + 3 * 16384);
        w2_read(4, L_W2A + 4 * 16384);
      }
      if (h == 0) STAMP(25); else STAMP(28);
      {
        LAS char* sp = sm + L_SST + ((kp * 2 + wn) * 64 + wm * 32) * 4;
        LAS char *s0 = sp + kp * 64, *s1 = sp + (kp ^ 1) * 64;
        if (wn == 0) {
          if (fr == 0) { *(LAS f32x4*)(s0 + g * 16) = sta[0]; *(LAS f32x4*)(s1 + g * 16) = sta[1]; }
        } else if (q_ == g) {
          *(LAS float*)(s0 + fr * 4) = pp == 0 ? sta[0][0] : (pp == 1 ? sta[0][1] : (pp == 2 ? sta[0][2] : sta[0][3]));
          *(LAS float*)(s1 + fr * 4) = pp == 0 ? sta[1][0] : (pp == 1 ? sta[1][1] : (pp == 2 ? sta[1][2] : sta[1][3]));
        }
      }
#pragma unroll
      for (int jn = 0; jn < 3; ++jn) *(LAS f32x4*)(sm + EX + w * 3072 + jn * 1024 + lane * 16) = acc[1][jn];
      if constexpr (h == 0) {
        if (w == 0) {
          WAIT_V(0);
          asm volatile("" : "+v"(fl[0]), "+v"(fl[1]), "+v"(fl[2]), "+v"(fl[3]));
          if (!__all((fl[0] == epoch) & (fl[1] == epoch) & (fl[2] == epoch) & (fl[3] == epoch))) wait_flags256(ws, WS_FLB + 16384, epoch, lane);
        }
      }
      LBAR();
      if (h == 0) STAMP(26); else STAMP(29);
      if constexpr (h == 0) {
        if (b == 0 && t == 0) __hip_atomic_store((gu32*)(ws + WS_EPOCH), epoch, RLX_AGENT);
        load_ht(1, 0);
      }
      if constexpr (h == 1) load_w2(9, L_W2A + 4 * 16384);
      if (t < 64) {
        const LAS float* sp = (const LAS float*)(sm + L_SST) + t;
        const float mean = (sp[0] + sp[128]) * (1.0f / D_DIM);
        const float var = fmaxf((sp[64] + sp[192]) * (1.0f / D_DIM) - mean * mean, 0.f);
        *(LAS float*)(sm + L_FST + t * 4) = -rbf(mean);
        *(LAS float*)(sm + L_FST + 256 + t * 4) = -1.44269504f * __builtin_amdgcn_rcpf(rbf(__builtin_amdgcn_sqrtf(rbf(rbf(var) + 1e-5f))));
      }
      f32x2 csv[3];
#pragma unroll
      for (int jn = 0; jn < 3; ++jn) {
        const int nl = wn * 48 + jn * 16 + fr;
        const float c = *(const LAS float*)(sm + L_CS + nl * 4) + *(const LAS float*)(sm + L_CS + (96 + nl) * 4);
        csv[jn] = (f32x2){c, c};
      }
#pragma unroll
      for (int jn = 0; jn < 3; ++jn) acc[0][jn] += *(const LAS f32x4*)(sm + EX + (w ^ 4) * 3072 + jn * 1024 + lane * 16);
      if constexpr (h == 1) {
        if (w == 0) {
          WAIT_V(6);
          asm volatile("" : "+v"(flc));
          if (!__all(flc == epoch)) poll_flc(0);
        }
      }
      LBAR();
      if (h == 0) STAMP(27); else STAMP(31 + 5);
      if constexpr (h == 0) load_ht(1, 1);
      {
        const int ms = wm * 2 + kp;
        const int ml = ms * 16 + g * 4;
        const f32x4 nmu = *(const LAS f32x4*)(sm + L_FST + ml * 4), zz = *(const LAS f32x4*)(sm + L_FST + 256 + ml * 4);
        const f32x2 nm0 = {nmu[0], nmu[1]}, nm1 = {nmu[2], nmu[3]}, z0 = {zz[0], zz[1]}, z1 = {zz[2], zz[3]};
        const f32x2 cL = {-1.44269504f, -1.44269504f};
#pragma unroll
        for (int jn = 0; jn < 3; ++jn) {
          if constexpr (h == 1) { SCHED(); load_h1(0, jn); SCHED(); }
          const f32x4 a = acc[0][jn];
          const f32x2 e0 = (nm0 * csv[jn] + (f32x2){a[0], a[1]}) * z0, e1 = (nm1 * csv[jn] + (f32x2){a[2], a[3]}) * z1;
          const f32x2 d0_ = (f32x2){__builtin_amdgcn_exp2f(e0[0]), __builtin_amdgcn_exp2f(e0[1])} * cL + cL;
          const f32x2 d1_ = (f32x2){__builtin_amdgcn_exp2f(e1[0]), __builtin_amdgcn_exp2f(e1[1])} * cL + cL;
          const f32x2 o0 = e0 * (f32x2){__builtin_amdgcn_rcpf(d0_[0]), __builtin_amdgcn_rcpf(d0_[1])};
          const f32x2 o1 = e1 * (f32x2){__builtin_amdgcn_rcpf(d1_[0]), __builtin_amdgcn_rcpf(d1_[1])};
          const int nl = wn * 48 + jn * 16 + fr;
          *(LAS u32x2*)(sm + L_OUT1 + nl * 128 + (((ms * 2 + (g >> 1)) ^ (nl & 7)) << 4) + (g & 1) * 8) = (u32x2){pk_bf16(o0[0], o0[1]), pk_bf16(o1[0], o1[1])};
        }
      }
      LBAR();
      if (h == 0) STAMP(21); else STAMP(39);
      if constexpr (h == 0) load_w2(2, L_W2A + 2 * 16384);
      if constexpr (h == 1) load_w2(10, L_W2X);
#pragma unroll
      for (int i = 0; i < 2; ++i) {
        const int pc = t + 512 * i;
        if (i == 0 || t < 256) {
          const int sbk = pc >= 384 ? 1 : 0, rem = pc - sbk * 384, nl = rem >> 2, c4 = rem & 3;
          const u32x4 o16 = *(const LAS u32x4*)(sm + L_OUT1 + nl * 128 + (((sbk * 4 + c4) ^ (nl & 7)) << 4));
          if (n0 + nl < F_DIM)
            __builtin_amdgcn_raw_buffer_store_b128(o16, rs, WS_H1B + (((h * 32 + rt * 2 + sbk) * F_DIM + n0 + nl) * 32 + c4 * 8) * 2, 0, AUX_SC1);
        }
      }
      WAIT_L0();
    });
  } else {
    WAIT_V(0);
    RAW_BAR();
    if (t == 0) __hip_atomic_store((gu32*)(ws + WS_FLB + 16384) + b * FLAG_STRIDE, epoch, RLX_AGENT);
    if (w == 0) wait_flags256(ws, WS_FLB + 16384, epoch, lane);
    RAW_BAR();
#pragma unroll
    for (int s = 0; s < 5; ++s) load_w2(s, L_W2A + s * 16384);
    WAIT_V(0);
    RAW_BAR();
#pragma unroll
    for (int s = 0; s < 5; ++s)
#pragma unroll
      for (int i = 0; i < 2; ++i) W2[s][i] = tr_frag(sm + L_W2A + woff[i] + s * 16384, 2048);
    LBAR();
#pragma unroll
    for (int s = 5; s < 10; ++s) load_w2(s, L_W2A + (s - 5) * 16384);
    if (w == 0) poll_flc(0);
    RAW_BAR();
    load_h1(0, 0); load_h1(0, 1); load_h1(0, 2);
    load_w2(10, L_W2X);
  }
  STAMP(22);
  {
    int boff[2];
#pragma unroll
    for (int i = 0; i < 2; ++i) boff[i] = kq * 4096 + rl * 64 + ((i ^ ((rl >> 2) & 1)) << 5) + 8 * pp;
    static_for<2>([&](auto HH) {
      constexpr int h = decltype(HH)::value;
      f32x4 acc[2][2];
#pragma unroll
      for (int i = 0; i < 2; ++i)
#pragma unroll
        for (int jm = 0; jm < 2; ++jm) acc[i][jm] = (f32x4){0, 0, 0, 0};
      bf16x8_t B[2][4];
      auto step2 = [&](const bf16x8_t (&Wf)[2], const bf16x8_t (&Bf)[4], int u) {
#pragma unroll
        for (int jm = 0; jm < 2; ++jm)
#pragma unroll
          for (int i = 0; i < 2; ++i) acc[i][jm] = mfma16(Wf[i], Bf[2 * u + jm], acc[i][jm]);
      };
#define G2_ITER(r, N, N0)                                                                                               \
      if ((N0) != (N) && w == 0 && c_on) WAIT_V(N0); else WAIT_V(N);          \
      LBAR();                                                                                                           \
      if ((r) == 0) { if (h == 0) STAMP(24); else STAMP(19); }                                                          \
      if ((r) == 1 && h == 0 && c_on && t == 0) __hip_atomic_store((gu32*)(ws + WS_FLC + 16384) + b * FLAG_STRIDE, epoch, RLX_AGENT); \
      _Pragma("unroll") for (int u = 0; u < 2; ++u)                                                                     \
        _Pragma("unroll") for (int i = 0; i < 2; ++i) B[(r) & 1][2 * u + i] = tr_frag(sm + boff[i] + (((r) + (h ? 0 : 3)) & 3) * 16384 + u * 2048, 1024); \
      if ((r) == 0 && h == 0) {                                                                                         \
        _Pragma("unroll") for (int s = 5; s < 10; ++s)                                                                  \
          _Pragma("unroll") for (int i = 0; i < 2; ++i) W2[s][i] = tr_frag(sm + L_W2A + woff[i] + (s - 5) * 16384, 2048); \
      }                                                                                                                 \
      if ((r) == 1 && h == 0) { _Pragma("unroll") for (int i = 0; i < 2; ++i) W2[10][i] = tr_frag(sm + L_W2X + woff[i], 2048); } \
      if ((r) > 0) { step2(W2[(r) > 0 ? 2 * (r) - 2 : 0], B[((r) - 1) & 1], 0);                                         \
        _Pragma("unroll") for (int i = 0; i < 4; ++i) { __builtin_amdgcn_sched_group_barrier(0x8, 1, 0); __builtin_amdgcn_sched_group_barrier(0x100, 2, 0); } } \
      SCHED();                                                                                                          \
      if ((r) + 3 <= 5) load_h1(h, (r) + 3);                                                                            \
      if ((r) == 1 && h == 0) {                                                                                         \
                          \
        const int rr_ = (w & 3) * 16 + (lane >> 2);                                                                     \
        dma16<AUX_SC1>(ws + WS_HT + ((size_t)(d0 + (w & 3) * 16) * S_LEN + (w >> 2) * 1024 + mt * 32) * 2,              \
                       (lane >> 2) * (S_LEN * 2) + (((lane & 3) ^ (2 * ((rr_ >> 2) & 1))) << 4), lb + L_RES + w * 1024); \
      }                                                                                                                 \
      if ((r) == 5 && h == 0) { if (w == 0) flc = ld32_sc1(ws + WS_FLC + 16384, flc_off); }                             \
      SCHED();                                                                                                          \
      if ((r) > 0) step2(W2[(r) > 0 ? 2 * (r) - 1 : 0], B[((r) - 1) & 1], 1);                                           \
      SCHED();
      if constexpr (h == 0) {
        G2_ITER(0, 6, 6) G2_ITER(1, 2, 2) G2_ITER(2, 63, 63) G2_ITER(3, 5, 6) G2_ITER(4, 3, 3) G2_ITER(5, 0, 0)
      } else {
        G2_ITER(0, 4, 4) G2_ITER(1, 4, 4) G2_ITER(2, 4, 4) G2_ITER(3, 4, 4) G2_ITER(4, 2, 2) G2_ITER(5, 0, 0)
      }
#undef G2_ITER
      if (kq < 3) step2(W2[10], B[1], 0);
      if (h == 0) STAMP(13); else STAMP(20);
      const int ds = dh * 2 + (kq >> 1), ms = kq & 1;
      const int rr = ds * 16 + rl;
      const u32x2 ru = __builtin_bit_cast(u32x2, tr_read(sm + L_RES + h * 4096 + rr * 64 + ((ms ^ ((rl >> 2) & 1)) << 5) + 8 * pp));
#pragma unroll
      for (int i = 0; i < 2; ++i)
#pragma unroll
        for (int jm = 0; jm < 2; ++jm) *(LAS f32x4*)(sm + L_EX2 + ((dh * 4 + i * 2 + jm) * 4 + kq) * 1024 + lane * 16) = acc[i][jm];
      if constexpr (h == 0) {
        if (w == 0) {
          WAIT_V(0);
          asm volatile("" : "+v"(flc));
          if (!__all(flc == epoch)) poll_flc(1);
        }
      }
      LBAR();
      if constexpr (h == 0) { load_h1(1, 0); load_h1(1, 1); load_h1(1, 2); STAMP(18); }
      const LAS char* e = sm + L_EX2 + ((dh * 4 + (kq >> 1) * 2 + ms) * 4) * 1024 + lane * 16;
      const f32x4 a = (*(const LAS f32x4*)(e) + *(const LAS f32x4*)(e + 1024)) + (*(const LAS f32x4*)(e + 2048) + *(const LAS f32x4*)(e + 3072));
      const u32x2 ov = {pk_bf16(bf_lo(ru[0]) + rbf(a[0]), bf_hi(ru[0]) + rbf(a[1])), pk_bf16(bf_lo(ru[1]) + rbf(a[2]), bf_hi(ru[1]) + rbf(a[3]))};
      *(u32x2*)(p.out + (size_t)(h * 1024 + mt * 32 + ms * 16 + fr) * D_DIM + d0 + ds * 16 + g * 4) = ov;
      if (h == 0) STAMP(14);
    });
  }
  asm volatile(".Lh3_code_end:");
  STAMP(23); CSTAMP(7);
}

extern "C" void kernel_launch(void* const* d_in, const int* in_sizes, int n_in, void* d_out, int out_size, void* d_ws, size_t ws_size,
                              hipStream_t stream) {
  Params p;
  p.q = (const u16*)d_in[0];
  p.k = (const u16*)d_in[1];
  p.v = (const u16*)d_in[2];
  p.shk = (const u16*)d_in[3];
  p.ssmk = (const u16*)d_in[4];
  p.ssmD = (const u16*)d_in[5];
  p.w1 = (const u16*)d_in[7];
  p.w2 = (const u16*)d_in[8];
  p.out = (u16*)d_out;
  p.ws = (char*)d_ws;
  (void)hipFuncSetAttribute((const void*)h3_fused, hipFuncAttributeMaxDynamicSharedMemorySize, LDS_BYTES);
  h3_fused<<<256, 512, LDS_BYTES, stream>>>(p);
}
```

```cpp
#include <hip/hip_runtime.h>
#include <hip/hip_bf16.h>
#include <stdint.h>
#include <type_traits>
#include <utility>

typedef unsigned short u16;
typedef short bf16x8 __attribute__((ext_vector_type(8)));
typedef short s16x4 __attribute__((ext_vector_type(4)));
typedef float f32x4 __attribute__((ext_vector_type(4)));
typedef float f32x2 __attribute__((ext_vector_type(2)));
typedef unsigned u32x4 __attribute__((ext_vector_type(4)));
typedef unsigned u32x2 __attribute__((ext_vector_type(2)));
typedef __bf16 bf16x2_t __attribute__((ext_vector_type(2)));
typedef __bf16 bf16x8_t __attribute__((ext_vector_type(8)));
typedef short v4i16_t __attribute__((ext_vector_type(4)));
#define LAS __attribute__((address_space(3)))
#define GAS __attribute__((address_space(1)))
typedef GAS unsigned gu32;
#define RLX_AGENT __ATOMIC_RELAXED, __HIP_MEMORY_SCOPE_AGENT

constexpr int S_LEN = 2048, D_DIM = 512, F_DIM = 1376;
constexpr int AUX_SC1 = 16;

constexpr unsigned WS_KVT = 0, WS_QT = 2u << 20, WS_HT = 4u << 20, WS_H1B = 6u << 20, WS_STQ = 12u << 20;
constexpr unsigned WS_FL1 = WS_STQ + 65536, WS_FLB = WS_FL1 + 16384, WS_FLC = WS_FLB + 2 * 16384, WS_EPOCH = WS_FLC + 2 * 16384;
constexpr unsigned WS_STAMP = WS_EPOCH + 4096;
constexpr unsigned WS_BYTES = 16u << 20;
constexpr int FLAG_STRIDE = 16;
constexpr unsigned SPIN_CAP = 1u << 18;

#define WAIT_V(n) asm volatile("s_waitcnt vmcnt(%0)" ::"n"(n) : "memory")
#define WAIT_L0() asm volatile("s_waitcnt lgkmcnt(0)" ::: "memory")
#define RAW_BAR() do { asm volatile("" ::: "memory"); __builtin_amdgcn_s_barrier(); asm volatile("" ::: "memory"); } while (0)
#define LBAR() do { WAIT_L0(); RAW_BAR(); } while (0)
#define SCHED() __builtin_amdgcn_sched_barrier(0)
template <int N> __device__ __forceinline__ void interleave_mfma_ds() {
#pragma unroll
  for (int i = 0; i < N; ++i) { __builtin_amdgcn_sched_group_barrier(0x8, 1, 0); __builtin_amdgcn_sched_group_barrier(0x100, 1, 0); }
}

#define STAMP(i)
#define CSTAMP(i)

__device__ __forceinline__ float bf_lo(unsigned x) { return __uint_as_float(x << 16); }
__device__ __forceinline__ float bf_hi(unsigned x) { return __uint_as_float(x & 0xffff0000u); }
__device__ __forceinline__ unsigned pk_bf16(float lo, float hi) {
  bf16x2_t v; v.x = (__bf16)lo; v.y = (__bf16)hi;
  return __builtin_bit_cast(unsigned, v);
}
__device__ __forceinline__ float rbf(float x) { return __uint_as_float(((unsigned)__builtin_bit_cast(u16, (__bf16)x)) << 16); }
__device__ __forceinline__ s16x4 tr_read(const LAS char* p) {
  return __builtin_bit_cast(s16x4, __builtin_amdgcn_ds_read_tr16_b64_v4i16((LAS v4i16_t*)p));
}
__device__ __forceinline__ bf16x8_t cat8(s16x4 a, s16x4 b) {
  bf16x8 r = __builtin_shufflevector(a, b, 0, 1, 2, 3, 4, 5, 6, 7);
  return __builtin_bit_cast(bf16x8_t, r);
}
__device__ __forceinline__ bf16x8_t tr_frag(const LAS char* p, int second) { return cat8(tr_read(p), tr_read(p + second)); }
__device__ __forceinline__ f32x4 mfma16(bf16x8_t a, bf16x8_t b, f32x4 c) {
  return __builtin_amdgcn_mfma_f32_16x16x32_bf16(a, b, c, 0, 0, 0);
}
template <int AUX>
__device__ __forceinline__ void dma16(const void* sbase, unsigned voff, unsigned lds) {
  if constexpr (AUX == AUX_SC1)
    asm volatile("s_mov_b32 m0, %2\n\ts_nop 0\n\tglobal_load_lds_dwordx4 %0, %1 sc1" ::"v"(voff), "s"(sbase), "s"(lds) : "memory");
  else
    asm volatile("s_mov_b32 m0, %2\n\ts_nop 0\n\tglobal_load_lds_dwordx4 %0, %1" ::"v"(voff), "s"(sbase), "s"(lds) : "memory");
}
template <int AUX, unsigned LC, unsigned SC>
__device__ __forceinline__ void dma16b(u32x4 srd, unsigned voff, unsigned ldsw) {
  unsigned tmp;
  if constexpr (AUX == AUX_SC1)
    asm volatile("s_add_u32 m0, %3, %4\n\ts_mov_b32 %0, %5\n\tbuffer_load_dwordx4 %1, %2, %0 offen sc1 lds"
                 : "=&s"(tmp) : "v"(voff), "s"(srd), "s"(ldsw), "n"(LC), "n"(SC) : "memory", "scc");
  else
    asm volatile("s_add_u32 m0, %3, %4\n\ts_mov_b32 %0, %5\n\tbuffer_load_dwordx4 %1, %2, %0 offen lds"
                 : "=&s"(tmp) : "v"(voff), "s"(srd), "s"(ldsw), "n"(LC), "n"(SC) : "memory", "scc");
}
__device__ __forceinline__ u32x4 make_srd(const void* p, unsigned bytes) {
  const unsigned long long a = (unsigned long long)p;
  return (u32x4){(unsigned)a, (unsigned)(a >> 32) & 0xffffu, bytes, 0x00020000u};
}
template <int V> using IC = std::integral_constant<int, V>;
__device__ __forceinline__ void dma4(const void* sbase, unsigned voff, unsigned lds) {
  asm volatile("s_mov_b32 m0, %2\n\ts_nop 0\n\tglobal_load_lds_dword %0, %1" ::"v"(voff), "s"(sbase), "s"(lds) : "memory");
}
__device__ __forceinline__ void dma4v(const void* vaddr, unsigned lds) {
  asm volatile("s_mov_b32 m0, %1\n\ts_nop 0\n\tglobal_load_lds_dword %0, off" ::"v"(vaddr), "s"(lds) : "memory");
}
__device__ __forceinline__ u32x4 ld128_asm(const void* sbase, unsigned voff) {
  u32x4 r; asm volatile("global_load_dwordx4 %0, %1, %2" : "=v"(r) : "v"(voff), "s"(sbase) : "memory"); return r;
}
__device__ __forceinline__ u32x4 ld128_sc1(const void* sbase, unsigned voff) {
  u32x4 r; asm volatile("global_load_dwordx4 %0, %1, %2 sc1" : "=v"(r) : "v"(voff), "s"(sbase) : "memory"); return r;
}
__device__ __forceinline__ unsigned ld32_sc1(const void* sbase, unsigned voff) {
  unsigned r; asm volatile("global_load_dword %0, %1, %2 sc1" : "=v"(r) : "v"(voff), "s"(sbase) : "memory"); return r;
}
__device__ __forceinline__ unsigned ld16_asm(const void* sbase, unsigned voff) {
  unsigned r; asm volatile("global_load_ushort %0, %1, %2" : "=v"(r) : "v"(voff), "s"(sbase) : "memory"); return r;
}
__device__ __forceinline__ u32x2 ld64_asm(const void* sbase, unsigned voff) {
  u32x2 r; asm volatile("global_load_dwordx2 %0, %1, %2" : "=v"(r) : "v"(voff), "s"(sbase) : "memory"); return r;
}
__device__ __forceinline__ u32x2 ld64_sc1(const void* sbase, unsigned voff) {
  u32x2 r; asm volatile("global_load_dwordx2 %0, %1, %2 sc1" : "=v"(r) : "v"(voff), "s"(sbase) : "memory"); return r;
}
template <int CTRL>
__device__ __forceinline__ float dpp_add(float x) {
  return x + __builtin_bit_cast(float, __builtin_amdgcn_mov_dpp(__builtin_bit_cast(int, x), CTRL, 0xf, 0xf, true));
}
__device__ __forceinline__ float wave_sum(float x) {
  x = dpp_add<0xB1>(x); x = dpp_add<0x4E>(x); x = dpp_add<0x141>(x); x = dpp_add<0x140>(x);
  auto s = __builtin_amdgcn_permlane16_swap(__float_as_uint(x), __float_as_uint(x), false, false);
  x = __uint_as_float(s[0]) + __uint_as_float(s[1]);
  auto u = __builtin_amdgcn_permlane32_swap(__float_as_uint(x), __float_as_uint(x), false, false);
  return __uint_as_float(u[0]) + __uint_as_float(u[1]);
}
__device__ __forceinline__ u32x4 widen16(u32x2 X, u32x2 Y) {
  auto a = __builtin_amdgcn_permlane16_swap(X[0], Y[0], false, false);
  auto b = __builtin_amdgcn_permlane16_swap(X[1], Y[1], false, false);
  return (u32x4){a[0], b[0], a[1], b[1]};
}

struct Params {
  const u16 *q, *k, *v, *shk, *ssmk, *ssmD, *w1, *w2;
  u16* out;
  char* ws;
};

__device__ __forceinline__ void wait_flags256(const char* ws, unsigned off, unsigned epoch, int lane) {
  gu32* f = (gu32*)(ws + off) + lane * FLAG_STRIDE;
#pragma unroll 1
  for (unsigned spins = 0; spins < SPIN_CAP; ++spins) {
    unsigned a = __hip_atomic_load(f, RLX_AGENT), b = __hip_atomic_load(f + 64 * FLAG_STRIDE, RLX_AGENT);
    unsigned c = __hip_atomic_load(f + 128 * FLAG_STRIDE, RLX_AGENT), d = __hip_atomic_load(f + 192 * FLAG_STRIDE, RLX_AGENT);
    if (__all((a == epoch) & (b == epoch) & (c == epoch) & (d == epoch))) break;
    __builtin_amdgcn_s_sleep(1);
  }
  __builtin_amdgcn_fence(__ATOMIC_ACQUIRE, "wavefront");
}

constexpr int L_KT = 32768, L_VT = L_KT + 72 * 128, L_QT = L_VT + 8192, L_OKV = L_QT + 8192, L_OQ = L_OKV + 8192, L_SS = L_OQ + 8192;
constexpr int H_ROW = 1152, H_COPY = 4728, H_BYTES = 9344, X_BYTES = 4160;
constexpr int L_H = 0, L_X = L_H + 2 * H_BYTES, L_QD = L_X + 2 * X_BYTES;
constexpr int CY_BYTES = 257 * 16;
constexpr int L_CY = L_QD + 8192;
constexpr int L_JUNKA = 76800;
constexpr int L_W1 = 77824;
constexpr int L_W1B = 51712;
static_assert(L_CY + 4 * CY_BYTES <= L_W1B && L_W1B + 12288 <= L_JUNKA, "lds");
static_assert(L_CY + 4 * CY_BYTES <= L_JUNKA && L_JUNKA + 1024 <= L_W1 && L_W1 + 7 * 12288 <= 163840 && L_SS + 64 <= L_JUNKA, "lds");
static_assert(L_X + 2 * X_BYTES <= L_KT, "lds A/B overlap");
constexpr int L_W2A = 4 * 16384;
constexpr int L_CS = 147456;
constexpr int L_SST = L_CS + 1024;
constexpr int L_FST = L_SST + 2560;
constexpr int L_JUNKC = 159744;
constexpr int L_EX1 = 0;
constexpr int L_OUT1 = 2 * 16384;
static_assert(L_JUNKC + 1024 <= 163840, "lds");
constexpr int L_W2X = 9 * 16384;
constexpr int L_RES = 4 * 16384;
constexpr int L_EX2 = 5 * 16384;
constexpr int LDS_BYTES = 163840;

template <class F, int... I>
__device__ __forceinline__ void static_for_impl(F&& f, std::integer_sequence<int, I...>) { (f(std::integral_constant<int, I>{}), ...); }
template <int N, class F>
__device__ __forceinline__ void static_for(F&& f) { static_for_impl(f, std::make_integer_sequence<int, N>{}); }

template <int TS, int PAR, int H, class F>
__device__ __forceinline__ void conv_pass(const LAS char* xa, const LAS char* hb, bf16x8_t (&A)[8], f32x4 (&acc)[2][4], F&& job) {
  constexpr int U0 = H ? (TS ? 5 : 4) : (TS ? 1 : 0), U1 = H ? (TS ? 6 : 7) : (TS ? 2 : 3);
  constexpr int T[2] = {PAR ? U1 : U0, PAR ? U0 : U1};
  constexpr int NA = U1 + 1, A0 = H ? (TS ? 3 : 4) : 0;
  bf16x8_t B[3][4];
#pragma unroll
  for (int p = 0; p < 4; ++p) B[0][p] = *(const LAS bf16x8_t*)(hb + p * H_ROW + 896);
#pragma unroll
  for (int a = NA - 1; a >= A0; --a) A[a] = tr_frag(xa + 512 * a, 32);
#pragma unroll
  for (int p = 0; p < 4; ++p) B[1][p] = *(const LAS bf16x8_t*)(hb + p * H_ROW + 896 - 128);
#pragma unroll
  for (int j = 0; j < 2; ++j)
#pragma unroll
    for (int p = 0; p < 4; ++p) acc[j][p] = (f32x4){0, 0, 0, 0};
  SCHED();
  static_for<NA>([&](auto BB) {
    constexpr int bb = decltype(BB)::value;
    if (bb + 2 < NA) {
#pragma unroll
      for (int p = 0; p < 4; ++p) B[(bb + 2) % 3][p] = *(const LAS bf16x8_t*)(hb + p * H_ROW + 896 - 128 * (bb + 2));
    }
#pragma unroll
    for (int p = 0; p < 4; ++p)
#pragma unroll
      for (int j = 0; j < 2; ++j)
        if (T[j] >= bb) acc[j][p] = mfma16(A[T[j] - bb], B[bb % 3][p], acc[j][p]);
    if (bb + 2 < NA) interleave_mfma_ds<4>();
    SCHED();
    job(std::integral_constant<int, H>{}, BB);
    SCHED();
  });
}

__global__ __launch_bounds__(512, 2) void h3_fused(Params p) {
  extern __shared__ __attribute__((aligned(16))) char smem_[];
  LAS char* sm = (LAS char*)smem_;
  const unsigned lb = (unsigned)(size_t)sm;
  const int b = blockIdx.x, t = threadIdx.x, lane = t & 63;
  const int w = __builtin_amdgcn_readfirstlane(t >> 6);
  const int x = b & 7, jb = b >> 3;
  const int g = lane >> 4, fr = lane & 15, q_ = fr >> 2, pp = lane & 3;
  const int rl = 4 * g + q_;
  char* ws = p.ws;
  const __amdgpu_buffer_rsrc_t rs = __builtin_amdgcn_make_buffer_rsrc(ws, 0, WS_BYTES, 0x00020000);
  STAMP(0); CSTAMP(0);

  const int dB = x * 64 + jb * 2;
  const int cb = w & 3, sl = 8 * (4 * (w >> 2) + g);
  const unsigned tvo = (lane >> 3) * (D_DIM * 2) + (lane & 7) * 16;
  const size_t tso = ((size_t)(jb * 64 + w * 8) * D_DIM + x * 64);
  dma16<0>(p.k + tso, tvo, lb + L_KT + 1024 + w * 1024);
  dma16<0>(p.v + tso, tvo, lb + L_VT + w * 1024);
  if (w == 0) {
    if (jb > 0) dma16<0>(p.k + tso - 8 * D_DIM, tvo, lb + L_KT);
    else *(LAS u32x4*)(sm + L_KT + lane * 16) = (u32x4){0, 0, 0, 0};
  }
  u32x2 wv = ld64_asm(p.shk + (size_t)(x * 64 + cb * 16) * 4, fr * 8);
  STAMP(32);
  const bool c_on = jb < 30;
  const int panel = x * 2 + (jb >= 15 ? 1 : 0), ct = jb >= 15 ? jb - 15 : jb;
  const int m0 = panel * 128, n0 = ct * 96;
  unsigned w1o[6];
#pragma unroll
  for (int u = 0; u < 6; ++u) {
    const int idx = ((w & 1) * 6 + u) * 64 + lane, r = (idx * 43691) >> 19, cp = idx - r * 12;
    int col = n0 + (cp ^ (2 * ((r >> 2) & 1))) * 8;
    col = col > F_DIM - 8 ? F_DIM - 8 : col;
    w1o[u] = (r * F_DIM + col) * 2;
  }
  auto w1_half = [&](int chunk, int ldst, int step) {
#pragma unroll
    for (int u = 0; u < 6; ++u) dma16<0>(p.w1 + (size_t)chunk * 64 * F_DIM, w1o[u], lb + ldst + ((w & 1) * 6 + u) * step);
  };
  STAMP(33);
  __builtin_amdgcn_s_sleep(8);
  dma16<0>(p.q + tso, tvo, lb + L_QT + w * 1024);
  u32x4 sq = ld128_asm(p.q + (size_t)(b * 8 + w) * D_DIM, lane * 16);
  unsigned ep0 = ld32_sc1(ws + WS_EPOCH, 0);
  unsigned dv0 = ld16_asm(p.ssmD + dB + (w >> 2), 0);
  if (w == 7) {
    unsigned long long pc; unsigned len;
    asm volatile("s_getpc_b64 %0\n.Lh3_ref_%=:\n\ts_mov_b32 %1, .Lh3_code_end-.Lh3_ref_%=" : "=s"(pc), "=s"(len));
    const unsigned nl = len >> 7;
    unsigned line = (unsigned)(jb * 8 + lane); line = line >= nl ? line - nl : line; line = line >= nl ? 0 : line;
    dma4((const void*)pc, line << 7, lb + L_JUNKA);
  }
  if (w == 6) {
    const char* a = ws + (size_t)(jb * 8 + (lane & 7)) * 65536;
    if (lane >= 8) a = (const char*)p.w1 + jb * 44032;
    if (lane >= 16) a = (const char*)p.w2 + jb * 44032;
    if (lane >= 24) a = (const char*)p.out + jb * 65536;
    if (lane >= 32) a = (const char*)p.ssmk + jb * 65536;
    dma4v(a, lb + L_JUNKA + 256);
  }
  if (w >= 6) WAIT_V(5); else WAIT_V(4);
  asm volatile("" : "+v"(wv));
  LBAR();
  STAMP(1);
  const int tro = q_ * 128 + cb * 32 + 8 * pp + sl * 128;
  const int oo = (cb * 16 + fr) * 128 + (((sl >> 3) ^ (fr & 7)) << 4);
  {
    const u32x2 k0 = __builtin_bit_cast(u32x2, tr_read(sm + L_KT + 4 * 128 + tro));
    const u32x2 k1 = __builtin_bit_cast(u32x2, tr_read(sm + L_KT + 8 * 128 + tro));
    const u32x2 k2 = __builtin_bit_cast(u32x2, tr_read(sm + L_KT + 12 * 128 + tro));
    const u32x2 v0 = __builtin_bit_cast(u32x2, tr_read(sm + L_VT + tro));
    const u32x2 v1 = __builtin_bit_cast(u32x2, tr_read(sm + L_VT + 4 * 128 + tro));
    const float w0 = bf_lo(wv[0]), w1 = bf_hi(wv[0]), w2 = bf_lo(wv[1]), w3 = bf_hi(wv[1]);
    const float kx[11] = {bf_hi(k0[0]), bf_lo(k0[1]), bf_hi(k0[1]), bf_lo(k1[0]), bf_hi(k1[0]), bf_lo(k1[1]), bf_hi(k1[1]),
                          bf_lo(k2[0]), bf_hi(k2[0]), bf_lo(k2[1]), bf_hi(k2[1])};
    const float vx[8] = {bf_lo(v0[0]), bf_hi(v0[0]), bf_lo(v0[1]), bf_hi(v0[1]), bf_lo(v1[0]), bf_hi(v1[0]), bf_lo(v1[1]), bf_hi(v1[1])};
    float o[8];
#pragma unroll
    for (int e = 0; e < 8; ++e) o[e] = (kx[e] * w3 + kx[e + 1] * w2 + kx[e + 2] * w1 + kx[e + 3] * w0) * vx[e];
    *(LAS u32x4*)(sm + L_OKV + oo) = (u32x4){pk_bf16(o[0], o[1]), pk_bf16(o[2], o[3]), pk_bf16(o[4], o[5]), pk_bf16(o[6], o[7])};
  }
  LBAR();
  STAMP(15);
  const int toff = (t >> 3) * 128 + (((t & 7) ^ ((t >> 3) & 7)) << 4);
  const unsigned tgo = ((x * 64 + (t >> 3)) * S_LEN + jb * 64 + (t & 7) * 8) * 2;
  __builtin_amdgcn_raw_buffer_store_b128(*(const LAS u32x4*)(sm + L_OKV + toff), rs, WS_KVT + tgo, 0, AUX_SC1);
  WAIT_V(1);
  asm volatile("" : "+v"(sq), "+v"(ep0), "+v"(dv0));
  RAW_BAR();
  const unsigned epoch = (unsigned)__builtin_amdgcn_readfirstlane(ep0) + 1u;
  const float Dv = bf_lo(dv0);
  {
    const u32x2 q0 = __builtin_bit_cast(u32x2, tr_read(sm + L_QT + tro));
    const u32x2 q1 = __builtin_bit_cast(u32x2, tr_read(sm + L_QT + 4 * 128 + tro));
    f32x2 xs[4];
#pragma unroll
    for (int i = 0; i < 4; ++i) xs[i] = (f32x2){bf_lo(sq[i]), bf_hi(sq[i])};
    const f32x2 s2 = (xs[0] + xs[1]) + (xs[2] + xs[3]);
    const float mean = wave_sum(s2[0] + s2[1]) * (1.0f / D_DIM);
    *(LAS u32x4*)(sm + L_OQ + oo) = (u32x4){q0[0], q0[1], q1[0], q1[1]};
    f32x2 vs = {0.f, 0.f};
#pragma unroll
    for (int i = 0; i < 4; ++i) { const f32x2 dd = xs[i] - mean; vs += dd * dd; }
    const float var = rbf(wave_sum(vs[0] + vs[1]) * (1.0f / D_DIM));
    if (lane == 0) *(LAS unsigned*)(sm + L_SS + w * 4) = pk_bf16(mean, __builtin_amdgcn_sqrtf(rbf(var + 1e-5f)));
  }
  LBAR();
  __builtin_amdgcn_raw_buffer_store_b128(*(const LAS u32x4*)(sm + L_OQ + toff), rs, WS_QT + tgo, 0, AUX_SC1);
  if (t < 2) __builtin_amdgcn_raw_buffer_store_b128(*(const LAS u32x4*)(sm + L_SS + t * 16), rs, WS_STQ + b * 32 + t * 16, 0, AUX_SC1);
  STAMP(31);
  WAIT_V(0);
  LBAR();
  if (t == 0) __hip_atomic_store((gu32*)(ws + WS_FL1) + b * FLAG_STRIDE, epoch, RLX_AGENT);
  RAW_BAR();
  STAMP(2);
  {
    const int hc = t >> 8, hw = t & 255;
    u32x4 hv = ld128_asm(p.ssmk + (size_t)dB * S_LEN, (hc * S_LEN + hw * 8) * 2);
    const unsigned o1 = min((unsigned)(b * 5504 + (w < 6 ? w : 5) * 1024), (unsigned)(D_DIM * F_DIM * 2 - 1024));
    dma16<0>((const char*)p.w1 + o1, lane * 16, lb + L_JUNKA);
    WAIT_V(1);
    asm volatile("" : "+v"(hv));
    LAS char* hb = sm + L_H + hc * H_BYTES + 1020 - 4 * hw;
    const unsigned h0 = __builtin_amdgcn_perm(hv[1], hv[3], 0x07060302u), h1 = __builtin_amdgcn_perm(hv[1], hv[3], 0x05040100u);
    const unsigned h2 = __builtin_amdgcn_perm(hv[0], hv[2], 0x07060302u), h3 = __builtin_amdgcn_perm(hv[0], hv[2], 0x05040100u);
    *(LAS unsigned*)(hb + 0 * H_ROW) = h0; *(LAS unsigned*)(hb + H_COPY + 0 * H_ROW) = h0;
    *(LAS unsigned*)(hb + 1 * H_ROW) = h1; *(LAS unsigned*)(hb + H_COPY + 1 * H_ROW) = h1;
    *(LAS unsigned*)(hb + 2 * H_ROW) = h2; *(LAS unsigned*)(hb + H_COPY + 2 * H_ROW) = h2;
    *(LAS unsigned*)(hb + 3 * H_ROW) = h3; *(LAS unsigned*)(hb + H_COPY + 3 * H_ROW) = h3;
    if (t < 256) {
      const int r = t >> 4;
      *(LAS u32x2*)(sm + L_H + (r >> 3) * H_BYTES + ((r >> 2) & 1) * H_COPY + (r & 3) * H_ROW + 1024 + (t & 15) * 8) = (u32x2){0, 0};
    } else if (t < 260) *(LAS u32x4*)(sm + L_X + ((t - 256) >> 1) * X_BYTES + (t & 1) * 16) = (u32x4){0, 0, 0, 0};
    else if (t < 264) *(LAS u32x4*)(sm + L_CY + (t - 260) * CY_BYTES) = (u32x4){0, 0, 0, 0};
  }

  const int c2 = w >> 2, ts = (w >> 1) & 1, par = w & 1;
  const int u00 = ts ? 1 : 0, u01 = ts ? 2 : 3, u10 = ts ? 5 : 4, u11 = ts ? 6 : 7;
  const int tf0 = par ? u01 : u00, tg0 = par ? u00 : u01, tf1 = par ? u11 : u10, tg1 = par ? u10 : u11;
  const int sa = tf0 * 256 + fr * 16 + g * 4, sb = tf1 * 256 + fr * 16 + g * 4;
  const int kp = w >> 2, wm = (w >> 1) & 1, wn = w & 1;
  const bf16x8_t ONES = __builtin_bit_cast(bf16x8_t, (u32x4){0x3f803f80u, 0x3f803f80u, 0x3f803f80u, 0x3f803f80u});
  bf16x8_t W1[8][3];
  int wo[3];
#pragma unroll
  for (int i = 0; i < 3; ++i) wo[i] = (kp * 32 + rl) * 192 + 8 * pp + (((wn * 3 + i) ^ ((rl >> 2) & 1)) << 5);
  if (w == 0) wait_flags256(ws, WS_FL1, epoch, lane);
  LBAR();
  STAMP(3);
  {
    const int d = dB + c2;
    dma16<AUX_SC1>(ws + WS_KVT + ((size_t)d * S_LEN + (w & 3) * 512) * 2, lane * 16, lb + L_X + c2 * X_BYTES + 32 + (w & 3) * 1024);
    u32x2 qv0 = ld64_sc1(ws + WS_QT + (size_t)d * S_LEN * 2, sa * 2), qv1 = ld64_sc1(ws + WS_QT + (size_t)d * S_LEN * 2, sb * 2);
    u32x4 sv0 = ld128_sc1(ws + WS_STQ, sa * 4), sv1 = ld128_sc1(ws + WS_STQ, sb * 4);
    WAIT_V(4);
    RAW_BAR();
    STAMP(4); CSTAMP(1);
    f32x4 acc[2][4];
    bf16x8_t A[8];
    const LAS char* xa = sm + L_X + c2 * X_BYTES + 8 + 64 * g + 8 * q_ + 8 * pp + 256 * par;
    const LAS char* hb = sm + L_H + c2 * H_BYTES + ((fr & 1) ? 0 : H_COPY) + 1016 - 8 * fr + 16 * g + 64 * par - 896;
    const int prev = g > 0 ? lane - 16 : (fr > 0 ? 47 + fr : -1);
    unsigned fl[4];
    auto w1_read = [&](auto CC) {
      constexpr int c = decltype(CC)::value;
#pragma unroll
      for (int i = 0; i < 3; ++i) W1[c][i] = tr_frag(sm + (c < 7 ? L_W1 + c * 12288 : L_W1B) + wo[i], 16 * 192);
    };
    auto w1_third = [&](int ch, int j3) {
      const int c = ch + (w >> 2), u = ((w >> 1) & 1) * 3 + j3;
      dma16<0>(p.w1 + (size_t)c * 64 * F_DIM, (w & 2) ? w1o[3 + j3] : w1o[j3], lb + (c < 7 ? L_W1 + c * 12288 : L_W1B) + ((w & 1) * 6 + u) * 1024);
    };
    auto job = [&](auto HH, auto BB) {
      constexpr int h = decltype(HH)::value, bb = decltype(BB)::value;
      if constexpr (h == 0) {
        if constexpr (bb < 3) w1_third(0, bb);
      } else {
        if constexpr (bb == 0) {
          int r2 = ((b * 43) >> 3) + w; r2 = r2 > F_DIM - 1 ? F_DIM - 1 : r2;
          dma16<0>(p.w2 + (size_t)r2 * D_DIM, lane * 16, lb + L_JUNKA);
        }
        if constexpr (bb == 2) {
          WAIT_V(5);
          RAW_BAR();
          if (t == 0) __hip_atomic_store((gu32*)(ws + WS_FLB) + b * FLAG_STRIDE, epoch, RLX_AGENT);
          if (t >= 64 && t < 68) {
            LAS char* c = sm + L_CY + (t - 64) * CY_BYTES;
            *(LAS f32x4*)c = *(const LAS f32x4*)(c + 256 * 16);
          }
        }
        if constexpr (bb < 3) {
          const int ch = 2 + (w >> 1);
#pragma unroll
          for (int u = 2 * bb; u < 2 * bb + 2; ++u) dma16<0>(p.w1 + (size_t)ch * 64 * F_DIM, w1o[u], lb + L_W1 + ch * 12288 + ((w & 1) * 6 + u) * 1024);
        }
        if constexpr (bb == 3) { w1_third(6, 0); w1_third(6, 1); w1_read(std::integral_constant<int, 0>{}); }
        if constexpr (bb == 4) { w1_third(6, 2); w1_read(std::integral_constant<int, 1>{}); }
        if constexpr (bb == 5) {
          WAIT_V(3);
          RAW_BAR();
          if (w == 0) {
#pragma unroll
            for (int i = 0; i < 4; ++i) fl[i] = ld32_sc1(ws + WS_FLB, (lane + 64 * i) * (FLAG_STRIDE * 4));
          }
          w1_read(std::integral_constant<int, 2>{}); w1_read(std::integral_constant<int, 3>{});
        }
        if constexpr (bb == 6) { w1_read(std::integral_constant<int, 4>{}); w1_read(std::integral_constant<int, 5>{}); }
      }
    };
    auto epilogue = [&](int tf, int tg, int s0, u32x2 qv, u32x4 sv, auto LAST) {
#pragma unroll
      for (int j = 0; j < 2; ++j) {
        acc[j][3][1] += acc[j][2][0];
        acc[j][3][2] += acc[j][2][1] + acc[j][1][0];
        acc[j][3][3] += (acc[j][2][2] + acc[j][1][1]) + acc[j][0][0];
        acc[j][0][1] += acc[j][2][3] + acc[j][1][2];
        acc[j][0][2] += acc[j][1][3];
      }
      LAS char* cy = sm + L_CY + (c2 * 2 + par) * CY_BYTES + 16 + lane * 16;
      *(LAS f32x4*)(cy + (tf & 3) * 1024) = acc[0][0];
      *(LAS f32x4*)(cy + (tg & 3) * 1024) = acc[1][0];
      *(LAS f32x4*)(sm + L_QD + w * 1024 + lane * 16) = acc[1][3];
      const u32x2 xv = *(const LAS u32x2*)(sm + L_X + c2 * X_BYTES + 32 + s0 * 2);
      if constexpr (decltype(LAST)::value) {
        WAIT_V(0);
        if (w == 0) {
          asm volatile("" : "+v"(fl[0]), "+v"(fl[1]), "+v"(fl[2]), "+v"(fl[3]));
          if (!__all((fl[0] == epoch) & (fl[1] == epoch) & (fl[2] == epoch) & (fl[3] == epoch))) wait_flags256(ws, WS_FLB, epoch, lane);
        }
      }
      LBAR();
      const LAS char* cy0 = sm + L_CY + (c2 * 2) * CY_BYTES + 16 + prev * 16 + (tf & 3) * 1024;
      const f32x4 pq = *(const LAS f32x4*)(sm + L_QD + (w ^ 1) * 1024 + lane * 16);
      const f32x4 ca = *(const LAS f32x4*)(cy0), cb_ = *(const LAS f32x4*)(cy0 + CY_BYTES);
      f32x4 y = acc[0][3] + pq;
      y[0] += ca[1] + cb_[1]; y[1] += ca[2] + cb_[2]; y[2] += ca[3] + cb_[3];
      const float xx[4] = {bf_lo(xv[0]), bf_hi(xv[0]), bf_lo(xv[1]), bf_hi(xv[1])};
      const float qx[4] = {bf_lo(qv[0]), bf_hi(qv[0]), bf_lo(qv[1]), bf_hi(qv[1])};
      float o[4];
#pragma unroll
      for (int e = 0; e < 4; ++e) o[e] = qx[e] + ((qx[e] - bf_lo(sv[e])) * __builtin_amdgcn_rcpf(bf_hi(sv[e]))) * (y[e] + xx[e] * Dv);
      __builtin_amdgcn_raw_buffer_store_b64((u32x2){pk_bf16(o[0], o[1]), pk_bf16(o[2], o[3])}, rs, WS_HT + (d * S_LEN + s0) * 2, 0, AUX_SC1);
    };
    if (ts == 0) { if (par == 0) conv_pass<0, 0, 0>(xa, hb, A, acc, job); else conv_pass<0, 1, 0>(xa, hb, A, acc, job); }
    else         { if (par == 0) conv_pass<1, 0, 0>(xa, hb, A, acc, job); else conv_pass<1, 1, 0>(xa, hb, A, acc, job); }
    STAMP(34);
    WAIT_V(3);
    asm volatile("" : "+v"(qv0), "+v"(qv1), "+v"(sv0), "+v"(sv1));
    epilogue(tf0, tg0, sa, qv0, sv0, std::false_type{});
    STAMP(35);
    if (ts == 0) { if (par == 0) conv_pass<0, 0, 1>(xa, hb, A, acc, job); else conv_pass<0, 1, 1>(xa, hb, A, acc, job); }
    else         { if (par == 0) conv_pass<1, 0, 1>(xa, hb, A, acc, job); else conv_pass<1, 1, 1>(xa, hb, A, acc, job); }
    STAMP(5); CSTAMP(2);
    epilogue(tf1, tg1, sb, qv1, sv1, std::true_type{});
    STAMP(30);
  }
#pragma unroll
  for (int c = 6; c < 8; ++c)
#pragma unroll
    for (int i = 0; i < 3; ++i) W1[c][i] = tr_frag(sm + (c < 7 ? L_W1 + c * 12288 : L_W1B) + wo[i], 16 * 192);
  LBAR();
  STAMP(6);

  const int rt = x * 2 + (jb >= 15 ? 1 : 0);
  const int mt = x * 4 + (jb >> 3), dt = jb & 7;
  const int d0 = dt * 64;
  const int kq = w >> 1, dh = w & 1;
  const int f3 = (rl >> 1) & 3;
  const u32x4 srd_ws = make_srd(ws, WS_BYTES), srd_w2 = make_srd(p.w2, F_DIM * D_DIM * 2);
  const unsigned ldsq = lb + kq * 4096 + (w & 1) * 2048, ldsh = lb + w * 2048;
  unsigned wvo[2], avo;
#pragma unroll
  for (int u = 0; u < 2; ++u) {
    const int r = ((w & 1) * 2 + u) * 8 + (lane >> 3), c = ((lane & 7) ^ (2 * ((r >> 1) & 3))) * 16;
    wvo[u] = (kq * 352 + r) * (D_DIM * 2) + d0 * 2 + c;
  }
  {
    const int ra = lane >> 3;
    avo = WS_HT + ((w * 16 + ra) * S_LEN + rt * 64) * 2 + (((lane & 7) ^ (2 * ((ra >> 1) & 3))) << 4);
  }
  auto load_w2 = [&](auto STEP, auto LDS) {
    constexpr unsigned step = decltype(STEP)::value, lds = decltype(LDS)::value;
    dma16b<0, lds, step * 32 * D_DIM * 2>(srd_w2, wvo[0], ldsq);
    dma16b<0, lds + 1024, step * 32 * D_DIM * 2>(srd_w2, wvo[1], ldsq);
  };
  bf16x8_t W2[11][2];
  const unsigned flc_off = (x + 8 * (15 * ((mt >> 1) & 1) + (lane < 15 ? lane : 0))) * (FLAG_STRIDE * 4);
  unsigned flc = 0;
  auto poll_flc = [&](int h) {
    gu32* f = (gu32*)(ws + WS_FLC + h * 16384 + flc_off);
#pragma unroll 1
    for (unsigned spins = 0; spins < SPIN_CAP; ++spins) {
      unsigned a = __hip_atomic_load(f, RLX_AGENT);
      if (__all(a == epoch)) break;
      __builtin_amdgcn_s_sleep(1);
    }
    __builtin_amdgcn_fence(__ATOMIC_ACQUIRE, "wavefront");
  };
  unsigned hvo[2];
#pragma unroll
  for (int u = 0; u < 2; ++u) {
    const int r = dh * 32 + u * 16 + (lane >> 2);
    hvo[u] = WS_H1B + (mt * F_DIM + kq * 352 + r) * 64 + (((lane & 3) ^ (2 * ((r >> 2) & 1))) << 4);
  }
  auto load_h1 = [&](auto HH, auto RR) {
    constexpr unsigned h = decltype(HH)::value, r = decltype(RR)::value, slot = (r + (h ? 0 : 3)) & 3;
    dma16b<AUX_SC1, slot * 16384, h * (32 * F_DIM * 64) + r * 4096>(srd_ws, hvo[0], ldsq);
    dma16b<AUX_SC1, slot * 16384 + 1024, h * (32 * F_DIM * 64) + r * 4096>(srd_ws, hvo[1], ldsq);
  };
  int woff[2];
#pragma unroll
  for (int i = 0; i < 2; ++i) woff[i] = kq * 4096 + rl * 128 + (((dh * 2 + i) ^ f3) << 5) + 8 * pp;
  auto w2_read = [&](int s, int lds) {
#pragma unroll
    for (int i = 0; i < 2; ++i) W2[s][i] = tr_frag(sm + lds + woff[i], 2048);
  };
  if (c_on) {
    auto load_ht = [&](auto HH, auto JJ) {
      constexpr unsigned h = decltype(HH)::value, j = decltype(JJ)::value, slot = h ? (j + 3) & 3 : j;
      dma16b<AUX_SC1, slot * 16384, j * (128 * S_LEN * 2) + h * 2048>(srd_ws, avo, ldsh);
      dma16b<AUX_SC1, slot * 16384 + 1024, j * (128 * S_LEN * 2) + h * 2048 + 8 * S_LEN * 2>(srd_ws, avo, ldsh);
    };
    load_ht(IC<0>{}, IC<0>{}); load_ht(IC<0>{}, IC<1>{});
    f32x4 csa[3];
#pragma unroll
    for (int i = 0; i < 3; ++i) csa[i] = (f32x4){0, 0, 0, 0};
#pragma unroll
    for (int c = 0; c < 8; ++c)
#pragma unroll
      for (int i = 0; i < 3; ++i) csa[i] = mfma16(ONES, W1[c][i], csa[i]);
    if (wm == 0 && g == 0) {
#pragma unroll
      for (int i = 0; i < 3; ++i) *(LAS float*)(sm + L_CS + (kp * 96 + wn * 48 + i * 16 + fr) * 4) = csa[i][0];
    }
    STAMP(7);
    int aoff[2];
#pragma unroll
    for (int i = 0; i < 2; ++i) aoff[i] = (kp * 32 + rl) * 128 + (((wm * 2 + (i ^ kp)) ^ f3) << 5) + 8 * pp;
    const bool st_on = wn ? q_ == g : fr == 0;
    const int st0 = wn ? L_SST + 512 + (kp * 64 + wm * 32 + kp * 16 + fr) * 16 : L_SST + (kp * 64 + wm * 32 + kp * 16 + g * 4) * 4;
    const int st1 = wn ? L_SST + 512 + (kp * 64 + wm * 32 + (kp ^ 1) * 16 + fr) * 16 : L_SST + (kp * 64 + wm * 32 + (kp ^ 1) * 16 + g * 4) * 4;
    const int ms = wm * 2 + kp;
    int rso[3];
#pragma unroll
    for (int jn = 0; jn < 3; ++jn) { const int nl = wn * 48 + jn * 16 + fr; rso[jn] = L_OUT1 + nl * 128 + (((ms * 2 + (g >> 1)) ^ (nl & 7)) << 4) + (g & 1) * 8; }
    int plo[2]; unsigned pgo[2];
#pragma unroll
    for (int i = 0; i < 2; ++i) {
      const int pc = t + 512 * i, sbk = pc >= 384 ? 1 : 0, rem = pc - sbk * 384, nl = rem >> 2, c4 = rem & 3;
      plo[i] = L_OUT1 + nl * 128 + (((sbk * 4 + c4) ^ (nl & 7)) << 4);
      pgo[i] = (pc < 768 && n0 + nl < F_DIM) ? WS_H1B + (((rt * 2 + sbk) * F_DIM + n0 + nl) * 32 + c4 * 8) * 2 : 0x80000000u;
      if (pc >= 768) plo[i] = L_OUT1;
    }
    f32x2 csv[3];
    static_for<2>([&](auto HH) {
      constexpr int h = decltype(HH)::value;
      unsigned fl[4];
      f32x4 acc[2][3], sta[2];
#pragma unroll
      for (int i = 0; i < 2; ++i) {
        sta[i] = (f32x4){0, 0, 0, 0};
#pragma unroll
        for (int jn = 0; jn < 3; ++jn) acc[i][jn] = (f32x4){0, 0, 0, 0};
      }
      auto g1_loop = [&](auto WN) {
        bf16x8_t A[2][4];
        auto half = [&](bf16x8_t (&Af)[4], const bf16x8_t (&Wf)[3], int u) {
#pragma unroll
          for (int i = 0; i < 2; ++i) {
#pragma unroll
            for (int jn = 0; jn < 3; ++jn) acc[i][jn] = mfma16(Af[2 * u + i], Wf[jn], acc[i][jn]);
            sta[i] = mfma16(Af[2 * u + i], decltype(WN)::value ? Af[2 * u + i] : ONES, sta[i]);
          }
        };
#define G1_ITER(j, N, N0)                                                                                    \
        if ((N0) != (N) && w == 0) WAIT_V(N0); else WAIT_V(N);                                               \
        if (h == 1 && (j) == 3) LBAR(); else RAW_BAR();                                                      \
               \
        if ((j) == h && t == 0) __hip_atomic_store((gu32*)(ws + (h == 0 ? WS_FLB + 16384 : WS_FLC)) + b * FLAG_STRIDE, epoch, RLX_AGENT); \
        if ((j) == 0) { if (h == 0) STAMP(16); else STAMP(37); }                                             \
        _Pragma("unroll") for (int u = 0; u < 2; ++u)                                                        \
          _Pragma("unroll") for (int i = 0; i < 2; ++i)                                                      \
            A[(j) & 1][2 * u + i] = tr_frag(sm + aoff[i] + (h ? ((j) + 3) & 3 : (j)) * 16384 + u * 8192, 2048); \
        if (h == 1 && (j) >= 2) { w2_read(2 * (j) - 4, L_W2A + (2 * (j) - 4) * 16384); w2_read(2 * (j) - 3, L_W2A + (2 * (j) - 3) * 16384); } \
        if ((j) > 0) { half(A[((j) - 1) & 1], W1[(j) > 0 ? 2 * (j) - 2 : 0], 0); interleave_mfma_ds<8>(); }  \
        SCHED();                                                                                             \
        if (h == 1 && (j) == 3) { load_w2(IC<5>{}, IC<L_W2A>{}); load_w2(IC<6>{}, IC<L_W2A + 16384>{}); }                            \
        if ((j) + 2 < 4) { load_ht(IC<h>{}, IC<(j) + 2>{}); load_w2(IC<3 * h + (j)>{}, IC<L_W2A + (3 * h + (j)) * 16384>{}); }       \
        if ((j) == 2) {                                                                                      \
          if (h == 0) {                                                                                      \
            if (w == 0) { _Pragma("unroll") for (int i = 0; i < 4; ++i) fl[i] = ld32_sc1(ws + WS_FLB + 16384, (lane + 64 * i) * (FLAG_STRIDE * 4)); } \
          } else {                                                                                           \
              \
            _Pragma("unroll") for (int v = 0; v < 2; ++v) {                                                  \
              int ks = kq * 11 + 5 + 3 * (w & 1) + v; ks = ks > 42 ? 42 : ks;                                \
              dma16<0>(p.w2 + (size_t)(ks * 32 + (jb >> 3) * 8) * D_DIM + d0, (lane >> 3) * (D_DIM * 2) + (lane & 7) * 16, lb + L_JUNKC); \
            }                                                                                                \
          }                                                                                                  \
        }                                                                                                    \
        SCHED();                                                                                             \
        if ((j) > 0) half(A[((j) - 1) & 1], W1[(j) > 0 ? 2 * (j) - 1 : 0], 1);                               \
        SCHED();
        if constexpr (h == 0) {
          G1_ITER(0, 2, 2) G1_ITER(1, 4, 5) G1_ITER(2, 6, 6) G1_ITER(3, 2, 6)
        } else {
          G1_ITER(0, 4, 4) G1_ITER(1, 4, 4) G1_ITER(2, 6, 7) G1_ITER(3, 4, 4)
        }
#undef G1_ITER
        half(A[1], W1[6], 0); half(A[1], W1[7], 1);
      };
      if (wn) g1_loop(std::integral_constant<bool, true>{}); else g1_loop(std::integral_constant<bool, false>{});
      if (h == 0) STAMP(9); else STAMP(38);
      constexpr int EX = h == 0 ? 7 * 16384 : L_EX1;
      if constexpr (h == 1) {
        WAIT_V(6);
        LBAR();
        if (w == 0) flc = ld32_sc1(ws + WS_FLC, flc_off);
        load_w2(IC<7>{}, IC<L_W2A + 2 * 16384>{}); load_w2(IC<8>{}, IC<L_W2A + 3 * 16384>{});
        w2_read(4, L_W2A + 4 * 16384);
      }
      if (h == 0) STAMP(25); else STAMP(28);
      if (st_on) { *(LAS f32x4*)(sm + st0) = sta[0]; *(LAS f32x4*)(sm + st1) = sta[1]; }
#pragma unroll
      for (int jn = 0; jn < 3; ++jn) *(LAS f32x4*)(sm + EX + w * 3072 + jn * 1024 + lane * 16) = acc[1][jn];
      if constexpr (h == 0) {
        if (w == 0) {
          WAIT_V(0);
          asm volatile("" : "+v"(fl[0]), "+v"(fl[1]), "+v"(fl[2]), "+v"(fl[3]));
          if (!__all((fl[0] == epoch) & (fl[1] == epoch) & (fl[2] == epoch) & (fl[3] == epoch))) wait_flags256(ws, WS_FLB + 16384, epoch, lane);
        }
      }
      LBAR();
      if (h == 0) STAMP(26); else STAMP(29);
      if constexpr (h == 0) {
        if (b == 0 && t == 0) __hip_atomic_store((gu32*)(ws + WS_EPOCH), epoch, RLX_AGENT);
        load_ht(IC<1>{}, IC<0>{});
      }
      if constexpr (h == 1) load_w2(IC<9>{}, IC<L_W2A + 4 * 16384>{});
      if (t < 64) {
        const LAS float *sp = (const LAS float*)(sm + L_SST) + t, *gp = (const LAS float*)(sm + L_SST + 512) + t * 4 + (t & 3);
        const float mean = (sp[0] + sp[64]) * (1.0f / D_DIM);
        const float var = fmaxf((gp[0] + gp[256]) * (1.0f / D_DIM) - mean * mean, 0.f);
        *(LAS float*)(sm + L_FST + t * 4) = -rbf(mean);
        *(LAS float*)(sm + L_FST + 256 + t * 4) = -1.44269504f * __builtin_amdgcn_rcpf(rbf(__builtin_amdgcn_sqrtf(rbf(rbf(var) + 1e-5f))));
      }
      if constexpr (h == 0) {
#pragma unroll
        for (int jn = 0; jn < 3; ++jn) {
          const int nl = wn * 48 + jn * 16 + fr;
          const float c = *(const LAS float*)(sm + L_CS + nl * 4) + *(const LAS float*)(sm + L_CS + (96 + nl) * 4);
          csv[jn] = (f32x2){c, c};
        }
      }
#pragma unroll
      for (int jn = 0; jn < 3; ++jn) acc[0][jn] += *(const LAS f32x4*)(sm + EX + (w ^ 4) * 3072 + jn * 1024 + lane * 16);
      if constexpr (h == 1) {
        if (w == 0) {
          WAIT_V(6);
          asm volatile("" : "+v"(flc));
          if (!__all(flc == epoch)) poll_flc(0);
        }
      }
      LBAR();
      if (h == 0) STAMP(27); else STAMP(31 + 5);
      if constexpr (h == 0) load_ht(IC<1>{}, IC<1>{});
      {
        const int ml = ms * 16 + g * 4;
        const f32x4 nmu = *(const LAS f32x4*)(sm + L_FST + ml * 4), zz = *(const LAS f32x4*)(sm + L_FST + 256 + ml * 4);
        const f32x2 nm0 = {nmu[0], nmu[1]}, nm1 = {nmu[2], nmu[3]}, z0 = {zz[0], zz[1]}, z1 = {zz[2], zz[3]};
        const f32x2 cL = {-1.44269504f, -1.44269504f};
        static_for<3>([&](auto JN) {
          constexpr int jn = decltype(JN)::value;
          if constexpr (h == 1) { SCHED(); load_h1(IC<0>{}, JN); SCHED(); }
          const f32x4 a = acc[0][jn];
          const f32x2 e0 = (nm0 * csv[jn] + (f32x2){a[0], a[1]}) * z0, e1 = (nm1 * csv[jn] + (f32x2){a[2], a[3]}) * z1;
          const f32x2 d0_ = (f32x2){__builtin_amdgcn_exp2f(e0[0]), __builtin_amdgcn_exp2f(e0[1])} * cL + cL;
          const f32x2 d1_ = (f32x2){__builtin_amdgcn_exp2f(e1[0]), __builtin_amdgcn_exp2f(e1[1])} * cL + cL;
          const f32x2 o0 = e0 * (f32x2){__builtin_amdgcn_rcpf(d0_[0]), __builtin_amdgcn_rcpf(d0_[1])};
          const f32x2 o1 = e1 * (f32x2){__builtin_amdgcn_rcpf(d1_[0]), __builtin_amdgcn_rcpf(d1_[1])};
          *(LAS u32x2*)(sm + rso[jn]) = (u32x2){pk_bf16(o0[0], o0[1]), pk_bf16(o1[0], o1[1])};
        });
      }
      LBAR();
      if (h == 0) STAMP(21); else STAMP(39);
      if constexpr (h == 0) load_w2(IC<2>{}, IC<L_W2A + 2 * 16384>{});
      if constexpr (h == 1) load_w2(IC<10>{}, IC<L_W2X>{});
      __builtin_amdgcn_raw_buffer_store_b128(*(const LAS u32x4*)(sm + plo[0]), rs, pgo[0], h * (32 * F_DIM * 64), AUX_SC1);
      if (w < 4) __builtin_amdgcn_raw_buffer_store_b128(*(const LAS u32x4*)(sm + plo[1]), rs, pgo[1], h * (32 * F_DIM * 64), AUX_SC1);
      WAIT_L0();
    });
  } else {
    WAIT_V(0);
    RAW_BAR();
    if (t == 0) __hip_atomic_store((gu32*)(ws + WS_FLB + 16384) + b * FLAG_STRIDE, epoch, RLX_AGENT);
    if (w == 0) wait_flags256(ws, WS_FLB + 16384, epoch, lane);
    RAW_BAR();
    static_for<5>([&](auto S) { load_w2(S, IC<L_W2A + decltype(S)::value * 16384>{}); });
    WAIT_V(0);
    RAW_BAR();
#pragma unroll
    for (int s = 0; s < 5; ++s)
#pragma unroll
      for (int i = 0; i < 2; ++i) W2[s][i] = tr_frag(sm + L_W2A + woff[i] + s * 16384, 2048);
    LBAR();
    static_for<5>([&](auto S) { load_w2(IC<5 + decltype(S)::value>{}, IC<L_W2A + decltype(S)::value * 16384>{}); });
    if (w == 0) poll_flc(0);
    RAW_BAR();
    load_h1(IC<0>{}, IC<0>{}); load_h1(IC<0>{}, IC<1>{}); load_h1(IC<0>{}, IC<2>{});
    load_w2(IC<10>{}, IC<L_W2X>{});
  }
  STAMP(22);
  {
    int boff[2];
#pragma unroll
    for (int i = 0; i < 2; ++i) boff[i] = kq * 4096 + rl * 64 + ((i ^ ((rl >> 2) & 1)) << 5) + 8 * pp;
    static_for<2>([&](auto HH) {
      constexpr int h = decltype(HH)::value;
      f32x4 acc[2][2];
#pragma unroll
      for (int i = 0; i < 2; ++i)
#pragma unroll
        for (int jm = 0; jm < 2; ++jm) acc[i][jm] = (f32x4){0, 0, 0, 0};
      bf16x8_t B[2][4];
      auto step2 = [&](const bf16x8_t (&Wf)[2], const bf16x8_t (&Bf)[4], int u) {
#pragma unroll
        for (int jm = 0; jm < 2; ++jm)
#pragma unroll
          for (int i = 0; i < 2; ++i) acc[i][jm] = mfma16(Wf[i], Bf[2 * u + jm], acc[i][jm]);
      };
#define G2_ITER(r, N, N0)                                                                                               \
      if ((N0) != (N) && w == 0 && c_on) WAIT_V(N0); else WAIT_V(N);          \
      LBAR();                                                                                                           \
      if ((r) == 0) { if (h == 0) STAMP(24); else STAMP(19); }                                                          \
      if ((r) == 1 && h == 0 && c_on && t == 0) __hip_atomic_store((gu32*)(ws + WS_FLC + 16384) + b * FLAG_STRIDE, epoch, RLX_AGENT); \
      _Pragma("unroll") for (int u = 0; u < 2; ++u)                                                                     \
        _Pragma("unroll") for (int i = 0; i < 2; ++i) B[(r) & 1][2 * u + i] = tr_frag(sm + boff[i] + (((r) + (h ? 0 : 3)) & 3) * 16384 + u * 2048, 1024); \
      if ((r) == 0 && h == 0) {                                                                                         \
        _Pragma("unroll") for (int s = 5; s < 10; ++s)                                                                  \
          _Pragma("unroll") for (int i = 0; i < 2; ++i) W2[s][i] = tr_frag(sm + L_W2A + woff[i] + (s - 5) * 16384, 2048); \
      }                                                                                                                 \
      if ((r) == 1 && h == 0) { _Pragma("unroll") for (int i = 0; i < 2; ++i) W2[10][i] = tr_frag(sm + L_W2X + woff[i], 2048); } \
      if ((r) > 0) { step2(W2[(r) > 0 ? 2 * (r) - 2 : 0], B[((r) - 1) & 1], 0);                                         \
        _Pragma("unroll") for (int i = 0; i < 4; ++i) { __builtin_amdgcn_sched_group_barrier(0x8, 1, 0); __builtin_amdgcn_sched_group_barrier(0x100, 2, 0); } } \
      SCHED();                                                                                                          \
      if ((r) + 3 <= 5) load_h1(IC<h>{}, IC<(r) + 3>{});                                                                            \
      if ((r) == 1 && h == 0) {                                                                                         \
                          \
        const int rr_ = (w & 3) * 16 + (lane >> 2);                                                                     \
        dma16<AUX_SC1>(ws + WS_HT + ((size_t)(d0 + (w & 3) * 16) * S_LEN + (w >> 2) * 1024 + mt * 32) * 2,              \
                       (lane >> 2) * (S_LEN * 2) + (((lane & 3) ^ (2 * ((rr_ >> 2) & 1))) << 4), lb + L_RES + w * 1024); \
      }                                                                                                                 \
      if ((r) == 5 && h == 0) { if (w == 0) flc = ld32_sc1(ws + WS_FLC + 16384, flc_off); }                             \
      SCHED();                                                                                                          \
      if ((r) > 0) step2(W2[(r) > 0 ? 2 * (r) - 1 : 0], B[((r) - 1) & 1], 1);                                           \
      SCHED();
      if constexpr (h == 0) {
        G2_ITER(0, 6, 6) G2_ITER(1, 2, 2) G2_ITER(2, 63, 63) G2_ITER(3, 5, 6) G2_ITER(4, 3, 3) G2_ITER(5, 0, 0)
      } else {
        G2_ITER(0, 4, 4) G2_ITER(1, 4, 4) G2_ITER(2, 4, 4) G2_ITER(3, 4, 4) G2_ITER(4, 2, 2) G2_ITER(5, 0, 0)
      }
#undef G2_ITER
      if (kq < 3) step2(W2[10], B[1], 0);
      if (h == 0) STAMP(13); else STAMP(20);
      const int ds = dh * 2 + (kq >> 1), ms = kq & 1;
      const int rr = ds * 16 + rl;
      const u32x2 ru = __builtin_bit_cast(u32x2, tr_read(sm + L_RES + h * 4096 + rr * 64 + ((ms ^ ((rl >> 2) & 1)) << 5) + 8 * pp));
#pragma unroll
      for (int i = 0; i < 2; ++i)
#pragma unroll
        for (int jm = 0; jm < 2; ++jm) *(LAS f32x4*)(sm + L_EX2 + ((dh * 4 + i * 2 + jm) * 4 + kq) * 1024 + lane * 16) = acc[i][jm];
      if constexpr (h == 0) {
        if (w == 0) {
          WAIT_V(0);
          asm volatile("" : "+v"(flc));
          if (!__all(flc == epoch)) poll_flc(1);
        }
      }
      LBAR();
      if constexpr (h == 0) { load_h1(IC<1>{}, IC<0>{}); load_h1(IC<1>{}, IC<1>{}); load_h1(IC<1>{}, IC<2>{}); STAMP(18); }
      const LAS char* e = sm + L_EX2 + ((dh * 4 + (kq >> 1) * 2 + ms) * 4) * 1024 + lane * 16;
      const f32x4 a = (*(const LAS f32x4*)(e) + *(const LAS f32x4*)(e + 1024)) + (*(const LAS f32x4*)(e + 2048) + *(const LAS f32x4*)(e + 3072));
      const u32x2 ov = {pk_bf16(bf_lo(ru[0]) + rbf(a[0]), bf_hi(ru[0]) + rbf(a[1])), pk_bf16(bf_lo(ru[1]) + rbf(a[2]), bf_hi(ru[1]) + rbf(a[3]))};
      *(u32x2*)(p.out + (size_t)(h * 1024 + mt * 32 + ms * 16 + fr) * D_DIM + d0 + ds * 16 + g * 4) = ov;
      if (h == 0) STAMP(14);
    });
  }
  asm volatile(".Lh3_code_end:");
  STAMP(23); CSTAMP(7);
}

extern "C" void kernel_launch(void* const* d_in, const int* in_sizes, int n_in, void* d_out, int out_size, void* d_ws, size_t ws_size,
                              hipStream_t stream) {
  Params p;
  p.q = (const u16*)d_in[0];
  p.k = (const u16*)d_in[1];
  p.v = (const u16*)d_in[2];
  p.shk = (const u16*)d_in[3];
  p.ssmk = (const u16*)d_in[4];
  p.ssmD = (const u16*)d_in[5];
  p.w1 = (const u16*)d_in[7];
  p.w2 = (const u16*)d_in[8];
  p.out = (u16*)d_out;
  p.ws = (char*)d_ws;
  (void)hipFuncSetAttribute((const void*)h3_fused, hipFuncAttributeMaxDynamicSharedMemorySize, LDS_BYTES);
  h3_fused<<<256, 512, LDS_BYTES, stream>>>(p);
}
```

```cpp
#include <hip/hip_runtime.h>
#include <hip/hip_bf16.h>
#include <stdint.h>
#include <type_traits>
#include <utility>

typedef unsigned short u16;
typedef short bf16x8 __attribute__((ext_vector_type(8)));
typedef short s16x4 __attribute__((ext_vector_type(4)));
typedef float f32x4 __attribute__((ext_vector_type(4)));
typedef float f32x2 __attribute__((ext_vector_type(2)));
typedef unsigned u32x4 __attribute__((ext_vector_type(4)));
typedef unsigned u32x2 __attribute__((ext_vector_type(2)));
typedef __bf16 bf16x2_t __attribute__((ext_vector_type(2)));
typedef __bf16 bf16x8_t __attribute__((ext_vector_type(8)));
typedef short v4i16_t __attribute__((ext_vector_type(4)));
#define LAS __attribute__((address_space(3)))
#define GAS __attribute__((address_space(1)))
typedef GAS unsigned gu32;
#define RLX_AGENT __ATOMIC_RELAXED, __HIP_MEMORY_SCOPE_AGENT

constexpr int S_LEN = 2048, D_DIM = 512, F_DIM = 1376;
constexpr int AUX_SC1 = 16;

constexpr unsigned WS_KVT = 0, WS_QT = 2u << 20, WS_HT = 4u << 20, WS_H1B = 6u << 20, WS_STQ = 12u << 20;
constexpr unsigned WS_FL1 = WS_STQ + 65536, WS_FLB = WS_FL1 + 16384, WS_FLC = WS_FLB + 2 * 16384, WS_EPOCH = WS_FLC + 2 * 16384;
constexpr unsigned WS_STAMP = WS_EPOCH + 4096;
constexpr unsigned WS_BYTES = 16u << 20;
constexpr int FLAG_STRIDE = 16;
constexpr unsigned SPIN_CAP = 1u << 18;

#define WAIT_V(n) asm volatile("s_waitcnt vmcnt(%0)" ::"n"(n) : "memory")
#define WAIT_L0() asm volatile("s_waitcnt lgkmcnt(0)" ::: "memory")
#define RAW_BAR() do { asm volatile("" ::: "memory"); __builtin_amdgcn_s_barrier(); asm volatile("" ::: "memory"); } while (0)
#define LBAR() do { WAIT_L0(); RAW_BAR(); } while (0)
#define SCHED() __builtin_amdgcn_sched_barrier(0)
template <int N> __device__ __forceinline__ void interleave_mfma_ds() {
#pragma unroll
  for (int i = 0; i < N; ++i) { __builtin_amdgcn_sched_group_barrier(0x8, 1, 0); __builtin_amdgcn_sched_group_barrier(0x100, 1, 0); }
}

#define STAMP(i)
#define CSTAMP(i)

__device__ __forceinline__ float bf_lo(unsigned x) { return __uint_as_float(x << 16); }
__device__ __forceinline__ float bf_hi(unsigned x) { return __uint_as_float(x & 0xffff0000u); }
__device__ __forceinline__ unsigned pk_bf16(float lo, float hi) {
  bf16x2_t v; v.x = (__bf16)lo; v.y = (__bf16)hi;
  return __builtin_bit_cast(unsigned, v);
}
__device__ __forceinline__ float rbf(float x) { return __uint_as_float(((unsigned)__builtin_bit_cast(u16, (__bf16)x)) << 16); }
__device__ __forceinline__ s16x4 tr_read(const LAS char* p) {
  return __builtin_bit_cast(s16x4, __builtin_amdgcn_ds_read_tr16_b64_v4i16((LAS v4i16_t*)p));
}
__device__ __forceinline__ bf16x8_t cat8(s16x4 a, s16x4 b) {
  bf16x8 r = __builtin_shufflevector(a, b, 0, 1, 2, 3, 4, 5, 6, 7);
  return __builtin_bit_cast(bf16x8_t, r);
}
__device__ __forceinline__ bf16x8_t tr_frag(const LAS char* p, int second) { return cat8(tr_read(p), tr_read(p + second)); }
__device__ __forceinline__ f32x4 mfma16(bf16x8_t a, bf16x8_t b, f32x4 c) {
  return __builtin_amdgcn_mfma_f32_16x16x32_bf16(a, b, c, 0, 0, 0);
}
template <int AUX>
__device__ __forceinline__ void dma16(const void* sbase, unsigned voff, unsigned lds) {
  if constexpr (AUX == AUX_SC1)
    asm volatile("s_mov_b32 m0, %2\n\ts_nop 0\n\tglobal_load_lds_dwordx4 %0, %1 sc1" ::"v"(voff), "s"(sbase), "s"(lds) : "memory");
  else
    asm volatile("s_mov_b32 m0, %2\n\ts_nop 0\n\tglobal_load_lds_dwordx4 %0, %1" ::"v"(voff), "s"(sbase), "s"(lds) : "memory");
}
template <int AUX, unsigned LC, unsigned SC>
__device__ __forceinline__ void dma16b(u32x4 srd, unsigned voff, unsigned ldsw) {
  unsigned tmp;
  if constexpr (AUX == AUX_SC1)
    asm volatile("s_add_u32 m0, %3, %4\n\ts_mov_b32 %0, %5\n\tbuffer_load_dwordx4 %1, %2, %0 offen sc1 lds"
                 : "=&s"(tmp) : "v"(voff), "s"(srd), "s"(ldsw), "n"(LC), "n"(SC) : "memory", "scc");
  else
    asm volatile("s_add_u32 m0, %3, %4\n\ts_mov_b32 %0, %5\n\tbuffer_load_dwordx4 %1, %2, %0 offen lds"
                 : "=&s"(tmp) : "v"(voff), "s"(srd), "s"(ldsw), "n"(LC), "n"(SC) : "memory", "scc");
}
__device__ __forceinline__ u32x4 make_srd(const void* p, unsigned bytes) {
  const unsigned long long a = (unsigned long long)p;
  return (u32x4){(unsigned)a, (unsigned)(a >> 32) & 0xffffu, bytes, 0x00020000u};
}
template <int V> using IC = std::integral_constant<int, V>;
__device__ __forceinline__ void dma4(const void* sbase, unsigned voff, unsigned lds) {
  asm volatile("s_mov_b32 m0, %2\n\ts_nop 0\n\tglobal_load_lds_dword %0, %1" ::"v"(voff), "s"(sbase), "s"(lds) : "memory");
}
__device__ __forceinline__ void dma4v(const void* vaddr, unsigned lds) {
  asm volatile("s_mov_b32 m0, %1\n\ts_nop 0\n\tglobal_load_lds_dword %0, off sc1" ::"v"(vaddr), "s"(lds) : "memory");
}
__device__ __forceinline__ u32x4 ld128_asm(const void* sbase, unsigned voff) {
  u32x4 r; asm volatile("global_load_dwordx4 %0, %1, %2" : "=v"(r) : "v"(voff), "s"(sbase) : "memory"); return r;
}
__device__ __forceinline__ u32x4 ld128_sc1(const void* sbase, unsigned voff) {
  u32x4 r; asm volatile("global_load_dwordx4 %0, %1, %2 sc1" : "=v"(r) : "v"(voff), "s"(sbase) : "memory"); return r;
}
__device__ __forceinline__ unsigned ld32_sc1(const void* sbase, unsigned voff) {
  unsigned r; asm volatile("global_load_dword %0, %1, %2 sc1" : "=v"(r) : "v"(voff), "s"(sbase) : "memory"); return r;
}
__device__ __forceinline__ unsigned ld16_asm(const void* sbase, unsigned voff) {
  unsigned r; asm volatile("global_load_ushort %0, %1, %2" : "=v"(r) : "v"(voff), "s"(sbase) : "memory"); return r;
}
__device__ __forceinline__ u32x2 ld64_asm(const void* sbase, unsigned voff) {
  u32x2 r; asm volatile("global_load_dwordx2 %0, %1, %2" : "=v"(r) : "v"(voff), "s"(sbase) : "memory"); return r;
}
__device__ __forceinline__ u32x2 ld64_sc1(const void* sbase, unsigned voff) {
  u32x2 r; asm volatile("global_load_dwordx2 %0, %1, %2 sc1" : "=v"(r) : "v"(voff), "s"(sbase) : "memory"); return r;
}
template <int CTRL>
__device__ __forceinline__ float dpp_add(float x) {
  return x + __builtin_bit_cast(float, __builtin_amdgcn_mov_dpp(__builtin_bit_cast(int, x), CTRL, 0xf, 0xf, true));
}
__device__ __forceinline__ float wave_sum(float x) {
  x = dpp_add<0xB1>(x); x = dpp_add<0x4E>(x); x = dpp_add<0x141>(x); x = dpp_add<0x140>(x);
  auto s = __builtin_amdgcn_permlane16_swap(__float_as_uint(x), __float_as_uint(x), false, false);
  x = __uint_as_float(s[0]) + __uint_as_float(s[1]);
  auto u = __builtin_amdgcn_permlane32_swap(__float_as_uint(x), __float_as_uint(x), false, false);
  return __uint_as_float(u[0]) + __uint_as_float(u[1]);
}
__device__ __forceinline__ u32x4 widen16(u32x2 X, u32x2 Y) {
  auto a = __builtin_amdgcn_permlane16_swap(X[0], Y[0], false, false);
  auto b = __builtin_amdgcn_permlane16_swap(X[1], Y[1], false, false);
  return (u32x4){a[0], b[0], a[1], b[1]};
}

struct Params {
  const u16 *q, *k, *v, *shk, *ssmk, *ssmD, *w1, *w2;
  u16* out;
  char* ws;
};

__device__ __forceinline__ void wait_flags256(const char* ws, unsigned off, unsigned epoch, int lane) {
  gu32* f = (gu32*)(ws + off) + lane * FLAG_STRIDE;
#pragma unroll 1
  for (unsigned spins = 0; spins < SPIN_CAP; ++spins) {
    unsigned a = __hip_atomic_load(f, RLX_AGENT), b = __hip_atomic_load(f + 64 * FLAG_STRIDE, RLX_AGENT);
    unsigned c = __hip_atomic_load(f + 128 * FLAG_STRIDE, RLX_AGENT), d = __hip_atomic_load(f + 192 * FLAG_STRIDE, RLX_AGENT);
    if (__all((a == epoch) & (b == epoch) & (c == epoch) & (d == epoch))) break;
    __builtin_amdgcn_s_sleep(1);
  }
  __builtin_amdgcn_fence(__ATOMIC_ACQUIRE, "wavefront");
}

constexpr int L_KT = 32768, L_VT = L_KT + 72 * 128, L_QT = L_VT + 8192, L_OKV = L_QT + 8192, L_OQ = L_OKV + 8192, L_SS = L_OQ + 8192;
constexpr int H_ROW = 1152, H_COPY = 4728, H_BYTES = 9344, X_BYTES = 4160;
constexpr int L_H = 0, L_X = L_H + 2 * H_BYTES, L_QD = L_X + 2 * X_BYTES;
constexpr int CY_BYTES = 257 * 16;
constexpr int L_CY = L_QD + 8192;
constexpr int L_JUNKA = 76800;
constexpr int L_W1 = 77824;
constexpr int L_W1B = 51712;
static_assert(L_CY + 4 * CY_BYTES <= L_W1B && L_W1B + 12288 <= L_JUNKA, "lds");
static_assert(L_CY + 4 * CY_BYTES <= L_JUNKA && L_JUNKA + 1024 <= L_W1 && L_W1 + 7 * 12288 <= 163840 && L_SS + 64 <= L_JUNKA, "lds");
static_assert(L_X + 2 * X_BYTES <= L_KT, "lds A/B overlap");
constexpr int L_W2A = 4 * 16384;
constexpr int L_CS = 147456;
constexpr int L_SST = L_CS + 1024;
constexpr int L_FST = L_SST + 2560;
constexpr int L_JUNKC = 159744;
constexpr int L_EX1 = 0;
constexpr int L_OUT1 = 2 * 16384;
static_assert(L_JUNKC + 1024 <= 163840, "lds");
constexpr int L_W2X = 9 * 16384;
constexpr int L_RES = 4 * 16384;
constexpr int L_EX2 = 5 * 16384;
constexpr int LDS_BYTES = 163840;

template <class F, int... I>
__device__ __forceinline__ void static_for_impl(F&& f, std::integer_sequence<int, I...>) { (f(std::integral_constant<int, I>{}), ...); }
template <int N, class F>
__device__ __forceinline__ void static_for(F&& f) { static_for_impl(f, std::make_integer_sequence<int, N>{}); }

template <int TS, int PAR, int H, class F>
__device__ __forceinline__ void conv_pass(const LAS char* xa, const LAS char* hb, bf16x8_t (&A)[8], bf16x8_t (&B)[3][4], f32x4 (&acc)[2][4], F&& job) {
  constexpr int U0 = H ? (TS ? 5 : 4) : (TS ? 1 : 0), U1 = H ? (TS ? 6 : 7) : (TS ? 2 : 3);
  constexpr int T[2] = {PAR ? U1 : U0, PAR ? U0 : U1};
  constexpr int NA = U1 + 1, A0 = H ? (TS ? 3 : 4) : 0;
  if constexpr (H == 1) {
#pragma unroll
    for (int p = 0; p < 4; ++p) B[0][p] = *(const LAS bf16x8_t*)(hb + p * H_ROW + 896);
  }
#pragma unroll
  for (int a = NA - 1; a >= A0; --a) A[a] = tr_frag(xa + 512 * a, 32);
  if constexpr (H == 1) {
#pragma unroll
    for (int p = 0; p < 4; ++p) B[1][p] = *(const LAS bf16x8_t*)(hb + p * H_ROW + 896 - 128);
  }
#pragma unroll
  for (int j = 0; j < 2; ++j)
#pragma unroll
    for (int p = 0; p < 4; ++p) acc[j][p] = (f32x4){0, 0, 0, 0};
  SCHED();
  static_for<NA>([&](auto BB) {
    constexpr int bb = decltype(BB)::value;
    if (bb + 2 < NA) {
#pragma unroll
      for (int p = 0; p < 4; ++p) B[(bb + 2) % 3][p] = *(const LAS bf16x8_t*)(hb + p * H_ROW + 896 - 128 * (bb + 2));
    }
#pragma unroll
    for (int p = 0; p < 4; ++p)
#pragma unroll
      for (int j = 0; j < 2; ++j)
        if (T[j] >= bb) acc[j][p] = mfma16(A[T[j] - bb], B[bb % 3][p], acc[j][p]);
    if (bb + 2 < NA) interleave_mfma_ds<4>();
    SCHED();
    job(std::integral_constant<int, H>{}, BB);
    SCHED();
  });
}

__global__ __launch_bounds__(512, 2) void h3_fused(Params p) {
  extern __shared__ __attribute__((aligned(16))) char smem_[];
  LAS char* sm = (LAS char*)smem_;
  const unsigned lb = (unsigned)(size_t)sm;
  const int b = blockIdx.x, t = threadIdx.x, lane = t & 63;
  const int w = __builtin_amdgcn_readfirstlane(t >> 6);
  const int x = b & 7, jb = b >> 3;
  const int g = lane >> 4, fr = lane & 15, q_ = fr >> 2, pp = lane & 3;
  const int rl = 4 * g + q_;
  char* ws = p.ws;
  const __amdgpu_buffer_rsrc_t rs = __builtin_amdgcn_make_buffer_rsrc(ws, 0, WS_BYTES, 0x00020000);
  STAMP(0); CSTAMP(0);

  const int dB = x * 64 + jb * 2;
  const int cb = w & 3, sl = 8 * (4 * (w >> 2) + g);
  const unsigned tvo = (lane >> 3) * (D_DIM * 2) + (lane & 7) * 16;
  const size_t tso = ((size_t)(jb * 64 + w * 8) * D_DIM + x * 64);
  dma16<0>(p.k + tso, tvo, lb + L_KT + 1024 + w * 1024);
  dma16<0>(p.v + tso, tvo, lb + L_VT + w * 1024);
  if (w == 0) {
    if (jb > 0) dma16<0>(p.k + tso - 8 * D_DIM, tvo, lb + L_KT);
    else *(LAS u32x4*)(sm + L_KT + lane * 16) = (u32x4){0, 0, 0, 0};
  }
  u32x2 wv = ld64_asm(p.shk + (size_t)(x * 64 + cb * 16) * 4, fr * 8);
  STAMP(32);
  const bool c_on = jb < 30;
  const int panel = x * 2 + (jb >= 15 ? 1 : 0), ct = jb >= 15 ? jb - 15 : jb;
  const int m0 = panel * 128, n0 = ct * 96;
  unsigned w1o[6];
#pragma unroll
  for (int u = 0; u < 6; ++u) {
    const int idx = ((w & 1) * 6 + u) * 64 + lane, r = (idx * 43691) >> 19, cp = idx - r * 12;
    int col = n0 + (cp ^ (2 * ((r >> 2) & 1))) * 8;
    col = col > F_DIM - 8 ? F_DIM - 8 : col;
    w1o[u] = (r * F_DIM + col) * 2;
  }
  auto w1_half = [&](int chunk, int ldst, int step) {
#pragma unroll
    for (int u = 0; u < 6; ++u) dma16<0>(p.w1 + (size_t)chunk * 64 * F_DIM, w1o[u], lb + ldst + ((w & 1) * 6 + u) * step);
  };
  STAMP(33);
  __builtin_amdgcn_s_sleep(8);
  dma16<0>(p.q + tso, tvo, lb + L_QT + w * 1024);
  u32x4 sq = ld128_asm(p.q + (size_t)(b * 8 + w) * D_DIM, lane * 16);
  unsigned ep0 = ld32_sc1(ws + WS_EPOCH, 0);
  unsigned dv0 = ld16_asm(p.ssmD + dB + (w >> 2), 0);
  if (w == 7) {
    unsigned long long pc; unsigned len;
    asm volatile("s_getpc_b64 %0\n.Lh3_ref_%=:\n\ts_mov_b32 %1, .Lh3_code_end-.Lh3_ref_%=" : "=s"(pc), "=s"(len));
    const unsigned nl = len >> 7;
    unsigned line = (unsigned)(jb * 8 + lane); line = line >= nl ? line - nl : line; line = line >= nl ? 0 : line;
    dma4((const void*)pc, line << 7, lb + L_JUNKA);
  }
  if (w == 6) {
    const char* a = ws + (size_t)(jb * 8 + (lane & 7)) * 65536;
    if (lane >= 8) a = (const char*)p.w1 + jb * 44032;
    if (lane >= 16) a = (const char*)p.w2 + jb * 44032;
    if (lane >= 24) a = (const char*)p.out + jb * 65536;
    if (lane >= 32) a = (const char*)p.ssmk + jb * 65536;
    dma4v(a, lb + L_JUNKA + 256);
  }
  if (w >= 6) WAIT_V(5); else WAIT_V(4);
  asm volatile("" : "+v"(wv));
  LBAR();
  STAMP(1);
  const int tro = q_ * 128 + cb * 32 + 8 * pp + sl * 128;
  const int oo = (cb * 16 + fr) * 128 + (((sl >> 3) ^ (fr & 7)) << 4);
  {
    const u32x2 k0 = __builtin_bit_cast(u32x2, tr_read(sm + L_KT + 4 * 128 + tro));
    const u32x2 k1 = __builtin_bit_cast(u32x2, tr_read(sm + L_KT + 8 * 128 + tro));
    const u32x2 k2 = __builtin_bit_cast(u32x2, tr_read(sm + L_KT + 12 * 128 + tro));
    const u32x2 v0 = __builtin_bit_cast(u32x2, tr_read(sm + L_VT + tro));
    const u32x2 v1 = __builtin_bit_cast(u32x2, tr_read(sm + L_VT + 4 * 128 + tro));
    const float w0 = bf_lo(wv[0]), w1 = bf_hi(wv[0]), w2 = bf_lo(wv[1]), w3 = bf_hi(wv[1]);
    const float kx[11] = {bf_hi(k0[0]), bf_lo(k0[1]), bf_hi(k0[1]), bf_lo(k1[0]), bf_hi(k1[0]), bf_lo(k1[1]), bf_hi(k1[1]),
                          bf_lo(k2[0]), bf_hi(k2[0]), bf_lo(k2[1]), bf_hi(k2[1])};
    const float vx[8] = {bf_lo(v0[0]), bf_hi(v0[0]), bf_lo(v0[1]), bf_hi(v0[1]), bf_lo(v1[0]), bf_hi(v1[0]), bf_lo(v1[1]), bf_hi(v1[1])};
    float o[8];
#pragma unroll
    for (int e = 0; e < 8; ++e) o[e] = (kx[e] * w3 + kx[e + 1] * w2 + kx[e + 2] * w1 + kx[e + 3] * w0) * vx[e];
    *(LAS u32x4*)(sm + L_OKV + oo) = (u32x4){pk_bf16(o[0], o[1]), pk_bf16(o[2], o[3]), pk_bf16(o[4], o[5]), pk_bf16(o[6], o[7])};
  }
  LBAR();
  STAMP(15);
  const int toff = (t >> 3) * 128 + (((t & 7) ^ ((t >> 3) & 7)) << 4);
  const unsigned tgo = ((x * 64 + (t >> 3)) * S_LEN + jb * 64 + (t & 7) * 8) * 2;
  __builtin_amdgcn_raw_buffer_store_b128(*(const LAS u32x4*)(sm + L_OKV + toff), rs, WS_KVT + tgo, 0, AUX_SC1);
  WAIT_V(1);
  asm volatile("" : "+v"(sq), "+v"(ep0), "+v"(dv0));
  RAW_BAR();
  const unsigned epoch = (unsigned)__builtin_amdgcn_readfirstlane(ep0) + 1u;
  const float Dv = bf_lo(dv0);
  {
    const u32x2 q0 = __builtin_bit_cast(u32x2, tr_read(sm + L_QT + tro));
    const u32x2 q1 = __builtin_bit_cast(u32x2, tr_read(sm + L_QT + 4 * 128 + tro));
    f32x2 xs[4];
#pragma unroll
    for (int i = 0; i < 4; ++i) xs[i] = (f32x2){bf_lo(sq[i]), bf_hi(sq[i])};
    const f32x2 s2 = (xs[0] + xs[1]) + (xs[2] + xs[3]);
    const float mean = wave_sum(s2[0] + s2[1]) * (1.0f / D_DIM);
    *(LAS u32x4*)(sm + L_OQ + oo) = (u32x4){q0[0], q0[1], q1[0], q1[1]};
    f32x2 vs = {0.f, 0.f};
#pragma unroll
    for (int i = 0; i < 4; ++i) { const f32x2 dd = xs[i] - mean; vs += dd * dd; }
    const float var = rbf(wave_sum(vs[0] + vs[1]) * (1.0f / D_DIM));
    if (lane == 0) *(LAS unsigned*)(sm + L_SS + w * 4) = pk_bf16(mean, __builtin_amdgcn_sqrtf(rbf(var + 1e-5f)));
  }
  LBAR();
  __builtin_amdgcn_raw_buffer_store_b128(*(const LAS u32x4*)(sm + L_OQ + toff), rs, WS_QT + tgo, 0, AUX_SC1);
  if (t < 2) __builtin_amdgcn_raw_buffer_store_b128(*(const LAS u32x4*)(sm + L_SS + t * 16), rs, WS_STQ + b * 32 + t * 16, 0, AUX_SC1);
  STAMP(31);
  WAIT_V(0);
  LBAR();
  if (t == 0) __hip_atomic_store((gu32*)(ws + WS_FL1) + b * FLAG_STRIDE, epoch, RLX_AGENT);
  RAW_BAR();
  STAMP(2);
  {
    const int hc = t >> 8, hw = t & 255;
    u32x4 hv = ld128_asm(p.ssmk + (size_t)dB * S_LEN, (hc * S_LEN + hw * 8) * 2);
    const unsigned o1 = min((unsigned)(b * 5504 + (w < 6 ? w : 5) * 1024), (unsigned)(D_DIM * F_DIM * 2 - 1024));
    dma16<0>((const char*)p.w1 + o1, lane * 16, lb + L_JUNKA);
    WAIT_V(1);
    asm volatile("" : "+v"(hv));
    LAS char* hb = sm + L_H + hc * H_BYTES + 1020 - 4 * hw;
    const unsigned h0 = __builtin_amdgcn_perm(hv[1], hv[3], 0x07060302u), h1 = __builtin_amdgcn_perm(hv[1], hv[3], 0x05040100u);
    const unsigned h2 = __builtin_amdgcn_perm(hv[0], hv[2], 0x07060302u), h3 = __builtin_amdgcn_perm(hv[0], hv[2], 0x05040100u);
    *(LAS unsigned*)(hb + 0 * H_ROW) = h0; *(LAS unsigned*)(hb + H_COPY + 0 * H_ROW) = h0;
    *(LAS unsigned*)(hb + 1 * H_ROW) = h1; *(LAS unsigned*)(hb + H_COPY + 1 * H_ROW) = h1;
    *(LAS unsigned*)(hb + 2 * H_ROW) = h2; *(LAS unsigned*)(hb + H_COPY + 2 * H_ROW) = h2;
    *(LAS unsigned*)(hb + 3 * H_ROW) = h3; *(LAS unsigned*)(hb + H_COPY + 3 * H_ROW) = h3;
    if (t < 256) {
      const int r = t >> 4;
      *(LAS u32x2*)(sm + L_H + (r >> 3) * H_BYTES + ((r >> 2) & 1) * H_COPY + (r & 3) * H_ROW + 1024 + (t & 15) * 8) = (u32x2){0, 0};
    } else if (t < 260) *(LAS u32x4*)(sm + L_X + ((t - 256) >> 1) * X_BYTES + (t & 1) * 16) = (u32x4){0, 0, 0, 0};
    else if (t < 264) *(LAS u32x4*)(sm + L_CY + (t - 260) * CY_BYTES) = (u32x4){0, 0, 0, 0};
  }

  const int c2 = w >> 2, ts = (w >> 1) & 1, par = w & 1;
  const int u00 = ts ? 1 : 0, u01 = ts ? 2 : 3, u10 = ts ? 5 : 4, u11 = ts ? 6 : 7;
  const int tf0 = par ? u01 : u00, tg0 = par ? u00 : u01, tf1 = par ? u11 : u10, tg1 = par ? u10 : u11;
  const int sa = tf0 * 256 + fr * 16 + g * 4, sb = tf1 * 256 + fr * 16 + g * 4;
  const int kp = w >> 2, wm = (w >> 1) & 1, wn = w & 1;
  const bf16x8_t ONES = __builtin_bit_cast(bf16x8_t, (u32x4){0x3f803f80u, 0x3f803f80u, 0x3f803f80u, 0x3f803f80u});
  bf16x8_t W1[8][3];
  int wo[3];
#pragma unroll
  for (int i = 0; i < 3; ++i) wo[i] = (kp * 32 + rl) * 192 + 8 * pp + (((wn * 3 + i) ^ ((rl >> 2) & 1)) << 5);
  if (w == 0) wait_flags256(ws, WS_FL1, epoch, lane);
  LBAR();
  STAMP(3);
  {
    const int d = dB + c2;
    dma16<AUX_SC1>(ws + WS_KVT + ((size_t)d * S_LEN + (w & 3) * 512) * 2, lane * 16, lb + L_X + c2 * X_BYTES + 32 + (w & 3) * 1024);
    u32x2 qv0 = ld64_sc1(ws + WS_QT + (size_t)d * S_LEN * 2, sa * 2), qv1 = ld64_sc1(ws + WS_QT + (size_t)d * S_LEN * 2, sb * 2);
    u32x4 sv0 = ld128_sc1(ws + WS_STQ, sa * 4), sv1 = ld128_sc1(ws + WS_STQ, sb * 4);
    f32x4 acc[2][4];
    bf16x8_t A[8], B[3][4];
    const LAS char* xa = sm + L_X + c2 * X_BYTES + 8 + 64 * g + 8 * q_ + 8 * pp + 256 * par;
    const LAS char* hb = sm + L_H + c2 * H_BYTES + ((fr & 1) ? 0 : H_COPY) + 1016 - 8 * fr + 16 * g + 64 * par - 896;
#pragma unroll
    for (int p = 0; p < 4; ++p) { B[0][p] = *(const LAS bf16x8_t*)(hb + p * H_ROW + 896); B[1][p] = *(const LAS bf16x8_t*)(hb + p * H_ROW + 896 - 128); }
    WAIT_V(4);
    RAW_BAR();
    STAMP(4); CSTAMP(1);
    const int prev = g > 0 ? lane - 16 : (fr > 0 ? 47 + fr : -1);
    unsigned fl[4];
    auto w1_read = [&](auto CC) {
      constexpr int c = decltype(CC)::value;
#pragma unroll
      for (int i = 0; i < 3; ++i) W1[c][i] = tr_frag(sm + (c < 7 ? L_W1 + c * 12288 : L_W1B) + wo[i], 16 * 192);
    };
    auto w1_third = [&](int ch, int j3) {
      const int c = ch + (w >> 2), u = ((w >> 1) & 1) * 3 + j3;
      dma16<0>(p.w1 + (size_t)c * 64 * F_DIM, (w & 2) ? w1o[3 + j3] : w1o[j3], lb + (c < 7 ? L_W1 + c * 12288 : L_W1B) + ((w & 1) * 6 + u) * 1024);
    };
    auto job = [&](auto HH, auto BB) {
      constexpr int h = decltype(HH)::value, bb = decltype(BB)::value;
      if constexpr (h == 0) {
        if constexpr (bb < 3) w1_third(0, bb);
      } else {
        if constexpr (bb == 0) {
          int r2 = ((b * 43) >> 3) + w; r2 = r2 > F_DIM - 1 ? F_DIM - 1 : r2;
          dma16<0>(p.w2 + (size_t)r2 * D_DIM, lane * 16, lb + L_JUNKA);
        }
        if constexpr (bb == 2) {
          WAIT_V(5);
          RAW_BAR();
          if (t == 0) __hip_atomic_store((gu32*)(ws + WS_FLB) + b * FLAG_STRIDE, epoch, RLX_AGENT);
          if (t >= 64 && t < 68) {
            LAS char* c = sm + L_CY + (t - 64) * CY_BYTES;
            *(LAS f32x4*)c = *(const LAS f32x4*)(c + 256 * 16);
          }
        }
        if constexpr (bb < 3) {
          const int ch = 2 + (w >> 1);
#pragma unroll
          for (int u = 2 * bb; u < 2 * bb + 2; ++u) dma16<0>(p.w1 + (size_t)ch * 64 * F_DIM, w1o[u], lb + L_W1 + ch * 12288 + ((w & 1) * 6 + u) * 1024);
        }
        if constexpr (bb == 3) { w1_third(6, 0); w1_third(6, 1); w1_read(std::integral_constant<int, 0>{}); }
        if constexpr (bb == 4) { w1_third(6, 2); w1_read(std::integral_constant<int, 1>{}); }
        if constexpr (bb == 5) {
          WAIT_V(3);
          RAW_BAR();
          if (w == 0) {
#pragma unroll
            for (int i = 0; i < 4; ++i) fl[i] = ld32_sc1(ws + WS_FLB, (lane + 64 * i) * (FLAG_STRIDE * 4));
          }
          w1_read(std::integral_constant<int, 2>{}); w1_read(std::integral_constant<int, 3>{});
        }
        if constexpr (bb == 6) { w1_read(std::integral_constant<int, 4>{}); w1_read(std::integral_constant<int, 5>{}); }
      }
    };
    auto epilogue = [&](int tf, int tg, int s0, u32x2 qv, u32x4 sv, auto LAST) {
#pragma unroll
      for (int j = 0; j < 2; ++j) {
        acc[j][3][1] += acc[j][2][0];
        acc[j][3][2] += acc[j][2][1] + acc[j][1][0];
        acc[j][3][3] += (acc[j][2][2] + acc[j][1][1]) + acc[j][0][0];
        acc[j][0][1] += acc[j][2][3] + acc[j][1][2];
        acc[j][0][2] += acc[j][1][3];
      }
      LAS char* cy = sm + L_CY + (c2 * 2 + par) * CY_BYTES + 16 + lane * 16;
      *(LAS f32x4*)(cy + (tf & 3) * 1024) = acc[0][0];
      *(LAS f32x4*)(cy + (tg & 3) * 1024) = acc[1][0];
      *(LAS f32x4*)(sm + L_QD + w * 1024 + lane * 16) = acc[1][3];
      const u32x2 xv = *(const LAS u32x2*)(sm + L_X + c2 * X_BYTES + 32 + s0 * 2);
      if constexpr (decltype(LAST)::value) {
        WAIT_V(0);
        if (w == 0) {
          asm volatile("" : "+v"(fl[0]), "+v"(fl[1]), "+v"(fl[2]), "+v"(fl[3]));
          if (!__all((fl[0] == epoch) & (fl[1] == epoch) & (fl[2] == epoch) & (fl[3] == epoch))) wait_flags256(ws, WS_FLB, epoch, lane);
        }
      }
      LBAR();
      const LAS char* cy0 = sm + L_CY + (c2 * 2) * CY_BYTES + 16 + prev * 16 + (tf & 3) * 1024;
      const f32x4 pq = *(const LAS f32x4*)(sm + L_QD + (w ^ 1) * 1024 + lane * 16);
      const f32x4 ca = *(const LAS f32x4*)(cy0), cb_ = *(const LAS f32x4*)(cy0 + CY_BYTES);
      f32x4 y = acc[0][3] + pq;
      y[0] += ca[1] + cb_[1]; y[1] += ca[2] + cb_[2]; y[2] += ca[3] + cb_[3];
      const float xx[4] = {bf_lo(xv[0]), bf_hi(xv[0]), bf_lo(xv[1]), bf_hi(xv[1])};
      const float qx[4] = {bf_lo(qv[0]), bf_hi(qv[0]), bf_lo(qv[1]), bf_hi(qv[1])};
      float o[4];
#pragma unroll
      for (int e = 0; e < 4; ++e) o[e] = qx[e] + ((qx[e] - bf_lo(sv[e])) * __builtin_amdgcn_rcpf(bf_hi(sv[e]))) * (y[e] + xx[e] * Dv);
      __builtin_amdgcn_raw_buffer_store_b64((u32x2){pk_bf16(o[0], o[1]), pk_bf16(o[2], o[3])}, rs, WS_HT + (d * S_LEN + s0) * 2, 0, AUX_SC1);
    };
    if (ts == 0) { if (par == 0) conv_pass<0, 0, 0>(xa, hb, A, B, acc, job); else conv_pass<0, 1, 0>(xa, hb, A, B, acc, job); }
    else         { if (par == 0) conv_pass<1, 0, 0>(xa, hb, A, B, acc, job); else conv_pass<1, 1, 0>(xa, hb, A, B, acc, job); }
    STAMP(34);
    WAIT_V(3);
    asm volatile("" : "+v"(qv0), "+v"(qv1), "+v"(sv0), "+v"(sv1));
    epilogue(tf0, tg0, sa, qv0, sv0, std::false_type{});
    STAMP(35);
    if (ts == 0) { if (par == 0) conv_pass<0, 0, 1>(xa, hb, A, B, acc, job); else conv_pass<0, 1, 1>(xa, hb, A, B, acc, job); }
    else         { if (par == 0) conv_pass<1, 0, 1>(xa, hb, A, B, acc, job); else conv_pass<1, 1, 1>(xa, hb, A, B, acc, job); }
    STAMP(5); CSTAMP(2);
    epilogue(tf1, tg1, sb, qv1, sv1, std::true_type{});
    STAMP(30);
  }
#pragma unroll
  for (int c = 6; c < 8; ++c)
#pragma unroll
    for (int i = 0; i < 3; ++i) W1[c][i] = tr_frag(sm + (c < 7 ? L_W1 + c * 12288 : L_W1B) + wo[i], 16 * 192);
  LBAR();
  STAMP(6);

  const int rt = x * 2 + (jb >= 15 ? 1 : 0);
  const int mt = x * 4 + (jb >> 3), dt = jb & 7;
  const int d0 = dt * 64;
  const int kq = w >> 1, dh = w & 1;
  const int f3 = (rl >> 1) & 3;
  const u32x4 srd_ws = make_srd(ws, WS_BYTES), srd_w2 = make_srd(p.w2, F_DIM * D_DIM * 2);
  const unsigned ldsq = lb + kq * 4096 + (w & 1) * 2048, ldsh = lb + w * 2048;
  unsigned wvo[2], avo;
#pragma unroll
  for (int u = 0; u < 2; ++u) {
    const int r = ((w & 1) * 2 + u) * 8 + (lane >> 3), c = ((lane & 7) ^ (2 * ((r >> 1) & 3))) * 16;
    wvo[u] = (kq * 352 + r) * (D_DIM * 2) + d0 * 2 + c;
  }
  {
    const int ra = lane >> 3;
    avo = WS_HT + ((w * 16 + ra) * S_LEN + rt * 64) * 2 + (((lane & 7) ^ (2 * ((ra >> 1) & 3))) << 4);
  }
  auto load_w2 = [&](auto STEP, auto LDS) {
    constexpr unsigned step = decltype(STEP)::value, lds = decltype(LDS)::value;
    dma16b<0, lds, step * 32 * D_DIM * 2>(srd_w2, wvo[0], ldsq);
    dma16b<0, lds + 1024, step * 32 * D_DIM * 2>(srd_w2, wvo[1], ldsq);
  };
  bf16x8_t W2[11][2];
  const unsigned flc_off = (x + 8 * (15 * ((mt >> 1) & 1) + (lane < 15 ? lane : 0))) * (FLAG_STRIDE * 4);
  unsigned flc = 0;
  auto poll_flc = [&](int h) {
    gu32* f = (gu32*)(ws + WS_FLC + h * 16384 + flc_off);
#pragma unroll 1
    for (unsigned spins = 0; spins < SPIN_CAP; ++spins) {
      unsigned a = __hip_atomic_load(f, RLX_AGENT);
      if (__all(a == epoch)) break;
      __builtin_amdgcn_s_sleep(1);
    }
    __builtin_amdgcn_fence(__ATOMIC_ACQUIRE, "wavefront");
  };
  unsigned hvo[2];
#pragma unroll
  for (int u = 0; u < 2; ++u) {
    const int r = dh * 32 + u * 16 + (lane >> 2);
    hvo[u] = WS_H1B + (mt * F_DIM + kq * 352 + r) * 64 + (((lane & 3) ^ (2 * ((r >> 2) & 1))) << 4);
  }
  auto load_h1 = [&](auto HH, auto RR) {
    constexpr unsigned h = decltype(HH)::value, r = decltype(RR)::value, slot = (r + (h ? 0 : 3)) & 3;
    dma16b<AUX_SC1, slot * 16384, h * (32 * F_DIM * 64) + r * 4096>(srd_ws, hvo[0], ldsq);
    dma16b<AUX_SC1, slot * 16384 + 1024, h * (32 * F_DIM * 64) + r * 4096>(srd_ws, hvo[1], ldsq);
  };
  int woff[2];
#pragma unroll
  for (int i = 0; i < 2; ++i) woff[i] = kq * 4096 + rl * 128 + (((dh * 2 + i) ^ f3) << 5) + 8 * pp;
  auto w2_read = [&](int s, int lds) {
#pragma unroll
    for (int i = 0; i < 2; ++i) W2[s][i] = tr_frag(sm + lds + woff[i], 2048);
  };
  if (c_on) {
    auto load_ht = [&](auto HH, auto JJ) {
      constexpr unsigned h = decltype(HH)::value, j = decltype(JJ)::value, slot = h ? (j + 3) & 3 : j;
      dma16b<AUX_SC1, slot * 16384, j * (128 * S_LEN * 2) + h * 2048>(srd_ws, avo, ldsh);
      dma16b<AUX_SC1, slot * 16384 + 1024, j * (128 * S_LEN * 2) + h * 2048 + 8 * S_LEN * 2>(srd_ws, avo, ldsh);
    };
    load_ht(IC<0>{}, IC<0>{}); load_ht(IC<0>{}, IC<1>{});
    f32x4 csa[3];
#pragma unroll
    for (int i = 0; i < 3; ++i) csa[i] = (f32x4){0, 0, 0, 0};
#pragma unroll
    for (int c = 0; c < 8; ++c)
#pragma unroll
      for (int i = 0; i < 3; ++i) csa[i] = mfma16(ONES, W1[c][i], csa[i]);
    if (wm == 0 && g == 0) {
#pragma unroll
      for (int i = 0; i < 3; ++i) *(LAS float*)(sm + L_CS + (kp * 96 + wn * 48 + i * 16 + fr) * 4) = csa[i][0];
    }
    STAMP(7);
    int aoff[2];
#pragma unroll
    for (int i = 0; i < 2; ++i) aoff[i] = (kp * 32 + rl) * 128 + (((wm * 2 + (i ^ kp)) ^ f3) << 5) + 8 * pp;
    const bool st_on = wn ? q_ == g : fr == 0;
    const int st0 = wn ? L_SST + 512 + (kp * 64 + wm * 32 + kp * 16 + fr) * 16 : L_SST + (kp * 64 + wm * 32 + kp * 16 + g * 4) * 4;
    const int st1 = wn ? L_SST + 512 + (kp * 64 + wm * 32 + (kp ^ 1) * 16 + fr) * 16 : L_SST + (kp * 64 + wm * 32 + (kp ^ 1) * 16 + g * 4) * 4;
    const int ms = wm * 2 + kp;
    int rso[3];
#pragma unroll
    for (int jn = 0; jn < 3; ++jn) { const int nl = wn * 48 + jn * 16 + fr; rso[jn] = L_OUT1 + nl * 128 + (((ms * 2 + (g >> 1)) ^ (nl & 7)) << 4) + (g & 1) * 8; }
    int plo[2]; unsigned pgo[2];
#pragma unroll
    for (int i = 0; i < 2; ++i) {
      const int pc = t + 512 * i, sbk = pc >= 384 ? 1 : 0, rem = pc - sbk * 384, nl = rem >> 2, c4 = rem & 3;
      plo[i] = L_OUT1 + nl * 128 + (((sbk * 4 + c4) ^ (nl & 7)) << 4);
      pgo[i] = (pc < 768 && n0 + nl < F_DIM) ? WS_H1B + (((rt * 2 + sbk) * F_DIM + n0 + nl) * 32 + c4 * 8) * 2 : 0x80000000u;
      if (pc >= 768) plo[i] = L_OUT1;
    }
    f32x2 csv[3];
    static_for<2>([&](auto HH) {
      constexpr int h = decltype(HH)::value;
      unsigned fl[4];
      f32x4 acc[2][3], sta[2];
#pragma unroll
      for (int i = 0; i < 2; ++i) {
        sta[i] = (f32x4){0, 0, 0, 0};
#pragma unroll
        for (int jn = 0; jn < 3; ++jn) acc[i][jn] = (f32x4){0, 0, 0, 0};
      }
      auto g1_loop = [&](auto WN) {
        bf16x8_t A[2][4];
        auto half = [&](bf16x8_t (&Af)[4], const bf16x8_t (&Wf)[3], int u) {
#pragma unroll
          for (int i = 0; i < 2; ++i) {
#pragma unroll
            for (int jn = 0; jn < 3; ++jn) acc[i][jn] = mfma16(Af[2 * u + i], Wf[jn], acc[i][jn]);
            sta[i] = mfma16(Af[2 * u + i], decltype(WN)::value ? Af[2 * u + i] : ONES, sta[i]);
          }
        };
#define G1_ITER(j, N, N0)                                                                                    \
        if ((N0) != (N) && w == 0) WAIT_V(N0); else WAIT_V(N);                                               \
        if (h == 1 && (j) == 3) LBAR(); else RAW_BAR();                                                      \
               \
        if ((j) == h && t == 0) __hip_atomic_store((gu32*)(ws + (h == 0 ? WS_FLB + 16384 : WS_FLC)) + b * FLAG_STRIDE, epoch, RLX_AGENT); \
        if ((j) == 0) { if (h == 0) STAMP(16); else STAMP(37); }                                             \
        _Pragma("unroll") for (int u = 0; u < 2; ++u)                                                        \
          _Pragma("unroll") for (int i = 0; i < 2; ++i)                                                      \
            A[(j) & 1][2 * u + i] = tr_frag(sm + aoff[i] + (h ? ((j) + 3) & 3 : (j)) * 16384 + u * 8192, 2048); \
        if (h == 1 && (j) >= 2) { w2_read(2 * (j) - 4, L_W2A + (2 * (j) - 4) * 16384); w2_read(2 * (j) - 3, L_W2A + (2 * (j) - 3) * 16384); } \
        if ((j) > 0) { half(A[((j) - 1) & 1], W1[(j) > 0 ? 2 * (j) - 2 : 0], 0); interleave_mfma_ds<8>(); }  \
        SCHED();                                                                                             \
        if (h == 1 && (j) == 3) { load_w2(IC<5>{}, IC<L_W2A>{}); load_w2(IC<6>{}, IC<L_W2A + 16384>{}); }                            \
        if ((j) + 2 < 4) { load_ht(IC<h>{}, IC<(j) + 2>{}); load_w2(IC<3 * h + (j)>{}, IC<L_W2A + (3 * h + (j)) * 16384>{}); }       \
        if ((j) == 2) {                                                                                      \
          if (h == 0) {                                                                                      \
            if (w == 0) { _Pragma("unroll") for (int i = 0; i < 4; ++i) fl[i] = ld32_sc1(ws + WS_FLB + 16384, (lane + 64 * i) * (FLAG_STRIDE * 4)); } \
          } else {                                                                                           \
              \
            _Pragma("unroll") for (int v = 0; v < 2; ++v) {                                                  \
              int ks = kq * 11 + 5 + 3 * (w & 1) + v; ks = ks > 42 ? 42 : ks;                                \
              dma16<0>(p.w2 + (size_t)(ks * 32 + (jb >> 3) * 8) * D_DIM + d0, (lane >> 3) * (D_DIM * 2) + (lane & 7) * 16, lb + L_JUNKC); \
            }                                                                                                \
          }                                                                                                  \
        }                                                                                                    \
        SCHED();                                                                                             \
        if ((j) > 0) half(A[((j) - 1) & 1], W1[(j) > 0 ? 2 * (j) - 1 : 0], 1);                               \
        SCHED();
        if constexpr (h == 0) {
          G1_ITER(0, 2, 2) G1_ITER(1, 4, 5) G1_ITER(2, 6, 6) G1_ITER(3, 2, 6)
        } else {
          G1_ITER(0, 4, 4) G1_ITER(1, 4, 4) G1_ITER(2, 6, 7) G1_ITER(3, 4, 4)
        }
#undef G1_ITER
        half(A[1], W1[6], 0); half(A[1], W1[7], 1);
      };
      if (wn) g1_loop(std::integral_constant<bool, true>{}); else g1_loop(std::integral_constant<bool, false>{});
      if (h == 0) STAMP(9); else STAMP(38);
      constexpr int EX = h == 0 ? 7 * 16384 : L_EX1;
      if constexpr (h == 1) {
        WAIT_V(6);
        LBAR();
        if (w == 0) flc = ld32_sc1(ws + WS_FLC, flc_off);
        load_w2(IC<7>{}, IC<L_W2A + 2 * 16384>{}); load_w2(IC<8>{}, IC<L_W2A + 3 * 16384>{});
        w2_read(4, L_W2A + 4 * 16384);
      }
      if (h == 0) STAMP(25); else STAMP(28);
      if (st_on) { *(LAS f32x4*)(sm + st0) = sta[0]; *(LAS f32x4*)(sm + st1) = sta[1]; }
#pragma unroll
      for (int jn = 0; jn < 3; ++jn) *(LAS f32x4*)(sm + EX + w * 3072 + jn * 1024 + lane * 16) = acc[1][jn];
      if constexpr (h == 0) {
        if (w == 0) {
          WAIT_V(0);
          asm volatile("" : "+v"(fl[0]), "+v"(fl[1]), "+v"(fl[2]), "+v"(fl[3]));
          if (!__all((fl[0] == epoch) & (fl[1] == epoch) & (fl[2] == epoch) & (fl[3] == epoch))) wait_flags256(ws, WS_FLB + 16384, epoch, lane);
        }
      }
      LBAR();
      if (h == 0) STAMP(26); else STAMP(29);
      if constexpr (h == 0) {
        if (b == 0 && t == 0) __hip_atomic_store((gu32*)(ws + WS_EPOCH), epoch, RLX_AGENT);
        load_ht(IC<1>{}, IC<0>{});
      }
      if constexpr (h == 1) load_w2(IC<9>{}, IC<L_W2A + 4 * 16384>{});
      if (t < 64) {
        const LAS float *sp = (const LAS float*)(sm + L_SST) + t, *gp = (const LAS float*)(sm + L_SST + 512) + t * 4 + (t & 3);
        const float mean = (sp[0] + sp[64]) * (1.0f / D_DIM);
        const float var = fmaxf((gp[0] + gp[256]) * (1.0f / D_DIM) - mean * mean, 0.f);
        *(LAS float*)(sm + L_FST + t * 4) = -rbf(mean);
        *(LAS float*)(sm + L_FST + 256 + t * 4) = -1.44269504f * __builtin_amdgcn_rcpf(rbf(__builtin_amdgcn_sqrtf(rbf(rbf(var) + 1e-5f))));
      }
      if constexpr (h == 0) {
#pragma unroll
        for (int jn = 0; jn < 3; ++jn) {
          const int nl = wn * 48 + jn * 16 + fr;
          const float c = *(const LAS float*)(sm + L_CS + nl * 4) + *(const LAS float*)(sm + L_CS + (96 + nl) * 4);
          csv[jn] = (f32x2){c, c};
        }
      }
#pragma unroll
      for (int jn = 0; jn < 3; ++jn) acc[0][jn] += *(const LAS f32x4*)(sm + EX + (w ^ 4) * 3072 + jn * 1024 + lane * 16);
      if constexpr (h == 1) {
        if (w == 0) {
          WAIT_V(6);
          asm volatile("" : "+v"(flc));
          if (!__all(flc == epoch)) poll_flc(0);
        }
      }
      LBAR();
      if (h == 0) STAMP(27); else STAMP(31 + 5);
      if constexpr (h == 0) load_ht(IC<1>{}, IC<1>{});
      {
        const int ml = ms * 16 + g * 4;
        const f32x4 nmu = *(const LAS f32x4*)(sm + L_FST + ml * 4), zz = *(const LAS f32x4*)(sm + L_FST + 256 + ml * 4);
        const f32x2 nm0 = {nmu[0], nmu[1]}, nm1 = {nmu[2], nmu[3]}, z0 = {zz[0], zz[1]}, z1 = {zz[2], zz[3]};
        const f32x2 cL = {-1.44269504f, -1.44269504f};
        static_for<3>([&](auto JN) {
          constexpr int jn = decltype(JN)::value;
          if constexpr (h == 1) { SCHED(); load_h1(IC<0>{}, JN); SCHED(); }
          const f32x4 a = acc[0][jn];
          const f32x2 e0 = (nm0 * csv[jn] + (f32x2){a[0], a[1]}) * z0, e1 = (nm1 * csv[jn] + (f32x2){a[2], a[3]}) * z1;
          const f32x2 d0_ = (f32x2){__builtin_amdgcn_exp2f(e0[0]), __builtin_amdgcn_exp2f(e0[1])} * cL + cL;
          const f32x2 d1_ = (f32x2){__builtin_amdgcn_exp2f(e1[0]), __builtin_amdgcn_exp2f(e1[1])} * cL + cL;
          const f32x2 o0 = e0 * (f32x2){__builtin_amdgcn_rcpf(d0_[0]), __builtin_amdgcn_rcpf(d0_[1])};
          const f32x2 o1 = e1 * (f32x2){__builtin_amdgcn_rcpf(d1_[0]), __builtin_amdgcn_rcpf(d1_[1])};
          *(LAS u32x2*)(sm + rso[jn]) = (u32x2){pk_bf16(o0[0], o0[1]), pk_bf16(o1[0], o1[1])};
        });
      }
      LBAR();
      if (h == 0) STAMP(21); else STAMP(39);
      if constexpr (h == 0) load_w2(IC<2>{}, IC<L_W2A + 2 * 16384>{});
      if constexpr (h == 1) load_w2(IC<10>{}, IC<L_W2X>{});
      __builtin_amdgcn_raw_buffer_store_b128(*(const LAS u32x4*)(sm + plo[0]), rs, pgo[0], h * (32 * F_DIM * 64), AUX_SC1);
      if (w < 4) __builtin_amdgcn_raw_buffer_store_b128(*(const LAS u32x4*)(sm + plo[1]), rs, pgo[1], h * (32 * F_DIM * 64), AUX_SC1);
      WAIT_L0();
    });
  } else {
    WAIT_V(0);
    RAW_BAR();
    if (t == 0) __hip_atomic_store((gu32*)(ws + WS_FLB + 16384) + b * FLAG_STRIDE, epoch, RLX_AGENT);
    if (w == 0) wait_flags256(ws, WS_FLB + 16384, epoch, lane);
    RAW_BAR();
    static_for<5>([&](auto S) { load_w2(S, IC<L_W2A + decltype(S)::value * 16384>{}); });
    WAIT_V(0);
    RAW_BAR();
#pragma unroll
    for (int s = 0; s < 5; ++s)
#pragma unroll
      for (int i = 0; i < 2; ++i) W2[s][i] = tr_frag(sm + L_W2A + woff[i] + s * 16384, 2048);
    LBAR();
    static_for<5>([&](auto S) { load_w2(IC<5 + decltype(S)::value>{}, IC<L_W2A + decltype(S)::value * 16384>{}); });
    if (w == 0) poll_flc(0);
    RAW_BAR();
    load_h1(IC<0>{}, IC<0>{}); load_h1(IC<0>{}, IC<1>{}); load_h1(IC<0>{}, IC<2>{});
    load_w2(IC<10>{}, IC<L_W2X>{});
  }
  STAMP(22);
  {
    int boff[2];
#pragma unroll
    for (int i = 0; i < 2; ++i) boff[i] = kq * 4096 + rl * 64 + ((i ^ ((rl >> 2) & 1)) << 5) + 8 * pp;
    static_for<2>([&](auto HH) {
      constexpr int h = decltype(HH)::value;
      f32x4 acc[2][2];
#pragma unroll
      for (int i = 0; i < 2; ++i)
#pragma unroll
        for (int jm = 0; jm < 2; ++jm) acc[i][jm] = (f32x4){0, 0, 0, 0};
      bf16x8_t B[2][4];
      auto step2 = [&](const bf16x8_t (&Wf)[2], const bf16x8_t (&Bf)[4], int u) {
#pragma unroll
        for (int jm = 0; jm < 2; ++jm)
#pragma unroll
          for (int i = 0; i < 2; ++i) acc[i][jm] = mfma16(Wf[i], Bf[2 * u + jm], acc[i][jm]);
      };
#define G2_ITER(r, N, N0)                                                                                               \
      if ((N0) != (N) && w == 0 && c_on) WAIT_V(N0); else WAIT_V(N);          \
      LBAR();                                                                                                           \
      if ((r) == 0) { if (h == 0) STAMP(24); else STAMP(19); }                                                          \
      if ((r) == 1 && h == 0 && c_on && t == 0) __hip_atomic_store((gu32*)(ws + WS_FLC + 16384) + b * FLAG_STRIDE, epoch, RLX_AGENT); \
      _Pragma("unroll") for (int u = 0; u < 2; ++u)                                                                     \
        _Pragma("unroll") for (int i = 0; i < 2; ++i) B[(r) & 1][2 * u + i] = tr_frag(sm + boff[i] + (((r) + (h ? 0 : 3)) & 3) * 16384 + u * 2048, 1024); \
      if ((r) == 0 && h == 0) {                                                                                         \
        _Pragma("unroll") for (int s = 5; s < 10; ++s)                                                                  \
          _Pragma("unroll") for (int i = 0; i < 2; ++i) W2[s][i] = tr_frag(sm + L_W2A + woff[i] + (s - 5) * 16384, 2048); \
      }                                                                                                                 \
      if ((r) == 1 && h == 0) { _Pragma("unroll") for (int i = 0; i < 2; ++i) W2[10][i] = tr_frag(sm + L_W2X + woff[i], 2048); } \
      if ((r) > 0) { step2(W2[(r) > 0 ? 2 * (r) - 2 : 0], B[((r) - 1) & 1], 0);                                         \
        _Pragma("unroll") for (int i = 0; i < 4; ++i) { __builtin_amdgcn_sched_group_barrier(0x8, 1, 0); __builtin_amdgcn_sched_group_barrier(0x100, 2, 0); } } \
      SCHED();                                                                                                          \
      if ((r) + 3 <= 5) load_h1(IC<h>{}, IC<(r) + 3>{});                                                                            \
      if ((r) == 1 && h == 0) {                                                                                         \
                          \
        const int rr_ = (w & 3) * 16 + (lane >> 2);                                                                     \
        dma16<AUX_SC1>(ws + WS_HT + ((size_t)(d0 + (w & 3) * 16) * S_LEN + (w >> 2) * 1024 + mt * 32) * 2,              \
                       (lane >> 2) * (S_LEN * 2) + (((lane & 3) ^ (2 * ((rr_ >> 2) & 1))) << 4), lb + L_RES + w * 1024); \
      }                                                                                                                 \
      if ((r) == 5 && h == 0) { if (w == 0) flc = ld32_sc1(ws + WS_FLC + 16384, flc_off); }                             \
      SCHED();                                                                                                          \
      if ((r) > 0) step2(W2[(r) > 0 ? 2 * (r) - 1 : 0], B[((r) - 1) & 1], 1);                                           \
      SCHED();
      if constexpr (h == 0) {
        G2_ITER(0, 6, 6) G2_ITER(1, 2, 2) G2_ITER(2, 63, 63) G2_ITER(3, 5, 6) G2_ITER(4, 3, 3) G2_ITER(5, 0, 0)
      } else {
        G2_ITER(0, 4, 4) G2_ITER(1, 4, 4) G2_ITER(2, 4, 4) G2_ITER(3, 4, 4) G2_ITER(4, 2, 2) G2_ITER(5, 0, 0)
      }
#undef G2_ITER
      if (kq < 3) step2(W2[10], B[1], 0);
      if (h == 0) STAMP(13); else STAMP(20);
      const int ds = dh * 2 + (kq >> 1), ms = kq & 1;
      const int rr = ds * 16 + rl;
      const u32x2 ru = __builtin_bit_cast(u32x2, tr_read(sm + L_RES + h * 4096 + rr * 64 + ((ms ^ ((rl >> 2) & 1)) << 5) + 8 * pp));
#pragma unroll
      for (int i = 0; i < 2; ++i)
#pragma unroll
        for (int jm = 0; jm < 2; ++jm) *(LAS f32x4*)(sm + L_EX2 + ((dh * 4 + i * 2 + jm) * 4 + kq) * 1024 + lane * 16) = acc[i][jm];
      if constexpr (h == 0) {
        if (w == 0) {
          WAIT_V(0);
          asm volatile("" : "+v"(flc));
          if (!__all(flc == epoch)) poll_flc(1);
        }
      }
      LBAR();
      if constexpr (h == 0) { load_h1(IC<1>{}, IC<0>{}); load_h1(IC<1>{}, IC<1>{}); load_h1(IC<1>{}, IC<2>{}); STAMP(18); }
      const LAS char* e = sm + L_EX2 + ((dh * 4 + (kq >> 1) * 2 + ms) * 4) * 1024 + lane * 16;
      const f32x4 a = (*(const LAS f32x4*)(e) + *(const LAS f32x4*)(e + 1024)) + (*(const LAS f32x4*)(e + 2048) + *(const LAS f32x4*)(e + 3072));
      const u32x2 ov = {pk_bf16(bf_lo(ru[0]) + rbf(a[0]), bf_hi(ru[0]) + rbf(a[1])), pk_bf16(bf_lo(ru[1]) + rbf(a[2]), bf_hi(ru[1]) + rbf(a[3]))};
      *(u32x2*)(p.out + (size_t)(h * 1024 + mt * 32 + ms * 16 + fr) * D_DIM + d0 + ds * 16 + g * 4) = ov;
      if (h == 0) STAMP(14);
    });
  }
  asm volatile(".Lh3_code_end:");
  STAMP(23); CSTAMP(7);
}

extern "C" void kernel_launch(void* const* d_in, const int* in_sizes, int n_in, void* d_out, int out_size, void* d_ws, size_t ws_size,
                              hipStream_t stream) {
  Params p;
  p.q = (const u16*)d_in[0];
  p.k = (const u16*)d_in[1];
  p.v = (const u16*)d_in[2];
  p.shk = (const u16*)d_in[3];
  p.ssmk = (const u16*)d_in[4];
  p.ssmD = (const u16*)d_in[5];
  p.w1 = (const u16*)d_in[7];
  p.w2 = (const u16*)d_in[8];
  p.out = (u16*)d_out;
  p.ws = (char*)d_ws;
  (void)hipFuncSetAttribute((const void*)h3_fused, hipFuncAttributeMaxDynamicSharedMemorySize, LDS_BYTES);
  h3_fused<<<256, 512, LDS_BYTES, stream>>>(p);
}
```

```cpp
#include <hip/hip_runtime.h>
#include <hip/hip_bf16.h>
#include <stdint.h>
#include <type_traits>
#include <utility>

typedef unsigned short u16;
typedef short bf16x8 __attribute__((ext_vector_type(8)));
typedef short s16x4 __attribute__((ext_vector_type(4)));
typedef float f32x4 __attribute__((ext_vector_type(4)));
typedef float f32x2 __attribute__((ext_vector_type(2)));
typedef unsigned u32x4 __attribute__((ext_vector_type(4)));
typedef unsigned u32x2 __attribute__((ext_vector_type(2)));
typedef __bf16 bf16x2_t __attribute__((ext_vector_type(2)));
typedef __bf16 bf16x8_t __attribute__((ext_vector_type(8)));
typedef short v4i16_t __attribute__((ext_vector_type(4)));
#define LAS __attribute__((address_space(3)))
#define GAS __attribute__((address_space(1)))
typedef GAS unsigned gu32;
#define RLX_AGENT __ATOMIC_RELAXED, __HIP_MEMORY_SCOPE_AGENT

constexpr int S_LEN = 2048, D_DIM = 512, F_DIM = 1376;
constexpr int AUX_SC1 = 16;

constexpr unsigned WS_KVT = 0, WS_QT = 2u << 20, WS_HT = 4u << 20, WS_H1B = 6u << 20, WS_STQ = 12u << 20;
constexpr unsigned WS_FL1 = WS_STQ + 65536, WS_FLB = WS_FL1 + 16384, WS_FLC = WS_FLB + 2 * 16384, WS_EPOCH = WS_FLC + 2 * 16384;
constexpr unsigned WS_BYTES = 16u << 20;
constexpr int FLAG_STRIDE = 16;
constexpr unsigned SPIN_CAP = 1u << 18;

#define WAIT_V(n) asm volatile("s_waitcnt vmcnt(%0)" ::"n"(n) : "memory")
#define WAIT_L0() asm volatile("s_waitcnt lgkmcnt(0)" ::: "memory")
#define RAW_BAR() do { asm volatile("" ::: "memory"); __builtin_amdgcn_s_barrier(); asm volatile("" ::: "memory"); } while (0)
#define LBAR() do { WAIT_L0(); RAW_BAR(); } while (0)
#define SCHED() __builtin_amdgcn_sched_barrier(0)
template <int N> __device__ __forceinline__ void interleave_mfma_ds() {
#pragma unroll
  for (int i = 0; i < N; ++i) { __builtin_amdgcn_sched_group_barrier(0x8, 1, 0); __builtin_amdgcn_sched_group_barrier(0x100, 1, 0); }
}


__device__ __forceinline__ float bf_lo(unsigned x) { return __uint_as_float(x << 16); }
__device__ __forceinline__ float bf_hi(unsigned x) { return __uint_as_float(x & 0xffff0000u); }
__device__ __forceinline__ unsigned pk_bf16(float lo, float hi) {
  bf16x2_t v; v.x = (__bf16)lo; v.y = (__bf16)hi;
  return __builtin_bit_cast(unsigned, v);
}
__device__ __forceinline__ float rbf(float x) { return __uint_as_float(((unsigned)__builtin_bit_cast(u16, (__bf16)x)) << 16); }
__device__ __forceinline__ s16x4 tr_read(const LAS char* p) {
  return __builtin_bit_cast(s16x4, __builtin_amdgcn_ds_read_tr16_b64_v4i16((LAS v4i16_t*)p));
}
__device__ __forceinline__ bf16x8_t cat8(s16x4 a, s16x4 b) {
  bf16x8 r = __builtin_shufflevector(a, b, 0, 1, 2, 3, 4, 5, 6, 7);
  return __builtin_bit_cast(bf16x8_t, r);
}
__device__ __forceinline__ bf16x8_t tr_frag(const LAS char* p, int second) { return cat8(tr_read(p), tr_read(p + second)); }
__device__ __forceinline__ f32x4 mfma16(bf16x8_t a, bf16x8_t b, f32x4 c) {
  return __builtin_amdgcn_mfma_f32_16x16x32_bf16(a, b, c, 0, 0, 0);
}
template <int AUX>
__device__ __forceinline__ void dma16(const void* sbase, unsigned voff, unsigned lds) {
  if constexpr (AUX == AUX_SC1)
    asm volatile("s_mov_b32 m0, %2\n\ts_nop 0\n\tglobal_load_lds_dwordx4 %0, %1 sc1" ::"v"(voff), "s"(sbase), "s"(lds) : "memory");
  else
    asm volatile("s_mov_b32 m0, %2\n\ts_nop 0\n\tglobal_load_lds_dwordx4 %0, %1" ::"v"(voff), "s"(sbase), "s"(lds) : "memory");
}
template <int AUX, unsigned LC, unsigned SC>
__device__ __forceinline__ void dma16b(u32x4 srd, unsigned voff, unsigned ldsw) {
  unsigned tmp;
  if constexpr (AUX == AUX_SC1)
    asm volatile("s_add_u32 m0, %3, %4\n\ts_mov_b32 %0, %5\n\tbuffer_load_dwordx4 %1, %2, %0 offen sc1 lds"
                 : "=&s"(tmp) : "v"(voff), "s"(srd), "s"(ldsw), "n"(LC), "n"(SC) : "memory", "scc");
  else
    asm volatile("s_add_u32 m0, %3, %4\n\ts_mov_b32 %0, %5\n\tbuffer_load_dwordx4 %1, %2, %0 offen lds"
                 : "=&s"(tmp) : "v"(voff), "s"(srd), "s"(ldsw), "n"(LC), "n"(SC) : "memory", "scc");
}
__device__ __forceinline__ u32x4 make_srd(const void* p, unsigned bytes) {
  const unsigned long long a = (unsigned long long)p;
  return (u32x4){(unsigned)a, (unsigned)(a >> 32) & 0xffffu, bytes, 0x00020000u};
}
template <int V> using IC = std::integral_constant<int, V>;
__device__ __forceinline__ void dma4(const void* sbase, unsigned voff, unsigned lds) {
  asm volatile("s_mov_b32 m0, %2\n\ts_nop 0\n\tglobal_load_lds_dword %0, %1" ::"v"(voff), "s"(sbase), "s"(lds) : "memory");
}
__device__ __forceinline__ void dma4v(const void* vaddr, unsigned lds) {
  asm volatile("s_mov_b32 m0, %1\n\ts_nop 0\n\tglobal_load_lds_dword %0, off sc1" ::"v"(vaddr), "s"(lds) : "memory");
}
__device__ __forceinline__ u32x4 ld128_asm(const void* sbase, unsigned voff) {
  u32x4 r; asm volatile("global_load_dwordx4 %0, %1, %2" : "=v"(r) : "v"(voff), "s"(sbase) : "memory"); return r;
}
__device__ __forceinline__ u32x4 ld128_sc1(const void* sbase, unsigned voff) {
  u32x4 r; asm volatile("global_load_dwordx4 %0, %1, %2 sc1" : "=v"(r) : "v"(voff), "s"(sbase) : "memory"); return r;
}
__device__ __forceinline__ unsigned ld32_sc1(const void* sbase, unsigned voff) {
  unsigned r; asm volatile("global_load_dword %0, %1, %2 sc1" : "=v"(r) : "v"(voff), "s"(sbase) : "memory"); return r;
}
__device__ __forceinline__ unsigned ld16_asm(const void* sbase, unsigned voff) {
  unsigned r; asm volatile("global_load_ushort %0, %1, %2" : "=v"(r) : "v"(voff), "s"(sbase) : "memory"); return r;
}
__device__ __forceinline__ u32x2 ld64_asm(const void* sbase, unsigned voff) {
  u32x2 r; asm volatile("global_load_dwordx2 %0, %1, %2" : "=v"(r) : "v"(voff), "s"(sbase) : "memory"); return r;
}
__device__ __forceinline__ u32x2 ld64_sc1(const void* sbase, unsigned voff) {
  u32x2 r; asm volatile("global_load_dwordx2 %0, %1, %2 sc1" : "=v"(r) : "v"(voff), "s"(sbase) : "memory"); return r;
}
template <int CTRL>
__device__ __forceinline__ float dpp_add(float x) {
  return x + __builtin_bit_cast(float, __builtin_amdgcn_mov_dpp(__builtin_bit_cast(int, x), CTRL, 0xf, 0xf, true));
}
__device__ __forceinline__ float wave_sum(float x) {
  x = dpp_add<0xB1>(x); x = dpp_add<0x4E>(x); x = dpp_add<0x141>(x); x = dpp_add<0x140>(x);
  auto s = __builtin_amdgcn_permlane16_swap(__float_as_uint(x), __float_as_uint(x), false, false);
  x = __uint_as_float(s[0]) + __uint_as_float(s[1]);
  auto u = __builtin_amdgcn_permlane32_swap(__float_as_uint(x), __float_as_uint(x), false, false);
  return __uint_as_float(u[0]) + __uint_as_float(u[1]);
}
__device__ __forceinline__ u32x4 widen16(u32x2 X, u32x2 Y) {
  auto a = __builtin_amdgcn_permlane16_swap(X[0], Y[0], false, false);
  auto b = __builtin_amdgcn_permlane16_swap(X[1], Y[1], false, false);
  return (u32x4){a[0], b[0], a[1], b[1]};
}

struct Params {
  const u16 *q, *k, *v, *shk, *ssmk, *ssmD, *w1, *w2;
  u16* out;
  char* ws;
};

__device__ __forceinline__ void wait_flags256(const char* ws, unsigned off, unsigned epoch, int lane) {
  gu32* f = (gu32*)(ws + off) + lane * FLAG_STRIDE;
#pragma unroll 1
  for (unsigned spins = 0; spins < SPIN_CAP; ++spins) {
    unsigned a = __hip_atomic_load(f, RLX_AGENT), b = __hip_atomic_load(f + 64 * FLAG_STRIDE, RLX_AGENT);
    unsigned c = __hip_atomic_load(f + 128 * FLAG_STRIDE, RLX_AGENT), d = __hip_atomic_load(f + 192 * FLAG_STRIDE, RLX_AGENT);
    if (__all((a == epoch) & (b == epoch) & (c == epoch) & (d == epoch))) break;
    __builtin_amdgcn_s_sleep(1);
  }
  __builtin_amdgcn_fence(__ATOMIC_ACQUIRE, "wavefront");
}

constexpr int L_KT = 32768, L_VT = L_KT + 72 * 128, L_QT = L_VT + 8192, L_OKV = L_QT + 8192, L_OQ = L_OKV + 8192, L_SS = L_OQ + 8192;
constexpr int H_ROW = 1152, H_COPY = 4728, H_BYTES = 9344, X_BYTES = 4160;
constexpr int L_H = 0, L_X = L_H + 2 * H_BYTES, L_QD = L_X + 2 * X_BYTES;
constexpr int CY_BYTES = 257 * 16;
constexpr int L_CY = L_QD + 8192;
constexpr int L_JUNKA = 76800;
constexpr int L_W1 = 77824;
constexpr int L_W1B = 51712;
static_assert(L_CY + 4 * CY_BYTES <= L_W1B && L_W1B + 12288 <= L_JUNKA, "lds");
static_assert(L_CY + 4 * CY_BYTES <= L_JUNKA && L_JUNKA + 1024 <= L_W1 && L_W1 + 7 * 12288 <= 163840 && L_SS + 64 <= L_JUNKA, "lds");
static_assert(L_X + 2 * X_BYTES <= L_KT, "lds A/B overlap");
constexpr int L_W2A = 4 * 16384;
constexpr int L_CS = 147456;
constexpr int L_SST = L_CS + 1024;
constexpr int L_FST = L_SST + 2560;
constexpr int L_JUNKC = 159744;
constexpr int L_EX1 = 0;
constexpr int L_OUT1 = 2 * 16384;
static_assert(L_JUNKC + 1024 <= 163840, "lds");
constexpr int L_W2X = 9 * 16384;
constexpr int L_RES = 4 * 16384;
constexpr int L_EX2 = 5 * 16384;
constexpr int LDS_BYTES = 163840;

template <class F, int... I>
__device__ __forceinline__ void static_for_impl(F&& f, std::integer_sequence<int, I...>) { (f(std::integral_constant<int, I>{}), ...); }
template <int N, class F>
__device__ __forceinline__ void static_for(F&& f) { static_for_impl(f, std::make_integer_sequence<int, N>{}); }

template <int TS, int PAR, int H, class F>
__device__ __forceinline__ void conv_pass(const LAS char* xa, const LAS char* hb, bf16x8_t (&A)[8], bf16x8_t (&B)[3][4], f32x4 (&acc)[2][4], F&& job) {
  constexpr int U0 = H ? (TS ? 5 : 4) : (TS ? 1 : 0), U1 = H ? (TS ? 6 : 7) : (TS ? 2 : 3);
  constexpr int T[2] = {PAR ? U1 : U0, PAR ? U0 : U1};
  constexpr int NA = U1 + 1, A0 = H ? (TS ? 3 : 4) : 0;
  if constexpr (H == 1) {
#pragma unroll
    for (int p = 0; p < 4; ++p) B[0][p] = *(const LAS bf16x8_t*)(hb + p * H_ROW + 896);
  }
#pragma unroll
  for (int a = NA - 1; a >= A0; --a) A[a] = tr_frag(xa + 512 * a, 32);
  if constexpr (H == 1) {
#pragma unroll
    for (int p = 0; p < 4; ++p) B[1][p] = *(const LAS bf16x8_t*)(hb + p * H_ROW + 896 - 128);
  }
#pragma unroll
  for (int j = 0; j < 2; ++j)
#pragma unroll
    for (int p = 0; p < 4; ++p) acc[j][p] = (f32x4){0, 0, 0, 0};
  SCHED();
  static_for<NA>([&](auto BB) {
    constexpr int bb = decltype(BB)::value;
    if (bb + 2 < NA) {
#pragma unroll
      for (int p = 0; p < 4; ++p) B[(bb + 2) % 3][p] = *(const LAS bf16x8_t*)(hb + p * H_ROW + 896 - 128 * (bb + 2));
    }
#pragma unroll
    for (int p = 0; p < 4; ++p)
#pragma unroll
      for (int j = 0; j < 2; ++j)
        if (T[j] >= bb) acc[j][p] = mfma16(A[T[j] - bb], B[bb % 3][p], acc[j][p]);
    if (bb + 2 < NA) interleave_mfma_ds<4>();
    SCHED();
    job(std::integral_constant<int, H>{}, BB);
    SCHED();
  });
}

__global__ __launch_bounds__(512, 2) void h3_fused(Params p) {
  extern __shared__ __attribute__((aligned(16))) char smem_[];
  LAS char* sm = (LAS char*)smem_;
  const unsigned lb = (unsigned)(size_t)sm;
  const int b = blockIdx.x, t = threadIdx.x, lane = t & 63;
  const int w = __builtin_amdgcn_readfirstlane(t >> 6);
  const int x = b & 7, jb = b >> 3;
  const int g = lane >> 4, fr = lane & 15, q_ = fr >> 2, pp = lane & 3;
  const int rl = 4 * g + q_;
  char* ws = p.ws;
  const __amdgpu_buffer_rsrc_t rs = __builtin_amdgcn_make_buffer_rsrc(ws, 0, WS_BYTES, 0x00020000);

  const int dB = x * 64 + jb * 2;
  const int cb = w & 3, sl = 8 * (4 * (w >> 2) + g);
  const unsigned tvo = (lane >> 3) * (D_DIM * 2) + (lane & 7) * 16;
  const size_t tso = ((size_t)(jb * 64 + w * 8) * D_DIM + x * 64);
  dma16<0>(p.k + tso, tvo, lb + L_KT + 1024 + w * 1024);
  dma16<0>(p.v + tso, tvo, lb + L_VT + w * 1024);
  if (w == 0) {
    if (jb > 0) dma16<0>(p.k + tso - 8 * D_DIM, tvo, lb + L_KT);
    else *(LAS u32x4*)(sm + L_KT + lane * 16) = (u32x4){0, 0, 0, 0};
  }
  u32x2 wv = ld64_asm(p.shk + (size_t)(x * 64 + cb * 16) * 4, fr * 8);
  const bool c_on = jb < 30;
  const int panel = x * 2 + (jb >= 15 ? 1 : 0), ct = jb >= 15 ? jb - 15 : jb;
  const int m0 = panel * 128, n0 = ct * 96;
  unsigned w1o[6];
#pragma unroll
  for (int u = 0; u < 6; ++u) {
    const int idx = ((w & 1) * 6 + u) * 64 + lane, r = (idx * 43691) >> 19, cp = idx - r * 12;
    int col = n0 + (cp ^ (2 * ((r >> 2) & 1))) * 8;
    col = col > F_DIM - 8 ? F_DIM - 8 : col;
    w1o[u] = (r * F_DIM + col) * 2;
  }
  auto w1_half = [&](int chunk, int ldst, int step) {
#pragma unroll
    for (int u = 0; u < 6; ++u) dma16<0>(p.w1 + (size_t)chunk * 64 * F_DIM, w1o[u], lb + ldst + ((w & 1) * 6 + u) * step);
  };
  __builtin_amdgcn_s_sleep(8);
  dma16<0>(p.q + tso, tvo, lb + L_QT + w * 1024);
  u32x4 sq = ld128_asm(p.q + (size_t)(b * 8 + w) * D_DIM, lane * 16);
  unsigned ep0 = ld32_sc1(ws + WS_EPOCH, 0);
  unsigned dv0 = ld16_asm(p.ssmD + dB + (w >> 2), 0);
  if (w == 7) {
    unsigned long long pc; unsigned len;
    asm volatile("s_getpc_b64 %0\n.Lh3_ref_%=:\n\ts_mov_b32 %1, .Lh3_code_end-.Lh3_ref_%=" : "=s"(pc), "=s"(len));
    const unsigned nl = len >> 7;
    unsigned line = (unsigned)(jb * 8 + lane); line = line >= nl ? line - nl : line; line = line >= nl ? 0 : line;
    dma4((const void*)pc, line << 7, lb + L_JUNKA);
  }
  if (w == 6) {
    const char* a = ws + (size_t)(jb * 8 + (lane & 7)) * 65536;
    if (lane >= 8) a = (const char*)p.w1 + jb * 44032;
    if (lane >= 16) a = (const char*)p.w2 + jb * 44032;
    if (lane >= 24) a = (const char*)p.out + jb * 65536;
    if (lane >= 32) a = (const char*)p.ssmk + jb * 65536;
    dma4v(a, lb + L_JUNKA + 256);
  }
  if (w >= 6) WAIT_V(5); else WAIT_V(4);
  asm volatile("" : "+v"(wv));
  LBAR();
  const int tro = q_ * 128 + cb * 32 + 8 * pp + sl * 128;
  const int oo = (cb * 16 + fr) * 128 + (((sl >> 3) ^ (fr & 7)) << 4);
  {
    const u32x2 k0 = __builtin_bit_cast(u32x2, tr_read(sm + L_KT + 4 * 128 + tro));
    const u32x2 k1 = __builtin_bit_cast(u32x2, tr_read(sm + L_KT + 8 * 128 + tro));
    const u32x2 k2 = __builtin_bit_cast(u32x2, tr_read(sm + L_KT + 12 * 128 + tro));
    const u32x2 v0 = __builtin_bit_cast(u32x2, tr_read(sm + L_VT + tro));
    const u32x2 v1 = __builtin_bit_cast(u32x2, tr_read(sm + L_VT + 4 * 128 + tro));
    const float w0 = bf_lo(wv[0]), w1 = bf_hi(wv[0]), w2 = bf_lo(wv[1]), w3 = bf_hi(wv[1]);
    const float kx[11] = {bf_hi(k0[0]), bf_lo(k0[1]), bf_hi(k0[1]), bf_lo(k1[0]), bf_hi(k1[0]), bf_lo(k1[1]), bf_hi(k1[1]),
                          bf_lo(k2[0]), bf_hi(k2[0]), bf_lo(k2[1]), bf_hi(k2[1])};
    const float vx[8] = {bf_lo(v0[0]), bf_hi(v0[0]), bf_lo(v0[1]), bf_hi(v0[1]), bf_lo(v1[0]), bf_hi(v1[0]), bf_lo(v1[1]), bf_hi(v1[1])};
    float o[8];
#pragma unroll
    for (int e = 0; e < 8; ++e) o[e] = (kx[e] * w3 + kx[e + 1] * w2 + kx[e + 2] * w1 + kx[e + 3] * w0) * vx[e];
    *(LAS u32x4*)(sm + L_OKV + oo) = (u32x4){pk_bf16(o[0], o[1]), pk_bf16(o[2], o[3]), pk_bf16(o[4], o[5]), pk_bf16(o[6], o[7])};
  }
  LBAR();
  const int toff = (t >> 3) * 128 + (((t & 7) ^ ((t >> 3) & 7)) << 4);
  const unsigned tgo = ((x * 64 + (t >> 3)) * S_LEN + jb * 64 + (t & 7) * 8) * 2;
  __builtin_amdgcn_raw_buffer_store_b128(*(const LAS u32x4*)(sm + L_OKV + toff), rs, WS_KVT + tgo, 0, AUX_SC1);
  WAIT_V(1);
  asm volatile("" : "+v"(sq), "+v"(ep0), "+v"(dv0));
  RAW_BAR();
  const unsigned epoch = (unsigned)__builtin_amdgcn_readfirstlane(ep0) + 1u;
  const float Dv = bf_lo(dv0);
  {
    const u32x2 q0 = __builtin_bit_cast(u32x2, tr_read(sm + L_QT + tro));
    const u32x2 q1 = __builtin_bit_cast(u32x2, tr_read(sm + L_QT + 4 * 128 + tro));
    f32x2 xs[4];
#pragma unroll
    for (int i = 0; i < 4; ++i) xs[i] = (f32x2){bf_lo(sq[i]), bf_hi(sq[i])};
    const f32x2 s2 = (xs[0] + xs[1]) + (xs[2] + xs[3]);
    const float mean = wave_sum(s2[0] + s2[1]) * (1.0f / D_DIM);
    *(LAS u32x4*)(sm + L_OQ + oo) = (u32x4){q0[0], q0[1], q1[0], q1[1]};
    f32x2 vs = {0.f, 0.f};
#pragma unroll
    for (int i = 0; i < 4; ++i) { const f32x2 dd = xs[i] - mean; vs += dd * dd; }
    const float var = rbf(wave_sum(vs[0] + vs[1]) * (1.0f / D_DIM));
    if (lane == 0) *(LAS unsigned*)(sm + L_SS + w * 4) = pk_bf16(mean, __builtin_amdgcn_sqrtf(rbf(var + 1e-5f)));
  }
  LBAR();
  __builtin_amdgcn_raw_buffer_store_b128(*(const LAS u32x4*)(sm + L_OQ + toff), rs, WS_QT + tgo, 0, AUX_SC1);
  if (t < 2) __builtin_amdgcn_raw_buffer_store_b128(*(const LAS u32x4*)(sm + L_SS + t * 16), rs, WS_STQ + b * 32 + t * 16, 0, AUX_SC1);
  WAIT_V(0);
  LBAR();
  if (t == 0) __hip_atomic_store((gu32*)(ws + WS_FL1) + b * FLAG_STRIDE, epoch, RLX_AGENT);
  RAW_BAR();
  {
    const int hc = t >> 8, hw = t & 255;
    u32x4 hv = ld128_asm(p.ssmk + (size_t)dB * S_LEN, (hc * S_LEN + hw * 8) * 2);
    const unsigned o1 = min((unsigned)(b * 5504 + (w < 6 ? w : 5) * 1024), (unsigned)(D_DIM * F_DIM * 2 - 1024));
    dma16<0>((const char*)p.w1 + o1, lane * 16, lb + L_JUNKA);
    WAIT_V(1);
    asm volatile("" : "+v"(hv));
    LAS char* hb = sm + L_H + hc * H_BYTES + 1020 - 4 * hw;
    const unsigned h0 = __builtin_amdgcn_perm(hv[1], hv[3], 0x07060302u), h1 = __builtin_amdgcn_perm(hv[1], hv[3], 0x05040100u);
    const unsigned h2 = __builtin_amdgcn_perm(hv[0], hv[2], 0x07060302u), h3 = __builtin_amdgcn_perm(hv[0], hv[2], 0x05040100u);
    *(LAS unsigned*)(hb + 0 * H_ROW) = h0; *(LAS unsigned*)(hb + H_COPY + 0 * H_ROW) = h0;
    *(LAS unsigned*)(hb + 1 * H_ROW) = h1; *(LAS unsigned*)(hb + H_COPY + 1 * H_ROW) = h1;
    *(LAS unsigned*)(hb + 2 * H_ROW) = h2; *(LAS unsigned*)(hb + H_COPY + 2 * H_ROW) = h2;
    *(LAS unsigned*)(hb + 3 * H_ROW) = h3; *(LAS unsigned*)(hb + H_COPY + 3 * H_ROW) = h3;
    if (t < 256) {
      const int r = t >> 4;
      *(LAS u32x2*)(sm + L_H + (r >> 3) * H_BYTES + ((r >> 2) & 1) * H_COPY + (r & 3) * H_ROW + 1024 + (t & 15) * 8) = (u32x2){0, 0};
    } else if (t < 260) *(LAS u32x4*)(sm + L_X + ((t - 256) >> 1) * X_BYTES + (t & 1) * 16) = (u32x4){0, 0, 0, 0};
    else if (t < 264) *(LAS u32x4*)(sm + L_CY + (t - 260) * CY_BYTES) = (u32x4){0, 0, 0, 0};
  }

  const int c2 = w >> 2, ts = (w >> 1) & 1, par = w & 1;
  const int u00 = ts ? 1 : 0, u01 = ts ? 2 : 3, u10 = ts ? 5 : 4, u11 = ts ? 6 : 7;
  const int tf0 = par ? u01 : u00, tg0 = par ? u00 : u01, tf1 = par ? u11 : u10, tg1 = par ? u10 : u11;
  const int sa = tf0 * 256 + fr * 16 + g * 4, sb = tf1 * 256 + fr * 16 + g * 4;
  const int kp = w >> 2, wm = (w >> 1) & 1, wn = w & 1;
  const bf16x8_t ONES = __builtin_bit_cast(bf16x8_t, (u32x4){0x3f803f80u, 0x3f803f80u, 0x3f803f80u, 0x3f803f80u});
  bf16x8_t W1[8][3];
  int wo[3];
#pragma unroll
  for (int i = 0; i < 3; ++i) wo[i] = (kp * 32 + rl) * 192 + 8 * pp + (((wn * 3 + i) ^ ((rl >> 2) & 1)) << 5);
  if (w == 0) wait_flags256(ws, WS_FL1, epoch, lane);
  LBAR();
  {
    const int d = dB + c2;
    dma16<AUX_SC1>(ws + WS_KVT + ((size_t)d * S_LEN + (w & 3) * 512) * 2, lane * 16, lb + L_X + c2 * X_BYTES + 32 + (w & 3) * 1024);
    u32x2 qv0 = ld64_sc1(ws + WS_QT + (size_t)d * S_LEN * 2, sa * 2), qv1 = ld64_sc1(ws + WS_QT + (size_t)d * S_LEN * 2, sb * 2);
    u32x4 sv0 = ld128_sc1(ws + WS_STQ, sa * 4), sv1 = ld128_sc1(ws + WS_STQ, sb * 4);
    f32x4 acc[2][4];
    bf16x8_t A[8], B[3][4];
    const LAS char* xa = sm + L_X + c2 * X_BYTES + 8 + 64 * g + 8 * q_ + 8 * pp + 256 * par;
    const LAS char* hb = sm + L_H + c2 * H_BYTES + ((fr & 1) ? 0 : H_COPY) + 1016 - 8 * fr + 16 * g + 64 * par - 896;
#pragma unroll
    for (int p = 0; p < 4; ++p) { B[0][p] = *(const LAS bf16x8_t*)(hb + p * H_ROW + 896); B[1][p] = *(const LAS bf16x8_t*)(hb + p * H_ROW + 896 - 128); }
    WAIT_V(4);
    RAW_BAR();
    const int prev = g > 0 ? lane - 16 : (fr > 0 ? 47 + fr : -1);
    unsigned fl[4];
    auto w1_read = [&](auto CC) {
      constexpr int c = decltype(CC)::value;
#pragma unroll
      for (int i = 0; i < 3; ++i) W1[c][i] = tr_frag(sm + (c < 7 ? L_W1 + c * 12288 : L_W1B) + wo[i], 16 * 192);
    };
    auto w1_third = [&](int ch, int j3) {
      const int c = ch + (w >> 2), u = ((w >> 1) & 1) * 3 + j3;
      dma16<0>(p.w1 + (size_t)c * 64 * F_DIM, (w & 2) ? w1o[3 + j3] : w1o[j3], lb + (c < 7 ? L_W1 + c * 12288 : L_W1B) + ((w & 1) * 6 + u) * 1024);
    };
    auto job = [&](auto HH, auto BB) {
      constexpr int h = decltype(HH)::value, bb = decltype(BB)::value;
      if constexpr (h == 0) {
        if constexpr (bb < 3) w1_third(0, bb);
      } else {
        if constexpr (bb == 0) {
          int r2 = ((b * 43) >> 3) + w; r2 = r2 > F_DIM - 1 ? F_DIM - 1 : r2;
          dma16<0>(p.w2 + (size_t)r2 * D_DIM, lane * 16, lb + L_JUNKA);
        }
        if constexpr (bb == 2) {
          WAIT_V(5);
          RAW_BAR();
          if (t == 0) __hip_atomic_store((gu32*)(ws + WS_FLB) + b * FLAG_STRIDE, epoch, RLX_AGENT);
          if (t >= 64 && t < 68) {
            LAS char* c = sm + L_CY + (t - 64) * CY_BYTES;
            *(LAS f32x4*)c = *(const LAS f32x4*)(c + 256 * 16);
          }
        }
        if constexpr (bb < 3) {
          const int ch = 2 + (w >> 1);
#pragma unroll
          for (int u = 2 * bb; u < 2 * bb + 2; ++u) dma16<0>(p.w1 + (size_t)ch * 64 * F_DIM, w1o[u], lb + L_W1 + ch * 12288 + ((w & 1) * 6 + u) * 1024);
        }
        if constexpr (bb == 3) { w1_third(6, 0); w1_third(6, 1); w1_read(std::integral_constant<int, 0>{}); }
        if constexpr (bb == 4) { w1_third(6, 2); w1_read(std::integral_constant<int, 1>{}); }
        if constexpr (bb == 5) {
          WAIT_V(3);
          RAW_BAR();
          if (w == 0) {
#pragma unroll
            for (int i = 0; i < 4; ++i) fl[i] = ld32_sc1(ws + WS_FLB, (lane + 64 * i) * (FLAG_STRIDE * 4));
          }
          w1_read(std::integral_constant<int, 2>{}); w1_read(std::integral_constant<int, 3>{});
        }
        if constexpr (bb == 6) { w1_read(std::integral_constant<int, 4>{}); w1_read(std::integral_constant<int, 5>{}); }
      }
    };
    auto epilogue = [&](int tf, int tg, int s0, u32x2 qv, u32x4 sv, auto LAST) {
#pragma unroll
      for (int j = 0; j < 2; ++j) {
        acc[j][3][1] += acc[j][2][0];
        acc[j][3][2] += acc[j][2][1] + acc[j][1][0];
        acc[j][3][3] += (acc[j][2][2] + acc[j][1][1]) + acc[j][0][0];
        acc[j][0][1] += acc[j][2][3] + acc[j][1][2];
        acc[j][0][2] += acc[j][1][3];
      }
      LAS char* cy = sm + L_CY + (c2 * 2 + par) * CY_BYTES + 16 + lane * 16;
      *(LAS f32x4*)(cy + (tf & 3) * 1024) = acc[0][0];
      *(LAS f32x4*)(cy + (tg & 3) * 1024) = acc[1][0];
      *(LAS f32x4*)(sm + L_QD + w * 1024 + lane * 16) = acc[1][3];
      const u32x2 xv = *(const LAS u32x2*)(sm + L_X + c2 * X_BYTES + 32 + s0 * 2);
      if constexpr (decltype(LAST)::value) {
        WAIT_V(0);
        if (w == 0) {
          asm volatile("" : "+v"(fl[0]), "+v"(fl[1]), "+v"(fl[2]), "+v"(fl[3]));
          if (!__all((fl[0] == epoch) & (fl[1] == epoch) & (fl[2] == epoch) & (fl[3] == epoch))) wait_flags256(ws, WS_FLB, epoch, lane);
        }
      }
      LBAR();
      const LAS char* cy0 = sm + L_CY + (c2 * 2) * CY_BYTES + 16 + prev * 16 + (tf & 3) * 1024;
      const f32x4 pq = *(const LAS f32x4*)(sm + L_QD + (w ^ 1) * 1024 + lane * 16);
      const f32x4 ca = *(const LAS f32x4*)(cy0), cb_ = *(const LAS f32x4*)(cy0 + CY_BYTES);
      f32x4 y = acc[0][3] + pq;
      y[0] += ca[1] + cb_[1]; y[1] += ca[2] + cb_[2]; y[2] += ca[3] + cb_[3];
      const float xx[4] = {bf_lo(xv[0]), bf_hi(xv[0]), bf_lo(xv[1]), bf_hi(xv[1])};
      const float qx[4] = {bf_lo(qv[0]), bf_hi(qv[0]), bf_lo(qv[1]), bf_hi(qv[1])};
      float o[4];
#pragma unroll
      for (int e = 0; e < 4; ++e) o[e] = qx[e] + ((qx[e] - bf_lo(sv[e])) * __builtin_amdgcn_rcpf(bf_hi(sv[e]))) * (y[e] + xx[e] * Dv);
      __builtin_amdgcn_raw_buffer_store_b64((u32x2){pk_bf16(o[0], o[1]), pk_bf16(o[2], o[3])}, rs, WS_HT + (d * S_LEN + s0) * 2, 0, AUX_SC1);
    };
    if (ts == 0) { if (par == 0) conv_pass<0, 0, 0>(xa, hb, A, B, acc, job); else conv_pass<0, 1, 0>(xa, hb, A, B, acc, job); }
    else         { if (par == 0) conv_pass<1, 0, 0>(xa, hb, A, B, acc, job); else conv_pass<1, 1, 0>(xa, hb, A, B, acc, job); }
    WAIT_V(3);
    asm volatile("" : "+v"(qv0), "+v"(qv1), "+v"(sv0), "+v"(sv1));
    epilogue(tf0, tg0, sa, qv0, sv0, std::false_type{});
    if (ts == 0) { if (par == 0) conv_pass<0, 0, 1>(xa, hb, A, B, acc, job); else conv_pass<0, 1, 1>(xa, hb, A, B, acc, job); }
    else         { if (par == 0) conv_pass<1, 0, 1>(xa, hb, A, B, acc, job); else conv_pass<1, 1, 1>(xa, hb, A, B, acc, job); }
    epilogue(tf1, tg1, sb, qv1, sv1, std::true_type{});
  }
#pragma unroll
  for (int c = 6; c < 8; ++c)
#pragma unroll
    for (int i = 0; i < 3; ++i) W1[c][i] = tr_frag(sm + (c < 7 ? L_W1 + c * 12288 : L_W1B) + wo[i], 16 * 192);
  LBAR();

  const int rt = x * 2 + (jb >= 15 ? 1 : 0);
  const int mt = x * 4 + (jb >> 3), dt = jb & 7;
  const int d0 = dt * 64;
  const int kq = w >> 1, dh = w & 1;
  const int f3 = (rl >> 1) & 3;
  const u32x4 srd_ws = make_srd(ws, WS_BYTES), srd_w2 = make_srd(p.w2, F_DIM * D_DIM * 2);
  const unsigned ldsq = lb + kq * 4096 + (w & 1) * 2048, ldsh = lb + w * 2048;
  unsigned wvo[2], avo;
#pragma unroll
  for (int u = 0; u < 2; ++u) {
    const int r = ((w & 1) * 2 + u) * 8 + (lane >> 3), c = ((lane & 7) ^ (2 * ((r >> 1) & 3))) * 16;
    wvo[u] = (kq * 352 + r) * (D_DIM * 2) + d0 * 2 + c;
  }
  {
    const int ra = lane >> 3;
    avo = WS_HT + ((w * 16 + ra) * S_LEN + rt * 64) * 2 + (((lane & 7) ^ (2 * ((ra >> 1) & 3))) << 4);
  }
  auto load_w2 = [&](auto STEP, auto LDS) {
    constexpr unsigned step = decltype(STEP)::value, lds = decltype(LDS)::value;
    dma16b<0, lds, step * 32 * D_DIM * 2>(srd_w2, wvo[0], ldsq);
    dma16b<0, lds + 1024, step * 32 * D_DIM * 2>(srd_w2, wvo[1], ldsq);
  };
  bf16x8_t W2[11][2];
  const unsigned flc_off = (x + 8 * (15 * ((mt >> 1) & 1) + (lane < 15 ? lane : 0))) * (FLAG_STRIDE * 4);
  unsigned flc = 0;
  auto poll_flc = [&](int h) {
    gu32* f = (gu32*)(ws + WS_FLC + h * 16384 + flc_off);
#pragma unroll 1
    for (unsigned spins = 0; spins < SPIN_CAP; ++spins) {
      unsigned a = __hip_atomic_load(f, RLX_AGENT);
      if (__all(a == epoch)) break;
      __builtin_amdgcn_s_sleep(1);
    }
    __builtin_amdgcn_fence(__ATOMIC_ACQUIRE, "wavefront");
  };
  unsigned hvo[2];
#pragma unroll
  for (int u = 0; u < 2; ++u) {
    const int r = dh * 32 + u * 16 + (lane >> 2);
    hvo[u] = WS_H1B + (mt * F_DIM + kq * 352 + r) * 64 + (((lane & 3) ^ (2 * ((r >> 2) & 1))) << 4);
  }
  auto load_h1 = [&](auto HH, auto RR) {
    constexpr unsigned h = decltype(HH)::value, r = decltype(RR)::value, slot = (r + (h ? 0 : 3)) & 3;
    dma16b<AUX_SC1, slot * 16384, h * (32 * F_DIM * 64) + r * 4096>(srd_ws, hvo[0], ldsq);
    dma16b<AUX_SC1, slot * 16384 + 1024, h * (32 * F_DIM * 64) + r * 4096>(srd_ws, hvo[1], ldsq);
  };
  int woff[2];
#pragma unroll
  for (int i = 0; i < 2; ++i) woff[i] = kq * 4096 + rl * 128 + (((dh * 2 + i) ^ f3) << 5) + 8 * pp;
  auto w2_read = [&](int s, int lds) {
#pragma unroll
    for (int i = 0; i < 2; ++i) W2[s][i] = tr_frag(sm + lds + woff[i], 2048);
  };
  if (c_on) {
    auto load_ht = [&](auto HH, auto JJ) {
      constexpr unsigned h = decltype(HH)::value, j = decltype(JJ)::value, slot = h ? (j + 3) & 3 : j;
      dma16b<AUX_SC1, slot * 16384, j * (128 * S_LEN * 2) + h * 2048>(srd_ws, avo, ldsh);
      dma16b<AUX_SC1, slot * 16384 + 1024, j * (128 * S_LEN * 2) + h * 2048 + 8 * S_LEN * 2>(srd_ws, avo, ldsh);
    };
    load_ht(IC<0>{}, IC<0>{}); load_ht(IC<0>{}, IC<1>{});
    f32x4 csa[3];
#pragma unroll
    for (int i = 0; i < 3; ++i) csa[i] = (f32x4){0, 0, 0, 0};
#pragma unroll
    for (int c = 0; c < 8; ++c)
#pragma unroll
      for (int i = 0; i < 3; ++i) csa[i] = mfma16(ONES, W1[c][i], csa[i]);
    if (wm == 0 && g == 0) {
#pragma unroll
      for (int i = 0; i < 3; ++i) *(LAS float*)(sm + L_CS + (kp * 96 + wn * 48 + i * 16 + fr) * 4) = csa[i][0];
    }
    int aoff[2];
#pragma unroll
    for (int i = 0; i < 2; ++i) aoff[i] = (kp * 32 + rl) * 128 + (((wm * 2 + (i ^ kp)) ^ f3) << 5) + 8 * pp;
    const bool st_on = wn ? q_ == g : fr == 0;
    const int st0 = wn ? L_SST + 512 + (kp * 64 + wm * 32 + kp * 16 + fr) * 16 : L_SST + (kp * 64 + wm * 32 + kp * 16 + g * 4) * 4;
    const int st1 = wn ? L_SST + 512 + (kp * 64 + wm * 32 + (kp ^ 1) * 16 + fr) * 16 : L_SST + (kp * 64 + wm * 32 + (kp ^ 1) * 16 + g * 4) * 4;
    const int ms = wm * 2 + kp;
    int rso[3];
#pragma unroll
    for (int jn = 0; jn < 3; ++jn) { const int nl = wn * 48 + jn * 16 + fr; rso[jn] = L_OUT1 + nl * 128 + (((ms * 2 + (g >> 1)) ^ (nl & 7)) << 4) + (g & 1) * 8; }
    int plo[2]; unsigned pgo[2];
#pragma unroll
    for (int i = 0; i < 2; ++i) {
      const int pc = t + 512 * i, sbk = pc >= 384 ? 1 : 0, rem = pc - sbk * 384, nl = rem >> 2, c4 = rem & 3;
      plo[i] = L_OUT1 + nl * 128 + (((sbk * 4 + c4) ^ (nl & 7)) << 4);
      pgo[i] = (pc < 768 && n0 + nl < F_DIM) ? WS_H1B + (((rt * 2 + sbk) * F_DIM + n0 + nl) * 32 + c4 * 8) * 2 : 0x80000000u;
      if (pc >= 768) plo[i] = L_OUT1;
    }
    f32x2 csv[3];
    static_for<2>([&](auto HH) {
      constexpr int h = decltype(HH)::value;
      unsigned fl[4];
      f32x4 acc[2][3], sta[2];
#pragma unroll
      for (int i = 0; i < 2; ++i) {
        sta[i] = (f32x4){0, 0, 0, 0};
#pragma unroll
        for (int jn = 0; jn < 3; ++jn) acc[i][jn] = (f32x4){0, 0, 0, 0};
      }
      auto g1_loop = [&](auto WN) {
        bf16x8_t A[2][4];
        auto half = [&](bf16x8_t (&Af)[4], const bf16x8_t (&Wf)[3], int u) {
#pragma unroll
          for (int i = 0; i < 2; ++i) {
#pragma unroll
            for (int jn = 0; jn < 3; ++jn) acc[i][jn] = mfma16(Af[2 * u + i], Wf[jn], acc[i][jn]);
            sta[i] = mfma16(Af[2 * u + i], decltype(WN)::value ? Af[2 * u + i] : ONES, sta[i]);
          }
        };
#define G1_ITER(j, N, N0)                                                                                    \
        if ((N0) != (N) && w == 0) WAIT_V(N0); else WAIT_V(N);                                               \
        if (h == 1 && (j) == 3) LBAR(); else RAW_BAR();                                                      \
               \
        if ((j) == h && t == 0) __hip_atomic_store((gu32*)(ws + (h == 0 ? WS_FLB + 16384 : WS_FLC)) + b * FLAG_STRIDE, epoch, RLX_AGENT); \
        _Pragma("unroll") for (int u = 0; u < 2; ++u)                                                        \
          _Pragma("unroll") for (int i = 0; i < 2; ++i)                                                      \
            A[(j) & 1][2 * u + i] = tr_frag(sm + aoff[i] + (h ? ((j) + 3) & 3 : (j)) * 16384 + u * 8192, 2048); \
        if (h == 1 && (j) >= 2) { w2_read(2 * (j) - 4, L_W2A + (2 * (j) - 4) * 16384); w2_read(2 * (j) - 3, L_W2A + (2 * (j) - 3) * 16384); } \
        if ((j) > 0) { half(A[((j) - 1) & 1], W1[(j) > 0 ? 2 * (j) - 2 : 0], 0); interleave_mfma_ds<8>(); }  \
        SCHED();                                                                                             \
        if (h == 1 && (j) == 3) { load_w2(IC<5>{}, IC<L_W2A>{}); load_w2(IC<6>{}, IC<L_W2A + 16384>{}); }                            \
        if ((j) + 2 < 4) { load_ht(IC<h>{}, IC<(j) + 2>{}); load_w2(IC<3 * h + (j)>{}, IC<L_W2A + (3 * h + (j)) * 16384>{}); }       \
        if ((j) == 2) {                                                                                      \
          if (h == 0) {                                                                                      \
            if (w == 0) { _Pragma("unroll") for (int i = 0; i < 4; ++i) fl[i] = ld32_sc1(ws + WS_FLB + 16384, (lane + 64 * i) * (FLAG_STRIDE * 4)); } \
          } else {                                                                                           \
              \
            _Pragma("unroll") for (int v = 0; v < 2; ++v) {                                                  \
              int ks = kq * 11 + 5 + 3 * (w & 1) + v; ks = ks > 42 ? 42 : ks;                                \
              dma16<0>(p.w2 + (size_t)(ks * 32 + (jb >> 3) * 8) * D_DIM + d0, (lane >> 3) * (D_DIM * 2) + (lane & 7) * 16, lb + L_JUNKC); \
            }                                                                                                \
          }                                                                                                  \
        }                                                                                                    \
        SCHED();                                                                                             \
        if ((j) > 0) half(A[((j) - 1) & 1], W1[(j) > 0 ? 2 * (j) - 1 : 0], 1);                               \
        SCHED();
        if constexpr (h == 0) {
          G1_ITER(0, 2, 2) G1_ITER(1, 4, 5) G1_ITER(2, 6, 6) G1_ITER(3, 2, 6)
        } else {
          G1_ITER(0, 4, 4) G1_ITER(1, 4, 4) G1_ITER(2, 6, 7) G1_ITER(3, 4, 4)
        }
#undef G1_ITER
        half(A[1], W1[6], 0); half(A[1], W1[7], 1);
      };
      if (wn) g1_loop(std::integral_constant<bool, true>{}); else g1_loop(std::integral_constant<bool, false>{});
      constexpr int EX = h == 0 ? 7 * 16384 : L_EX1;
      if constexpr (h == 1) {
        WAIT_V(6);
        LBAR();
        if (w == 0) flc = ld32_sc1(ws + WS_FLC, flc_off);
        load_w2(IC<7>{}, IC<L_W2A + 2 * 16384>{}); load_w2(IC<8>{}, IC<L_W2A + 3 * 16384>{});
        w2_read(4, L_W2A + 4 * 16384);
      }
      if (st_on) { *(LAS f32x4*)(sm + st0) = sta[0]; *(LAS f32x4*)(sm + st1) = sta[1]; }
#pragma unroll
      for (int jn = 0; jn < 3; ++jn) *(LAS f32x4*)(sm + EX + w * 3072 + jn * 1024 + lane * 16) = acc[1][jn];
      if constexpr (h == 0) {
        if (w == 0) {
          WAIT_V(0);
          asm volatile("" : "+v"(fl[0]), "+v"(fl[1]), "+v"(fl[2]), "+v"(fl[3]));
          if (!__all((fl[0] == epoch) & (fl[1] == epoch) & (fl[2] == epoch) & (fl[3] == epoch))) wait_flags256(ws, WS_FLB + 16384, epoch, lane);
        }
      }
      LBAR();
      if constexpr (h == 0) {
        if (b == 0 && t == 0) __hip_atomic_store((gu32*)(ws + WS_EPOCH), epoch, RLX_AGENT);
        load_ht(IC<1>{}, IC<0>{});
      }
      if constexpr (h == 1) load_w2(IC<9>{}, IC<L_W2A + 4 * 16384>{});
      if (t < 64) {
        const LAS float *sp = (const LAS float*)(sm + L_SST) + t, *gp = (const LAS float*)(sm + L_SST + 512) + t * 4 + (t & 3);
        const float mean = (sp[0] + sp[64]) * (1.0f / D_DIM);
        const float var = fmaxf((gp[0] + gp[256]) * (1.0f / D_DIM) - mean * mean, 0.f);
        *(LAS float*)(sm + L_FST + t * 4) = -rbf(mean);
        *(LAS float*)(sm + L_FST + 256 + t * 4) = -1.44269504f * __builtin_amdgcn_rcpf(rbf(__builtin_amdgcn_sqrtf(rbf(rbf(var) + 1e-5f))));
      }
      if constexpr (h == 0) {
#pragma unroll
        for (int jn = 0; jn < 3; ++jn) {
          const int nl = wn * 48 + jn * 16 + fr;
          const float c = *(const LAS float*)(sm + L_CS + nl * 4) + *(const LAS float*)(sm + L_CS + (96 + nl) * 4);
          csv[jn] = (f32x2){c, c};
        }
      }
#pragma unroll
      for (int jn = 0; jn < 3; ++jn) acc[0][jn] += *(const LAS f32x4*)(sm + EX + (w ^ 4) * 3072 + jn * 1024 + lane * 16);
      if constexpr (h == 1) {
        if (w == 0) {
          WAIT_V(6);
          asm volatile("" : "+v"(flc));
          if (!__all(flc == epoch)) poll_flc(0);
        }
      }
      LBAR();
      if constexpr (h == 0) load_ht(IC<1>{}, IC<1>{});
      {
        const int ml = ms * 16 + g * 4;
        const f32x4 nmu = *(const LAS f32x4*)(sm + L_FST + ml * 4), zz = *(const LAS f32x4*)(sm + L_FST + 256 + ml * 4);
        const f32x2 nm0 = {nmu[0], nmu[1]}, nm1 = {nmu[2], nmu[3]}, z0 = {zz[0], zz[1]}, z1 = {zz[2], zz[3]};
        const f32x2 cL = {-1.44269504f, -1.44269504f};
        static_for<3>([&](auto JN) {
          constexpr int jn = decltype(JN)::value;
          if constexpr (h == 1) { SCHED(); load_h1(IC<0>{}, JN); SCHED(); }
          const f32x4 a = acc[0][jn];
          const f32x2 e0 = (nm0 * csv[jn] + (f32x2){a[0], a[1]}) * z0, e1 = (nm1 * csv[jn] + (f32x2){a[2], a[3]}) * z1;
          const f32x2 d0_ = (f32x2){__builtin_amdgcn_exp2f(e0[0]), __builtin_amdgcn_exp2f(e0[1])} * cL + cL;
          const f32x2 d1_ = (f32x2){__builtin_amdgcn_exp2f(e1[0]), __builtin_amdgcn_exp2f(e1[1])} * cL + cL;
          const f32x2 o0 = e0 * (f32x2){__builtin_amdgcn_rcpf(d0_[0]), __builtin_amdgcn_rcpf(d0_[1])};
          const f32x2 o1 = e1 * (f32x2){__builtin_amdgcn_rcpf(d1_[0]), __builtin_amdgcn_rcpf(d1_[1])};
          *(LAS u32x2*)(sm + rso[jn]) = (u32x2){pk_bf16(o0[0], o0[1]), pk_bf16(o1[0], o1[1])};
        });
      }
      LBAR();
      if constexpr (h == 0) load_w2(IC<2>{}, IC<L_W2A + 2 * 16384>{});
      if constexpr (h == 1) load_w2(IC<10>{}, IC<L_W2X>{});
      __builtin_amdgcn_raw_buffer_store_b128(*(const LAS u32x4*)(sm + plo[0]), rs, pgo[0], h * (32 * F_DIM * 64), AUX_SC1);
      if (w < 4) __builtin_amdgcn_raw_buffer_store_b128(*(const LAS u32x4*)(sm + plo[1]), rs, pgo[1], h * (32 * F_DIM * 64), AUX_SC1);
      WAIT_L0();
    });
  } else {
    WAIT_V(0);
    RAW_BAR();
    if (t == 0) __hip_atomic_store((gu32*)(ws + WS_FLB + 16384) + b * FLAG_STRIDE, epoch, RLX_AGENT);
    if (w == 0) wait_flags256(ws, WS_FLB + 16384, epoch, lane);
    RAW_BAR();
    static_for<5>([&](auto S) { load_w2(S, IC<L_W2A + decltype(S)::value * 16384>{}); });
    WAIT_V(0);
    RAW_BAR();
#pragma unroll
    for (int s = 0; s < 5; ++s)
#pragma unroll
      for (int i = 0; i < 2; ++i) W2[s][i] = tr_frag(sm + L_W2A + woff[i] + s * 16384, 2048);
    LBAR();
    static_for<5>([&](auto S) { load_w2(IC<5 + decltype(S)::value>{}, IC<L_W2A + decltype(S)::value * 16384>{}); });
    if (w == 0) poll_flc(0);
    RAW_BAR();
    load_h1(IC<0>{}, IC<0>{}); load_h1(IC<0>{}, IC<1>{}); load_h1(IC<0>{}, IC<2>{});
    load_w2(IC<10>{}, IC<L_W2X>{});
  }
  {
    int boff[2];
#pragma unroll
    for (int i = 0; i < 2; ++i) boff[i] = kq * 4096 + rl * 64 + ((i ^ ((rl >> 2) & 1)) << 5) + 8 * pp;
    static_for<2>([&](auto HH) {
      constexpr int h = decltype(HH)::value;
      f32x4 acc[2][2];
#pragma unroll
      for (int i = 0; i < 2; ++i)
#pragma unroll
        for (int jm = 0; jm < 2; ++jm) acc[i][jm] = (f32x4){0, 0, 0, 0};
      bf16x8_t B[2][4];
      auto step2 = [&](const bf16x8_t (&Wf)[2], const bf16x8_t (&Bf)[4], int u) {
#pragma unroll
        for (int jm = 0; jm < 2; ++jm)
#pragma unroll
          for (int i = 0; i < 2; ++i) acc[i][jm] = mfma16(Wf[i], Bf[2 * u + jm], acc[i][jm]);
      };
#define G2_ITER(r, N, N0)                                                                                               \
      if ((N0) != (N) && w == 0 && c_on) WAIT_V(N0); else WAIT_V(N);          \
      LBAR();                                                                                                           \
      if ((r) == 1 && h == 0 && c_on && t == 0) __hip_atomic_store((gu32*)(ws + WS_FLC + 16384) + b * FLAG_STRIDE, epoch, RLX_AGENT); \
      _Pragma("unroll") for (int u = 0; u < 2; ++u)                                                                     \
        _Pragma("unroll") for (int i = 0; i < 2; ++i) B[(r) & 1][2 * u + i] = tr_frag(sm + boff[i] + (((r) + (h ? 0 : 3)) & 3) * 16384 + u * 2048, 1024); \
      if ((r) == 0 && h == 0) {                                                                                         \
        _Pragma("unroll") for (int s = 5; s < 10; ++s)                                                                  \
          _Pragma("unroll") for (int i = 0; i < 2; ++i) W2[s][i] = tr_frag(sm + L_W2A + woff[i] + (s - 5) * 16384, 2048); \
      }                                                                                                                 \
      if ((r) == 1 && h == 0) { _Pragma("unroll") for (int i = 0; i < 2; ++i) W2[10][i] = tr_frag(sm + L_W2X + woff[i], 2048); } \
      if ((r) > 0) { step2(W2[(r) > 0 ? 2 * (r) - 2 : 0], B[((r) - 1) & 1], 0);                                         \
        _Pragma("unroll") for (int i = 0; i < 4; ++i) { __builtin_amdgcn_sched_group_barrier(0x8, 1, 0); __builtin_amdgcn_sched_group_barrier(0x100, 2, 0); } } \
      SCHED();                                                                                                          \
      if ((r) + 3 <= 5) load_h1(IC<h>{}, IC<(r) + 3>{});                                                                            \
      if ((r) == 1 && h == 0) {                                                                                         \
                          \
        const int rr_ = (w & 3) * 16 + (lane >> 2);                                                                     \
        dma16<AUX_SC1>(ws + WS_HT + ((size_t)(d0 + (w & 3) * 16) * S_LEN + (w >> 2) * 1024 + mt * 32) * 2,              \
                       (lane >> 2) * (S_LEN * 2) + (((lane & 3) ^ (2 * ((rr_ >> 2) & 1))) << 4), lb + L_RES + w * 1024); \
      }                                                                                                                 \
      if ((r) == 5 && h == 0) { if (w == 0) flc = ld32_sc1(ws + WS_FLC + 16384, flc_off); }                             \
      SCHED();                                                                                                          \
      if ((r) > 0) step2(W2[(r) > 0 ? 2 * (r) - 1 : 0], B[((r) - 1) & 1], 1);                                           \
      SCHED();
      if constexpr (h == 0) {
        G2_ITER(0, 6, 6) G2_ITER(1, 2, 2) G2_ITER(2, 63, 63) G2_ITER(3, 5, 6) G2_ITER(4, 3, 3) G2_ITER(5, 0, 0)
      } else {
        G2_ITER(0, 4, 4) G2_ITER(1, 4, 4) G2_ITER(2, 4, 4) G2_ITER(3, 4, 4) G2_ITER(4, 2, 2) G2_ITER(5, 0, 0)
      }
#undef G2_ITER
      if (kq < 3) step2(W2[10], B[1], 0);
      const int ds = dh * 2 + (kq >> 1), ms = kq & 1;
      const int rr = ds * 16 + rl;
      const u32x2 ru = __builtin_bit_cast(u32x2, tr_read(sm + L_RES + h * 4096 + rr * 64 + ((ms ^ ((rl >> 2) & 1)) << 5) + 8 * pp));
#pragma unroll
      for (int i = 0; i < 2; ++i)
#pragma unroll
        for (int jm = 0; jm < 2; ++jm) *(LAS f32x4*)(sm + L_EX2 + ((dh * 4 + i * 2 + jm) * 4 + kq) * 1024 + lane * 16) = acc[i][jm];
      if constexpr (h == 0) {
        if (w == 0) {
          WAIT_V(0);
          asm volatile("" : "+v"(flc));
          if (!__all(flc == epoch)) poll_flc(1);
        }
      }
      LBAR();
      if constexpr (h == 0) { load_h1(IC<1>{}, IC<0>{}); load_h1(IC<1>{}, IC<1>{}); load_h1(IC<1>{}, IC<2>{}); }
      const LAS char* e = sm + L_EX2 + ((dh * 4 + (kq >> 1) * 2 + ms) * 4) * 1024 + lane * 16;
      const f32x4 a = (*(const LAS f32x4*)(e) + *(const LAS f32x4*)(e + 1024)) + (*(const LAS f32x4*)(e + 2048) + *(const LAS f32x4*)(e + 3072));
      const u32x2 ov = {pk_bf16(bf_lo(ru[0]) + rbf(a[0]), bf_hi(ru[0]) + rbf(a[1])), pk_bf16(bf_lo(ru[1]) + rbf(a[2]), bf_hi(ru[1]) + rbf(a[3]))};
      *(u32x2*)(p.out + (size_t)(h * 1024 + mt * 32 + ms * 16 + fr) * D_DIM + d0 + ds * 16 + g * 4) = ov;
    });
  }
  asm volatile(".Lh3_code_end:");
}

extern "C" void kernel_launch(void* const* d_in, const int* in_sizes, int n_in, void* d_out, int out_size, void* d_ws, size_t ws_size,
                              hipStream_t stream) {
  Params p;
  p.q = (const u16*)d_in[0];
  p.k = (const u16*)d_in[1];
  p.v = (const u16*)d_in[2];
  p.shk = (const u16*)d_in[3];
  p.ssmk = (const u16*)d_in[4];
  p.ssmD = (const u16*)d_in[5];
  p.w1 = (const u16*)d_in[7];
  p.w2 = (const u16*)d_in[8];
  p.out = (u16*)d_out;
  p.ws = (char*)d_ws;
  (void)hipFuncSetAttribute((const void*)h3_fused, hipFuncAttributeMaxDynamicSharedMemorySize, LDS_BYTES);
  h3_fused<<<256, 512, LDS_BYTES, stream>>>(p);
}
```

```cpp
#include <hip/hip_runtime.h>
#include <hip/hip_bf16.h>
#include <stdint.h>
#include <type_traits>
#include <utility>

typedef unsigned short u16;
typedef short bf16x8 __attribute__((ext_vector_type(8)));
typedef short s16x4 __attribute__((ext_vector_type(4)));
typedef float f32x4 __attribute__((ext_vector_type(4)));
typedef float f32x2 __attribute__((ext_vector_type(2)));
typedef unsigned u32x4 __attribute__((ext_vector_type(4)));
typedef unsigned u32x2 __attribute__((ext_vector_type(2)));
typedef __bf16 bf16x2_t __attribute__((ext_vector_type(2)));
typedef __bf16 bf16x8_t __attribute__((ext_vector_type(8)));
typedef short v4i16_t __attribute__((ext_vector_type(4)));
#define LAS __attribute__((address_space(3)))
#define GAS __attribute__((address_space(1)))
typedef GAS unsigned gu32;
#define RLX_AGENT __ATOMIC_RELAXED, __HIP_MEMORY_SCOPE_AGENT

constexpr int S_LEN = 2048, D_DIM = 512, F_DIM = 1376;
constexpr int AUX_SC1 = 16;

constexpr unsigned WS_KVT = 0, WS_QT = 2u << 20, WS_HT = 4u << 20, WS_H1B = 6u << 20, WS_STQ = 12u << 20;
constexpr unsigned WS_FL1 = WS_STQ + 65536, WS_FLB = WS_FL1 + 16384, WS_FLC = WS_FLB + 2 * 16384, WS_EPOCH = WS_FLC + 2 * 16384;
constexpr unsigned WS_BYTES = 16u << 20;
constexpr int FLAG_STRIDE = 16;
constexpr unsigned SPIN_CAP = 1u << 18;

#define WAIT_V(n) asm volatile("s_waitcnt vmcnt(%0)" ::"n"(n) : "memory")
#define WAIT_L0() asm volatile("s_waitcnt lgkmcnt(0)" ::: "memory")
#define RAW_BAR() do { asm volatile("" ::: "memory"); __builtin_amdgcn_s_barrier(); asm volatile("" ::: "memory"); } while (0)
#define LBAR() do { WAIT_L0(); RAW_BAR(); } while (0)
#define SCHED() __builtin_amdgcn_sched_barrier(0)
template <int N> __device__ __forceinline__ void interleave_mfma_ds() {
#pragma unroll
  for (int i = 0; i < N; ++i) { __builtin_amdgcn_sched_group_barrier(0x8, 1, 0); __builtin_amdgcn_sched_group_barrier(0x100, 1, 0); }
}

__device__ __forceinline__ float bf_lo(unsigned x) { return __uint_as_float(x << 16); }
__device__ __forceinline__ float bf_hi(unsigned x) { return __uint_as_float(x & 0xffff0000u); }
__device__ __forceinline__ unsigned pk_bf16(float lo, float hi) {
  bf16x2_t v; v.x = (__bf16)lo; v.y = (__bf16)hi;
  return __builtin_bit_cast(unsigned, v);
}
__device__ __forceinline__ float rbf(float x) { return __uint_as_float(((unsigned)__builtin_bit_cast(u16, (__bf16)x)) << 16); }
__device__ __forceinline__ s16x4 tr_read(const LAS char* p) {
  return __builtin_bit_cast(s16x4, __builtin_amdgcn_ds_read_tr16_b64_v4i16((LAS v4i16_t*)p));
}
__device__ __forceinline__ bf16x8_t cat8(s16x4 a, s16x4 b) {
  bf16x8 r = __builtin_shufflevector(a, b, 0, 1, 2, 3, 4, 5, 6, 7);
  return __builtin_bit_cast(bf16x8_t, r);
}
__device__ __forceinline__ bf16x8_t tr_frag(const LAS char* p, int second) { return cat8(tr_read(p), tr_read(p + second)); }
__device__ __forceinline__ f32x4 mfma16(bf16x8_t a, bf16x8_t b, f32x4 c) {
  return __builtin_amdgcn_mfma_f32_16x16x32_bf16(a, b, c, 0, 0, 0);
}
template <int AUX>
__device__ __forceinline__ void dma16(const void* sbase, unsigned voff, unsigned lds) {
  if constexpr (AUX == AUX_SC1)
    asm volatile("s_mov_b32 m0, %2\n\ts_nop 0\n\tglobal_load_lds_dwordx4 %0, %1 sc1" ::"v"(voff), "s"(sbase), "s"(lds) : "memory");
  else
    asm volatile("s_mov_b32 m0, %2\n\ts_nop 0\n\tglobal_load_lds_dwordx4 %0, %1" ::"v"(voff), "s"(sbase), "s"(lds) : "memory");
}
template <int AUX, unsigned LC, unsigned SC>
__device__ __forceinline__ void dma16b(u32x4 srd, unsigned voff, unsigned ldsw) {
  unsigned tmp;
  if constexpr (AUX == AUX_SC1)
    asm volatile("s_add_u32 m0, %3, %4\n\ts_mov_b32 %0, %5\n\tbuffer_load_dwordx4 %1, %2, %0 offen sc1 lds"
                 : "=&s"(tmp) : "v"(voff), "s"(srd), "s"(ldsw), "n"(LC), "n"(SC) : "memory", "scc");
  else
    asm volatile("s_add_u32 m0, %3, %4\n\ts_mov_b32 %0, %5\n\tbuffer_load_dwordx4 %1, %2, %0 offen lds"
                 : "=&s"(tmp) : "v"(voff), "s"(srd), "s"(ldsw), "n"(LC), "n"(SC) : "memory", "scc");
}
__device__ __forceinline__ u32x4 make_srd(const void* p, unsigned bytes) {
  const unsigned long long a = (unsigned long long)p;
  return (u32x4){(unsigned)a, (unsigned)(a >> 32) & 0xffffu, bytes, 0x00020000u};
}
template <int V> using IC = std::integral_constant<int, V>;
__device__ __forceinline__ void dma4(const void* sbase, unsigned voff, unsigned lds) {
  asm volatile("s_mov_b32 m0, %2\n\ts_nop 0\n\tglobal_load_lds_dword %0, %1" ::"v"(voff), "s"(sbase), "s"(lds) : "memory");
}
__device__ __forceinline__ void dma4v(const void* vaddr, unsigned lds) {
  asm volatile("s_mov_b32 m0, %1\n\ts_nop 0\n\tglobal_load_lds_dword %0, off sc1" ::"v"(vaddr), "s"(lds) : "memory");
}
__device__ __forceinline__ u32x4 ld128_asm(const void* sbase, unsigned voff) {
  u32x4 r; asm volatile("global_load_dwordx4 %0, %1, %2" : "=v"(r) : "v"(voff), "s"(sbase) : "memory"); return r;
}
__device__ __forceinline__ u32x4 ld128_sc1(const void* sbase, unsigned voff) {
  u32x4 r; asm volatile("global_load_dwordx4 %0, %1, %2 sc1" : "=v"(r) : "v"(voff), "s"(sbase) : "memory"); return r;
}
__device__ __forceinline__ unsigned ld32_sc1(const void* sbase, unsigned voff) {
  unsigned r; asm volatile("global_load_dword %0, %1, %2 sc1" : "=v"(r) : "v"(voff), "s"(sbase) : "memory"); return r;
}
__device__ __forceinline__ unsigned ld16_asm(const void* sbase, unsigned voff) {
  unsigned r; asm volatile("global_load_ushort %0, %1, %2" : "=v"(r) : "v"(voff), "s"(sbase) : "memory"); return r;
}
__device__ __forceinline__ u32x2 ld64_asm(const void* sbase, unsigned voff) {
  u32x2 r; asm volatile("global_load_dwordx2 %0, %1, %2" : "=v"(r) : "v"(voff), "s"(sbase) : "memory"); return r;
}
__device__ __forceinline__ u32x2 ld64_sc1(const void* sbase, unsigned voff) {
  u32x2 r; asm volatile("global_load_dwordx2 %0, %1, %2 sc1" : "=v"(r) : "v"(voff), "s"(sbase) : "memory"); return r;
}
template <int CTRL>
__device__ __forceinline__ float dpp_add(float x) {
  return x + __builtin_bit_cast(float, __builtin_amdgcn_mov_dpp(__builtin_bit_cast(int, x), CTRL, 0xf, 0xf, true));
}
__device__ __forceinline__ float wave_sum(float x) {
  x = dpp_add<0xB1>(x); x = dpp_add<0x4E>(x); x = dpp_add<0x141>(x); x = dpp_add<0x140>(x);
  auto s = __builtin_amdgcn_permlane16_swap(__float_as_uint(x), __float_as_uint(x), false, false);
  x = __uint_as_float(s[0]) + __uint_as_float(s[1]);
  auto u = __builtin_amdgcn_permlane32_swap(__float_as_uint(x), __float_as_uint(x), false, false);
  return __uint_as_float(u[0]) + __uint_as_float(u[1]);
}
struct Params {
  const u16 *q, *k, *v, *shk, *ssmk, *ssmD, *w1, *w2;
  u16* out;
  char* ws;
};

__device__ __forceinline__ void wait_flags256(const char* ws, unsigned off, unsigned epoch, int lane) {
  gu32* f = (gu32*)(ws + off) + lane * FLAG_STRIDE;
#pragma unroll 1
  for (unsigned spins = 0; spins < SPIN_CAP; ++spins) {
    unsigned a = __hip_atomic_load(f, RLX_AGENT), b = __hip_atomic_load(f + 64 * FLAG_STRIDE, RLX_AGENT);
    unsigned c = __hip_atomic_load(f + 128 * FLAG_STRIDE, RLX_AGENT), d = __hip_atomic_load(f + 192 * FLAG_STRIDE, RLX_AGENT);
    if (__all((a == epoch) & (b == epoch) & (c == epoch) & (d == epoch))) break;
    __builtin_amdgcn_s_sleep(1);
  }
  __builtin_amdgcn_fence(__ATOMIC_ACQUIRE, "wavefront");
}

constexpr int L_KT = 32768, L_VT = L_KT + 72 * 128, L_QT = L_VT + 8192, L_OKV = L_QT + 8192, L_OQ = L_OKV + 8192, L_SS = L_OQ + 8192;
constexpr int H_ROW = 1152, H_COPY = 4728, H_BYTES = 9344, X_BYTES = 4160;
constexpr int L_H = 0, L_X = L_H + 2 * H_BYTES, L_QD = L_X + 2 * X_BYTES;
constexpr int CY_BYTES = 257 * 16;
constexpr int L_CY = L_QD + 8192;
constexpr int L_JUNKA = 76800;
constexpr int L_W1 = 77824;
constexpr int L_W1B = 51712;
static_assert(L_CY + 4 * CY_BYTES <= L_W1B && L_W1B + 12288 <= L_JUNKA, "lds");
static_assert(L_JUNKA + 1024 <= L_W1 && L_W1 + 7 * 12288 <= 163840 && L_SS + 64 <= L_JUNKA, "lds");
static_assert(L_X + 2 * X_BYTES <= L_KT, "lds A/B overlap");
constexpr int L_W2A = 4 * 16384;
constexpr int L_CS = 147456;
constexpr int L_SST = L_CS + 1024;
constexpr int L_FST = L_SST + 2560;
constexpr int L_JUNKC = 159744;
constexpr int L_EX1 = 0;
constexpr int L_OUT1 = 2 * 16384;
static_assert(L_JUNKC + 1024 <= 163840, "lds");
constexpr int L_W2X = 9 * 16384;
constexpr int L_RES = 4 * 16384;
constexpr int L_EX2 = 5 * 16384;
constexpr int LDS_BYTES = 163840;

template <class F, int... I>
__device__ __forceinline__ void static_for_impl(F&& f, std::integer_sequence<int, I...>) { (f(std::integral_constant<int, I>{}), ...); }
template <int N, class F>
__device__ __forceinline__ void static_for(F&& f) { static_for_impl(f, std::make_integer_sequence<int, N>{}); }

template <int TS, int PAR, int H, class F>
__device__ __forceinline__ void conv_pass(const LAS char* xa, const LAS char* hb, bf16x8_t (&A)[8], bf16x8_t (&B)[3][4], f32x4 (&acc)[2][4], F&& job) {
  constexpr int U0 = H ? (TS ? 5 : 4) : (TS ? 1 : 0), U1 = H ? (TS ? 6 : 7) : (TS ? 2 : 3);
  constexpr int T[2] = {PAR ? U1 : U0, PAR ? U0 : U1};
  constexpr int NA = U1 + 1, A0 = H ? (TS ? 3 : 4) : 0;
  if constexpr (H == 1) {
#pragma unroll
    for (int p = 0; p < 4; ++p) B[0][p] = *(const LAS bf16x8_t*)(hb + p * H_ROW + 896);
  }
#pragma unroll
  for (int a = NA - 1; a >= A0; --a) A[a] = tr_frag(xa + 512 * a, 32);
  if constexpr (H == 1) {
#pragma unroll
    for (int p = 0; p < 4; ++p) B[1][p] = *(const LAS bf16x8_t*)(hb + p * H_ROW + 896 - 128);
  }
#pragma unroll
  for (int j = 0; j < 2; ++j)
#pragma unroll
    for (int p = 0; p < 4; ++p) acc[j][p] = (f32x4){0, 0, 0, 0};
  SCHED();
  static_for<NA>([&](auto BB) {
    constexpr int bb = decltype(BB)::value;
    if (bb + 2 < NA) {
#pragma unroll
      for (int p = 0; p < 4; ++p) B[(bb + 2) % 3][p] = *(const LAS bf16x8_t*)(hb + p * H_ROW + 896 - 128 * (bb + 2));
    }
#pragma unroll
    for (int p = 0; p < 4; ++p)
#pragma unroll
      for (int j = 0; j < 2; ++j)
        if (T[j] >= bb) acc[j][p] = mfma16(A[T[j] - bb], B[bb % 3][p], acc[j][p]);
    if (bb + 2 < NA) interleave_mfma_ds<4>();
    SCHED();
    job(std::integral_constant<int, H>{}, BB);
    SCHED();
  });
}

__global__ __launch_bounds__(512, 2) void h3_fused(Params p) {
  extern __shared__ __attribute__((aligned(16))) char smem_[];
  LAS char* sm = (LAS char*)smem_;
  const unsigned lb = (unsigned)(size_t)sm;
  const int b = blockIdx.x, t = threadIdx.x, lane = t & 63;
  const int w = __builtin_amdgcn_readfirstlane(t >> 6);
  const int x = b & 7, jb = b >> 3;
  const int g = lane >> 4, fr = lane & 15, q_ = fr >> 2, pp = lane & 3;
  const int rl = 4 * g + q_;
  char* ws = p.ws;
  const __amdgpu_buffer_rsrc_t rs = __builtin_amdgcn_make_buffer_rsrc(ws, 0, WS_BYTES, 0x00020000);

  const int dB = x * 64 + jb * 2;
  const int cb = w & 3, sl = 8 * (4 * (w >> 2) + g);
  const unsigned tvo = (lane >> 3) * (D_DIM * 2) + (lane & 7) * 16;
  const size_t tso = ((size_t)(jb * 64 + w * 8) * D_DIM + x * 64);
  dma16<0>(p.k + tso, tvo, lb + L_KT + 1024 + w * 1024);
  dma16<0>(p.v + tso, tvo, lb + L_VT + w * 1024);
  if (w == 0) {
    if (jb > 0) dma16<0>(p.k + tso - 8 * D_DIM, tvo, lb + L_KT);
    else *(LAS u32x4*)(sm + L_KT + lane * 16) = (u32x4){0, 0, 0, 0};
  }
  u32x2 wv = ld64_asm(p.shk + (size_t)(x * 64 + cb * 16) * 4, fr * 8);
  const bool c_on = jb < 30;
  const int ct = jb >= 15 ? jb - 15 : jb, n0 = ct * 96;
  unsigned w1o[6];
#pragma unroll
  for (int u = 0; u < 6; ++u) {
    const int idx = ((w & 1) * 6 + u) * 64 + lane, r = (idx * 43691) >> 19, cp = idx - r * 12;
    int col = n0 + (cp ^ (2 * ((r >> 2) & 1))) * 8;
    col = col > F_DIM - 8 ? F_DIM - 8 : col;
    w1o[u] = (r * F_DIM + col) * 2;
  }
  __builtin_amdgcn_s_sleep(8);
  dma16<0>(p.q + tso, tvo, lb + L_QT + w * 1024);
  u32x4 sq = ld128_asm(p.q + (size_t)(b * 8 + w) * D_DIM, lane * 16);
  unsigned ep0 = ld32_sc1(ws + WS_EPOCH, 0);
  unsigned dv0 = ld16_asm(p.ssmD + dB + (w >> 2), 0);
  if (w == 7) {
    unsigned long long pc; unsigned len;
    asm volatile("s_getpc_b64 %0\n.Lh3_ref_%=:\n\ts_mov_b32 %1, .Lh3_code_end-.Lh3_ref_%=" : "=s"(pc), "=s"(len));
    const unsigned nl = len >> 7;
    unsigned line = (unsigned)(jb * 8 + lane); line = line >= nl ? line - nl : line; line = line >= nl ? 0 : line;
    dma4((const void*)pc, line << 7, lb + L_JUNKA);
  }
  if (w == 6) {
    const char* a = ws + (size_t)(jb * 8 + (lane & 7)) * 65536;
    if (lane >= 8) a = (const char*)p.w1 + jb * 44032;
    if (lane >= 16) a = (const char*)p.w2 + jb * 44032;
    if (lane >= 24) a = (const char*)p.out + jb * 65536;
    if (lane >= 32) a = (const char*)p.ssmk + jb * 65536;
    dma4v(a, lb + L_JUNKA + 256);
  }
  if (w >= 6) WAIT_V(5); else WAIT_V(4);
  asm volatile("" : "+v"(wv));
  LBAR();
  const int tro = q_ * 128 + cb * 32 + 8 * pp + sl * 128;
  const int oo = (cb * 16 + fr) * 128 + (((sl >> 3) ^ (fr & 7)) << 4);
  {
    const u32x2 k0 = __builtin_bit_cast(u32x2, tr_read(sm + L_KT + 4 * 128 + tro));
    const u32x2 k1 = __builtin_bit_cast(u32x2, tr_read(sm + L_KT + 8 * 128 + tro));
    const u32x2 k2 = __builtin_bit_cast(u32x2, tr_read(sm + L_KT + 12 * 128 + tro));
    const u32x2 v0 = __builtin_bit_cast(u32x2, tr_read(sm + L_VT + tro));
    const u32x2 v1 = __builtin_bit_cast(u32x2, tr_read(sm + L_VT + 4 * 128 + tro));
    const float w0 = bf_lo(wv[0]), w1 = bf_hi(wv[0]), w2 = bf_lo(wv[1]), w3 = bf_hi(wv[1]);
    const float kx[11] = {bf_hi(k0[0]), bf_lo(k0[1]), bf_hi(k0[1]), bf_lo(k1[0]), bf_hi(k1[0]), bf_lo(k1[1]), bf_hi(k1[1]),
                          bf_lo(k2[0]), bf_hi(k2[0]), bf_lo(k2[1]), bf_hi(k2[1])};
    const float vx[8] = {bf_lo(v0[0]), bf_hi(v0[0]), bf_lo(v0[1]), bf_hi(v0[1]), bf_lo(v1[0]), bf_hi(v1[0]), bf_lo(v1[1]), bf_hi(v1[1])};
    float o[8];
#pragma unroll
    for (int e = 0; e < 8; ++e) o[e] = (kx[e] * w3 + kx[e + 1] * w2 + kx[e + 2] * w1 + kx[e + 3] * w0) * vx[e];
    *(LAS u32x4*)(sm + L_OKV + oo) = (u32x4){pk_bf16(o[0], o[1]), pk_bf16(o[2], o[3]), pk_bf16(o[4], o[5]), pk_bf16(o[6], o[7])};
  }
  LBAR();
  const int toff = (t >> 3) * 128 + (((t & 7) ^ ((t >> 3) & 7)) << 4);
  const unsigned tgo = ((x * 64 + (t >> 3)) * S_LEN + jb * 64 + (t & 7) * 8) * 2;
  __builtin_amdgcn_raw_buffer_store_b128(*(const LAS u32x4*)(sm + L_OKV + toff), rs, WS_KVT + tgo, 0, AUX_SC1);
  WAIT_V(1);
  asm volatile("" : "+v"(sq), "+v"(ep0), "+v"(dv0));
  RAW_BAR();
  const unsigned epoch = (unsigned)__builtin_amdgcn_readfirstlane(ep0) + 1u;
  const float Dv = bf_lo(dv0);
  {
    const u32x2 q0 = __builtin_bit_cast(u32x2, tr_read(sm + L_QT + tro));
    const u32x2 q1 = __builtin_bit_cast(u32x2, tr_read(sm + L_QT + 4 * 128 + tro));
    f32x2 xs[4];
#pragma unroll
    for (int i = 0; i < 4; ++i) xs[i] = (f32x2){bf_lo(sq[i]), bf_hi(sq[i])};
    const f32x2 s2 = (xs[0] + xs[1]) + (xs[2] + xs[3]);
    const float mean = wave_sum(s2[0] + s2[1]) * (1.0f / D_DIM);
    *(LAS u32x4*)(sm + L_OQ + oo) = (u32x4){q0[0], q0[1], q1[0], q1[1]};
    f32x2 vs = {0.f, 0.f};
#pragma unroll
    for (int i = 0; i < 4; ++i) { const f32x2 dd = xs[i] - mean; vs += dd * dd; }
    const float var = rbf(wave_sum(vs[0] + vs[1]) * (1.0f / D_DIM));
    if (lane == 0) *(LAS unsigned*)(sm + L_SS + w * 4) = pk_bf16(mean, __builtin_amdgcn_sqrtf(rbf(var + 1e-5f)));
  }
  LBAR();
  __builtin_amdgcn_raw_buffer_store_b128(*(const LAS u32x4*)(sm + L_OQ + toff), rs, WS_QT + tgo, 0, AUX_SC1);
  if (t < 2) __builtin_amdgcn_raw_buffer_store_b128(*(const LAS u32x4*)(sm + L_SS + t * 16), rs, WS_STQ + b * 32 + t * 16, 0, AUX_SC1);
  WAIT_V(0);
  LBAR();
  if (t == 0) __hip_atomic_store((gu32*)(ws + WS_FL1) + b * FLAG_STRIDE, epoch, RLX_AGENT);
  RAW_BAR();
  {
    const int hc = t >> 8, hw = t & 255;
    u32x4 hv = ld128_asm(p.ssmk + (size_t)dB * S_LEN, (hc * S_LEN + hw * 8) * 2);
    const unsigned o1 = min((unsigned)(b * 5504 + (w < 6 ? w : 5) * 1024), (unsigned)(D_DIM * F_DIM * 2 - 1024));
    dma16<0>((const char*)p.w1 + o1, lane * 16, lb + L_JUNKA);
    WAIT_V(1);
    asm volatile("" : "+v"(hv));
    LAS char* hb = sm + L_H + hc * H_BYTES + 1020 - 4 * hw;
    const unsigned h0 = __builtin_amdgcn_perm(hv[1], hv[3], 0x07060302u), h1 = __builtin_amdgcn_perm(hv[1], hv[3], 0x05040100u);
    const unsigned h2 = __builtin_amdgcn_perm(hv[0], hv[2], 0x07060302u), h3 = __builtin_amdgcn_perm(hv[0], hv[2], 0x05040100u);
    *(LAS unsigned*)(hb + 0 * H_ROW) = h0; *(LAS unsigned*)(hb + H_COPY + 0 * H_ROW) = h0;
    *(LAS unsigned*)(hb + 1 * H_ROW) = h1; *(LAS unsigned*)(hb + H_COPY + 1 * H_ROW) = h1;
    *(LAS unsigned*)(hb + 2 * H_ROW) = h2; *(LAS unsigned*)(hb + H_COPY + 2 * H_ROW) = h2;
    *(LAS unsigned*)(hb + 3 * H_ROW) = h3; *(LAS unsigned*)(hb + H_COPY + 3 * H_ROW) = h3;
    if (t < 256) {
      const int r = t >> 4;
      *(LAS u32x2*)(sm + L_H + (r >> 3) * H_BYTES + ((r >> 2) & 1) * H_COPY + (r & 3) * H_ROW + 1024 + (t & 15) * 8) = (u32x2){0, 0};
    } else if (t < 260) *(LAS u32x4*)(sm + L_X + ((t - 256) >> 1) * X_BYTES + (t & 1) * 16) = (u32x4){0, 0, 0, 0};
    else if (t < 264) *(LAS u32x4*)(sm + L_CY + (t - 260) * CY_BYTES) = (u32x4){0, 0, 0, 0};
  }

  const int c2 = w >> 2, ts = (w >> 1) & 1, par = w & 1;
  const int u00 = ts ? 1 : 0, u01 = ts ? 2 : 3, u10 = ts ? 5 : 4, u11 = ts ? 6 : 7;
  const int tf0 = par ? u01 : u00, tg0 = par ? u00 : u01, tf1 = par ? u11 : u10, tg1 = par ? u10 : u11;
  const int sa = tf0 * 256 + fr * 16 + g * 4, sb = tf1 * 256 + fr * 16 + g * 4;
  const int kp = w >> 2, wm = (w >> 1) & 1, wn = w & 1;
  const bf16x8_t ONES = __builtin_bit_cast(bf16x8_t, (u32x4){0x3f803f80u, 0x3f803f80u, 0x3f803f80u, 0x3f803f80u});
  bf16x8_t W1[8][3];
  int wo[3];
#pragma unroll
  for (int i = 0; i < 3; ++i) wo[i] = (kp * 32 + rl) * 192 + 8 * pp + (((wn * 3 + i) ^ ((rl >> 2) & 1)) << 5);
  if (w == 0) wait_flags256(ws, WS_FL1, epoch, lane);
  LBAR();
  {
    const int d = dB + c2;
    dma16<AUX_SC1>(ws + WS_KVT + ((size_t)d * S_LEN + (w & 3) * 512) * 2, lane * 16, lb + L_X + c2 * X_BYTES + 32 + (w & 3) * 1024);
    u32x2 qv0 = ld64_sc1(ws + WS_QT + (size_t)d * S_LEN * 2, sa * 2), qv1 = ld64_sc1(ws + WS_QT + (size_t)d * S_LEN * 2, sb * 2);
    u32x4 sv0 = ld128_sc1(ws + WS_STQ, sa * 4), sv1 = ld128_sc1(ws + WS_STQ, sb * 4);
    f32x4 acc[2][4];
    bf16x8_t A[8], B[3][4];
    const LAS char* xa = sm + L_X + c2 * X_BYTES + 8 + 64 * g + 8 * q_ + 8 * pp + 256 * par;
    const LAS char* hb = sm + L_H + c2 * H_BYTES + ((fr & 1) ? 0 : H_COPY) + 1016 - 8 * fr + 16 * g + 64 * par - 896;
#pragma unroll
    for (int p = 0; p < 4; ++p) { B[0][p] = *(const LAS bf16x8_t*)(hb + p * H_ROW + 896); B[1][p] = *(const LAS bf16x8_t*)(hb + p * H_ROW + 896 - 128); }
    WAIT_V(4);
    RAW_BAR();
    const int prev = g > 0 ? lane - 16 : (fr > 0 ? 47 + fr : -1);
    unsigned fl[4];
    auto w1_read = [&](auto CC) {
      constexpr int c = decltype(CC)::value;
#pragma unroll
      for (int i = 0; i < 3; ++i) W1[c][i] = tr_frag(sm + (c < 7 ? L_W1 + c * 12288 : L_W1B) + wo[i], 16 * 192);
    };
    auto w1_third = [&](int ch, int j3) {
      const int c = ch + (w >> 2), u = ((w >> 1) & 1) * 3 + j3;
      dma16<0>(p.w1 + (size_t)c * 64 * F_DIM, (w & 2) ? w1o[3 + j3] : w1o[j3], lb + (c < 7 ? L_W1 + c * 12288 : L_W1B) + ((w & 1) * 6 + u) * 1024);
    };
    auto job = [&](auto HH, auto BB) {
      constexpr int h = decltype(HH)::value, bb = decltype(BB)::value;
      if constexpr (h == 0) {
        if constexpr (bb < 3) w1_third(0, bb);
      } else {
        if constexpr (bb == 0) {
          int r2 = ((b * 43) >> 3) + w; r2 = r2 > F_DIM - 1 ? F_DIM - 1 : r2;
          dma16<0>(p.w2 + (size_t)r2 * D_DIM, lane * 16, lb + L_JUNKA);
        }
        if constexpr (bb == 2) {
          WAIT_V(5);
          RAW_BAR();
          if (t == 0) __hip_atomic_store((gu32*)(ws + WS_FLB) + b * FLAG_STRIDE, epoch, RLX_AGENT);
          if (t >= 64 && t < 68) {
            LAS char* c = sm + L_CY + (t - 64) * CY_BYTES;
            *(LAS f32x4*)c = *(const LAS f32x4*)(c + 256 * 16);
          }
        }
        if constexpr (bb < 3) {
          const int ch = 2 + (w >> 1);
#pragma unroll
          for (int u = 2 * bb; u < 2 * bb + 2; ++u) dma16<0>(p.w1 + (size_t)ch * 64 * F_DIM, w1o[u], lb + L_W1 + ch * 12288 + ((w & 1) * 6 + u) * 1024);
        }
        if constexpr (bb == 3) { w1_third(6, 0); w1_third(6, 1); w1_read(std::integral_constant<int, 0>{}); }
        if constexpr (bb == 4) { w1_third(6, 2); w1_read(std::integral_constant<int, 1>{}); }
        if constexpr (bb == 5) {
          WAIT_V(3);
          RAW_BAR();
          if (w == 0) {
#pragma unroll
            for (int i = 0; i < 4; ++i) fl[i] = ld32_sc1(ws + WS_FLB, (lane + 64 * i) * (FLAG_STRIDE * 4));
          }
          w1_read(std::integral_constant<int, 2>{}); w1_read(std::integral_constant<int, 3>{});
        }
        if constexpr (bb == 6) { w1_read(std::integral_constant<int, 4>{}); w1_read(std::integral_constant<int, 5>{}); }
      }
    };
    auto epilogue = [&](int tf, int tg, int s0, u32x2 qv, u32x4 sv, auto LAST) {
#pragma unroll
      for (int j = 0; j < 2; ++j) {
        acc[j][3][1] += acc[j][2][0];
        acc[j][3][2] += acc[j][2][1] + acc[j][1][0];
        acc[j][3][3] += (acc[j][2][2] + acc[j][1][1]) + acc[j][0][0];
        acc[j][0][1] += acc[j][2][3] + acc[j][1][2];
        acc[j][0][2] += acc[j][1][3];
      }
      LAS char* cy = sm + L_CY + (c2 * 2 + par) * CY_BYTES + 16 + lane * 16;
      *(LAS f32x4*)(cy + (tf & 3) * 1024) = acc[0][0];
      *(LAS f32x4*)(cy + (tg & 3) * 1024) = acc[1][0];
      *(LAS f32x4*)(sm + L_QD + w * 1024 + lane * 16) = acc[1][3];
      const u32x2 xv = *(const LAS u32x2*)(sm + L_X + c2 * X_BYTES + 32 + s0 * 2);
      if constexpr (decltype(LAST)::value) {
        WAIT_V(0);
        if (w == 0) {
          asm volatile("" : "+v"(fl[0]), "+v"(fl[1]), "+v"(fl[2]), "+v"(fl[3]));
          if (!__all((fl[0] == epoch) & (fl[1] == epoch) & (fl[2] == epoch) & (fl[3] == epoch))) wait_flags256(ws, WS_FLB, epoch, lane);
        }
      }
      LBAR();
      const LAS char* cy0 = sm + L_CY + (c2 * 2) * CY_BYTES + 16 + prev * 16 + (tf & 3) * 1024;
      const f32x4 pq = *(const LAS f32x4*)(sm + L_QD + (w ^ 1) * 1024 + lane * 16);
      const f32x4 ca = *(const LAS f32x4*)(cy0), cb_ = *(const LAS f32x4*)(cy0 + CY_BYTES);
      f32x4 y = acc[0][3] + pq;
      y[0] += ca[1] + cb_[1]; y[1] += ca[2] + cb_[2]; y[2] += ca[3] + cb_[3];
      const float xx[4] = {bf_lo(xv[0]), bf_hi(xv[0]), bf_lo(xv[1]), bf_hi(xv[1])};
      const float qx[4] = {bf_lo(qv[0]), bf_hi(qv[0]), bf_lo(qv[1]), bf_hi(qv[1])};
      float o[4];
#pragma unroll
      for (int e = 0; e < 4; ++e) o[e] = qx[e] + ((qx[e] - bf_lo(sv[e])) * __builtin_amdgcn_rcpf(bf_hi(sv[e]))) * (y[e] + xx[e] * Dv);
      __builtin_amdgcn_raw_buffer_store_b64((u32x2){pk_bf16(o[0], o[1]), pk_bf16(o[2], o[3])}, rs, WS_HT + (d * S_LEN + s0) * 2, 0, AUX_SC1);
    };
    if (ts == 0) { if (par == 0) conv_pass<0, 0, 0>(xa, hb, A, B, acc, job); else conv_pass<0, 1, 0>(xa, hb, A, B, acc, job); }
    else         { if (par == 0) conv_pass<1, 0, 0>(xa, hb, A, B, acc, job); else conv_pass<1, 1, 0>(xa, hb, A, B, acc, job); }
    WAIT_V(3);
    asm volatile("" : "+v"(qv0), "+v"(qv1), "+v"(sv0), "+v"(sv1));
    epilogue(tf0, tg0, sa, qv0, sv0, std::false_type{});
    if (ts == 0) { if (par == 0) conv_pass<0, 0, 1>(xa, hb, A, B, acc, job); else conv_pass<0, 1, 1>(xa, hb, A, B, acc, job); }
    else         { if (par == 0) conv_pass<1, 0, 1>(xa, hb, A, B, acc, job); else conv_pass<1, 1, 1>(xa, hb, A, B, acc, job); }
    epilogue(tf1, tg1, sb, qv1, sv1, std::true_type{});
  }
#pragma unroll
  for (int c = 6; c < 8; ++c)
#pragma unroll
    for (int i = 0; i < 3; ++i) W1[c][i] = tr_frag(sm + (c < 7 ? L_W1 + c * 12288 : L_W1B) + wo[i], 16 * 192);
  LBAR();

  const int rt = x * 2 + (jb >= 15 ? 1 : 0);
  const int mt = x * 4 + (jb >> 3), dt = jb & 7;
  const int d0 = dt * 64;
  const int kq = w >> 1, dh = w & 1;
  const int f3 = (rl >> 1) & 3;
  const u32x4 srd_ws = make_srd(ws, WS_BYTES), srd_w2 = make_srd(p.w2, F_DIM * D_DIM * 2);
  const unsigned ldsq = lb + kq * 4096 + (w & 1) * 2048, ldsh = lb + w * 2048;
  unsigned wvo[2], avo;
#pragma unroll
  for (int u = 0; u < 2; ++u) {
    const int r = ((w & 1) * 2 + u) * 8 + (lane >> 3), c = ((lane & 7) ^ (2 * ((r >> 1) & 3))) * 16;
    wvo[u] = (kq * 352 + r) * (D_DIM * 2) + d0 * 2 + c;
  }
  {
    const int ra = lane >> 3;
    avo = WS_HT + ((w * 16 + ra) * S_LEN + rt * 64) * 2 + (((lane & 7) ^ (2 * ((ra >> 1) & 3))) << 4);
  }
  auto load_w2 = [&](auto STEP, auto LDS) {
    constexpr unsigned step = decltype(STEP)::value, lds = decltype(LDS)::value;
    dma16b<0, lds, step * 32 * D_DIM * 2>(srd_w2, wvo[0], ldsq);
    dma16b<0, lds + 1024, step * 32 * D_DIM * 2>(srd_w2, wvo[1], ldsq);
  };
  bf16x8_t W2[11][2];
  const unsigned flc_off = (x + 8 * (15 * ((mt >> 1) & 1) + (lane < 15 ? lane : 0))) * (FLAG_STRIDE * 4);
  unsigned flc = 0;
  auto poll_flc = [&](int h) {
    gu32* f = (gu32*)(ws + WS_FLC + h * 16384 + flc_off);
#pragma unroll 1
    for (unsigned spins = 0; spins < SPIN_CAP; ++spins) {
      unsigned a = __hip_atomic_load(f, RLX_AGENT);
      if (__all(a == epoch)) break;
      __builtin_amdgcn_s_sleep(1);
    }
    __builtin_amdgcn_fence(__ATOMIC_ACQUIRE, "wavefront");
  };
  unsigned hvo[2];
#pragma unroll
  for (int u = 0; u < 2; ++u) {
    const int r = dh * 32 + u * 16 + (lane >> 2);
    hvo[u] = WS_H1B + (mt * F_DIM + kq * 352 + r) * 64 + (((lane & 3) ^ (2 * ((r >> 2) & 1))) << 4);
  }
  auto load_h1 = [&](auto HH, auto RR) {
    constexpr unsigned h = decltype(HH)::value, r = decltype(RR)::value, slot = (r + (h ? 0 : 3)) & 3;
    dma16b<AUX_SC1, slot * 16384, h * (32 * F_DIM * 64) + r * 4096>(srd_ws, hvo[0], ldsq);
    dma16b<AUX_SC1, slot * 16384 + 1024, h * (32 * F_DIM * 64) + r * 4096>(srd_ws, hvo[1], ldsq);
  };
  int woff[2];
#pragma unroll
  for (int i = 0; i < 2; ++i) woff[i] = kq * 4096 + rl * 128 + (((dh * 2 + i) ^ f3) << 5) + 8 * pp;
  auto w2_read = [&](int s, int lds) {
#pragma unroll
    for (int i = 0; i < 2; ++i) W2[s][i] = tr_frag(sm + lds + woff[i], 2048);
  };
  if (c_on) {
    auto load_ht = [&](auto HH, auto JJ) {
      constexpr unsigned h = decltype(HH)::value, j = decltype(JJ)::value, slot = h ? (j + 3) & 3 : j;
      dma16b<AUX_SC1, slot * 16384, j * (128 * S_LEN * 2) + h * 2048>(srd_ws, avo, ldsh);
      dma16b<AUX_SC1, slot * 16384 + 1024, j * (128 * S_LEN * 2) + h * 2048 + 8 * S_LEN * 2>(srd_ws, avo, ldsh);
    };
    load_ht(IC<0>{}, IC<0>{}); load_ht(IC<0>{}, IC<1>{});
    f32x4 csa[3];
#pragma unroll
    for (int i = 0; i < 3; ++i) csa[i] = (f32x4){0, 0, 0, 0};
#pragma unroll
    for (int c = 0; c < 8; ++c)
#pragma unroll
      for (int i = 0; i < 3; ++i) csa[i] = mfma16(ONES, W1[c][i], csa[i]);
    if (wm == 0 && g == 0) {
#pragma unroll
      for (int i = 0; i < 3; ++i) *(LAS float*)(sm + L_CS + (kp * 96 + wn * 48 + i * 16 + fr) * 4) = csa[i][0];
    }
    int aoff[2];
#pragma unroll
    for (int i = 0; i < 2; ++i) aoff[i] = (kp * 32 + rl) * 128 + (((wm * 2 + (i ^ kp)) ^ f3) << 5) + 8 * pp;
    const bool st_on = wn ? q_ == g : fr == 0;
    const int st0 = wn ? L_SST + 512 + (kp * 64 + wm * 32 + kp * 16 + fr) * 16 : L_SST + (kp * 64 + wm * 32 + kp * 16 + g * 4) * 4;
    const int st1 = wn ? L_SST + 512 + (kp * 64 + wm * 32 + (kp ^ 1) * 16 + fr) * 16 : L_SST + (kp * 64 + wm * 32 + (kp ^ 1) * 16 + g * 4) * 4;
    const int ms = wm * 2 + kp;
    int rso[3];
#pragma unroll
    for (int jn = 0; jn < 3; ++jn) { const int nl = wn * 48 + jn * 16 + fr; rso[jn] = L_OUT1 + nl * 128 + (((ms * 2 + (g >> 1)) ^ (nl & 7)) << 4) + (g & 1) * 8; }
    int plo[2]; unsigned pgo[2];
#pragma unroll
    for (int i = 0; i < 2; ++i) {
      const int pc = t + 512 * i, sbk = pc >= 384 ? 1 : 0, rem = pc - sbk * 384, nl = rem >> 2, c4 = rem & 3;
      plo[i] = L_OUT1 + nl * 128 + (((sbk * 4 + c4) ^ (nl & 7)) << 4);
      pgo[i] = (pc < 768 && n0 + nl < F_DIM) ? WS_H1B + (((rt * 2 + sbk) * F_DIM + n0 + nl) * 32 + c4 * 8) * 2 : 0x80000000u;
      if (pc >= 768) plo[i] = L_OUT1;
    }
    f32x2 csv[3];
    static_for<2>([&](auto HH) {
      constexpr int h = decltype(HH)::value;
      unsigned fl[4];
      f32x4 acc[2][3], sta[2];
#pragma unroll
      for (int i = 0; i < 2; ++i) {
        sta[i] = (f32x4){0, 0, 0, 0};
#pragma unroll
        for (int jn = 0; jn < 3; ++jn) acc[i][jn] = (f32x4){0, 0, 0, 0};
      }
      auto g1_loop = [&](auto WN) {
        bf16x8_t A[2][4];
        auto half = [&](bf16x8_t (&Af)[4], const bf16x8_t (&Wf)[3], int u) {
#pragma unroll
          for (int i = 0; i < 2; ++i) {
#pragma unroll
            for (int jn = 0; jn < 3; ++jn) acc[i][jn] = mfma16(Af[2 * u + i], Wf[jn], acc[i][jn]);
            sta[i] = mfma16(Af[2 * u + i], decltype(WN)::value ? Af[2 * u + i] : ONES, sta[i]);
          }
        };
#define G1_ITER(j, N, N0)                                                                                    \
        if ((N0) != (N) && w == 0) WAIT_V(N0); else WAIT_V(N);                                               \
        if (h == 1 && (j) == 3) LBAR(); else RAW_BAR();                                                      \
               \
        if ((j) == h && t == 0) __hip_atomic_store((gu32*)(ws + (h == 0 ? WS_FLB + 16384 : WS_FLC)) + b * FLAG_STRIDE, epoch, RLX_AGENT); \
        _Pragma("unroll") for (int u = 0; u < 2; ++u)                                                        \
          _Pragma("unroll") for (int i = 0; i < 2; ++i)                                                      \
            A[(j) & 1][2 * u + i] = tr_frag(sm + aoff[i] + (h ? ((j) + 3) & 3 : (j)) * 16384 + u * 8192, 2048); \
        if (h == 1 && (j) >= 2) { w2_read(2 * (j) - 4, L_W2A + (2 * (j) - 4) * 16384); w2_read(2 * (j) - 3, L_W2A + (2 * (j) - 3) * 16384); } \
        if ((j) > 0) { half(A[((j) - 1) & 1], W1[(j) > 0 ? 2 * (j) - 2 : 0], 0); interleave_mfma_ds<8>(); }  \
        SCHED();                                                                                             \
        if (h == 1 && (j) == 3) { load_w2(IC<5>{}, IC<L_W2A>{}); load_w2(IC<6>{}, IC<L_W2A + 16384>{}); }                            \
        if ((j) + 2 < 4) { load_ht(IC<h>{}, IC<(j) + 2>{}); load_w2(IC<3 * h + (j)>{}, IC<L_W2A + (3 * h + (j)) * 16384>{}); }       \
        if ((j) == 2) {                                                                                      \
          if (h == 0) {                                                                                      \
            if (w == 0) { _Pragma("unroll") for (int i = 0; i < 4; ++i) fl[i] = ld32_sc1(ws + WS_FLB + 16384, (lane + 64 * i) * (FLAG_STRIDE * 4)); } \
          } else {                                                                                           \
              \
            _Pragma("unroll") for (int v = 0; v < 2; ++v) {                                                  \
              int ks = kq * 11 + 5 + 3 * (w & 1) + v; ks = ks > 42 ? 42 : ks;                                \
              dma16<0>(p.w2 + (size_t)(ks * 32 + (jb >> 3) * 8) * D_DIM + d0, (lane >> 3) * (D_DIM * 2) + (lane & 7) * 16, lb + L_JUNKC); \
            }                                                                                                \
          }                                                                                                  \
        }                                                                                                    \
        SCHED();                                                                                             \
        if ((j) > 0) half(A[((j) - 1) & 1], W1[(j) > 0 ? 2 * (j) - 1 : 0], 1);                               \
        SCHED();
        if constexpr (h == 0) {
          G1_ITER(0, 2, 2) G1_ITER(1, 4, 5) G1_ITER(2, 6, 6) G1_ITER(3, 2, 6)
        } else {
          G1_ITER(0, 4, 4) G1_ITER(1, 4, 4) G1_ITER(2, 6, 7) G1_ITER(3, 4, 4)
        }
#undef G1_ITER
        half(A[1], W1[6], 0); half(A[1], W1[7], 1);
      };
      if (wn) g1_loop(std::integral_constant<bool, true>{}); else g1_loop(std::integral_constant<bool, false>{});
      constexpr int EX = h == 0 ? 7 * 16384 : L_EX1;
      if constexpr (h == 1) {
        WAIT_V(6);
        LBAR();
        if (w == 0) flc = ld32_sc1(ws + WS_FLC, flc_off);
        load_w2(IC<7>{}, IC<L_W2A + 2 * 16384>{}); load_w2(IC<8>{}, IC<L_W2A + 3 * 16384>{});
        w2_read(4, L_W2A + 4 * 16384);
      }
      if (st_on) { *(LAS f32x4*)(sm + st0) = sta[0]; *(LAS f32x4*)(sm + st1) = sta[1]; }
#pragma unroll
      for (int jn = 0; jn < 3; ++jn) *(LAS f32x4*)(sm + EX + w * 3072 + jn * 1024 + lane * 16) = acc[1][jn];
      if constexpr (h == 0) {
        if (w == 0) {
          WAIT_V(0);
          asm volatile("" : "+v"(fl[0]), "+v"(fl[1]), "+v"(fl[2]), "+v"(fl[3]));
          if (!__all((fl[0] == epoch) & (fl[1] == epoch) & (fl[2] == epoch) & (fl[3] == epoch))) wait_flags256(ws, WS_FLB + 16384, epoch, lane);
        }
      }
      LBAR();
      if constexpr (h == 0) {
        if (b == 0 && t == 0) __hip_atomic_store((gu32*)(ws + WS_EPOCH), epoch, RLX_AGENT);
        load_ht(IC<1>{}, IC<0>{});
      }
      if constexpr (h == 1) load_w2(IC<9>{}, IC<L_W2A + 4 * 16384>{});
      if (t < 64) {
        const LAS float *sp = (const LAS float*)(sm + L_SST) + t, *gp = (const LAS float*)(sm + L_SST + 512) + t * 4 + (t & 3);
        const float mean = (sp[0] + sp[64]) * (1.0f / D_DIM);
        const float var = fmaxf((gp[0] + gp[256]) * (1.0f / D_DIM) - mean * mean, 0.f);
        *(LAS float*)(sm + L_FST + t * 4) = -rbf(mean);
        *(LAS float*)(sm + L_FST + 256 + t * 4) = -1.44269504f * __builtin_amdgcn_rcpf(rbf(__builtin_amdgcn_sqrtf(rbf(rbf(var) + 1e-5f))));
      }
      if constexpr (h == 0) {
#pragma unroll
        for (int jn = 0; jn < 3; ++jn) {
          const int nl = wn * 48 + jn * 16 + fr;
          const float c = *(const LAS float*)(sm + L_CS + nl * 4) + *(const LAS float*)(sm + L_CS + (96 + nl) * 4);
          csv[jn] = (f32x2){c, c};
        }
      }
#pragma unroll
      for (int jn = 0; jn < 3; ++jn) acc[0][jn] += *(const LAS f32x4*)(sm + EX + (w ^ 4) * 3072 + jn * 1024 + lane * 16);
      if constexpr (h == 1) {
        if (w == 0) {
          WAIT_V(6);
          asm volatile("" : "+v"(flc));
          if (!__all(flc == epoch)) poll_flc(0);
        }
      }
      LBAR();
      if constexpr (h == 0) load_ht(IC<1>{}, IC<1>{});
      {
        const int ml = ms * 16 + g * 4;
        const f32x4 nmu = *(const LAS f32x4*)(sm + L_FST + ml * 4), zz = *(const LAS f32x4*)(sm + L_FST + 256 + ml * 4);
        const f32x2 nm0 = {nmu[0], nmu[1]}, nm1 = {nmu[2], nmu[3]}, z0 = {zz[0], zz[1]}, z1 = {zz[2], zz[3]};
        const f32x2 cL = {-1.44269504f, -1.44269504f};
        static_for<3>([&](auto JN) {
          constexpr int jn = decltype(JN)::value;
          if constexpr (h == 1) { SCHED(); load_h1(IC<0>{}, JN); SCHED(); }
          const f32x4 a = acc[0][jn];
          const f32x2 e0 = (nm0 * csv[jn] + (f32x2){a[0], a[1]}) * z0, e1 = (nm1 * csv[jn] + (f32x2){a[2], a[3]}) * z1;
          const f32x2 d0_ = (f32x2){__builtin_amdgcn_exp2f(e0[0]), __builtin_amdgcn_exp2f(e0[1])} * cL + cL;
          const f32x2 d1_ = (f32x2){__builtin_amdgcn_exp2f(e1[0]), __builtin_amdgcn_exp2f(e1[1])} * cL + cL;
          const f32x2 o0 = e0 * (f32x2){__builtin_amdgcn_rcpf(d0_[0]), __builtin_amdgcn_rcpf(d0_[1])};
          const f32x2 o1 = e1 * (f32x2){__builtin_amdgcn_rcpf(d1_[0]), __builtin_amdgcn_rcpf(d1_[1])};
          *(LAS u32x2*)(sm + rso[jn]) = (u32x2){pk_bf16(o0[0], o0[1]), pk_bf16(o1[0], o1[1])};
        });
      }
      LBAR();
      if constexpr (h == 0) load_w2(IC<2>{}, IC<L_W2A + 2 * 16384>{});
      if constexpr (h == 1) load_w2(IC<10>{}, IC<L_W2X>{});
      __builtin_amdgcn_raw_buffer_store_b128(*(const LAS u32x4*)(sm + plo[0]), rs, pgo[0], h * (32 * F_DIM * 64), AUX_SC1);
      if (w < 4) __builtin_amdgcn_raw_buffer_store_b128(*(const LAS u32x4*)(sm + plo[1]), rs, pgo[1], h * (32 * F_DIM * 64), AUX_SC1);
      WAIT_L0();
    });
  } else {
    WAIT_V(0);
    RAW_BAR();
    if (t == 0) __hip_atomic_store((gu32*)(ws + WS_FLB + 16384) + b * FLAG_STRIDE, epoch, RLX_AGENT);
    if (w == 0) wait_flags256(ws, WS_FLB + 16384, epoch, lane);
    RAW_BAR();
    static_for<5>([&](auto S) { load_w2(S, IC<L_W2A + decltype(S)::value * 16384>{}); });
    WAIT_V(0);
    RAW_BAR();
#pragma unroll
    for (int s = 0; s < 5; ++s)
#pragma unroll
      for (int i = 0; i < 2; ++i) W2[s][i] = tr_frag(sm + L_W2A + woff[i] + s * 16384, 2048);
    LBAR();
    static_for<5>([&](auto S) { load_w2(IC<5 + decltype(S)::value>{}, IC<L_W2A + decltype(S)::value * 16384>{}); });
    if (w == 0) poll_flc(0);
    RAW_BAR();
    load_h1(IC<0>{}, IC<0>{}); load_h1(IC<0>{}, IC<1>{}); load_h1(IC<0>{}, IC<2>{});
    load_w2(IC<10>{}, IC<L_W2X>{});
  }
  {
    int boff[2];
#pragma unroll
    for (int i = 0; i < 2; ++i) boff[i] = kq * 4096 + rl * 64 + ((i ^ ((rl >> 2) & 1)) << 5) + 8 * pp;
    static_for<2>([&](auto HH) {
      constexpr int h = decltype(HH)::value;
      f32x4 acc[2][2];
#pragma unroll
      for (int i = 0; i < 2; ++i)
#pragma unroll
        for (int jm = 0; jm < 2; ++jm) acc[i][jm] = (f32x4){0, 0, 0, 0};
      bf16x8_t B[2][4];
      auto step2 = [&](const bf16x8_t (&Wf)[2], const bf16x8_t (&Bf)[4], int u) {
#pragma unroll
        for (int jm = 0; jm < 2; ++jm)
#pragma unroll
          for (int i = 0; i < 2; ++i) acc[i][jm] = mfma16(Wf[i], Bf[2 * u + jm], acc[i][jm]);
      };
#define G2_ITER(r, N, N0)                                                                                               \
      if ((N0) != (N) && w == 0 && c_on) WAIT_V(N0); else WAIT_V(N);          \
      LBAR();                                                                                                           \
      if ((r) == 1 && h == 0 && c_on && t == 0) __hip_atomic_store((gu32*)(ws + WS_FLC + 16384) + b * FLAG_STRIDE, epoch, RLX_AGENT); \
      _Pragma("unroll") for (int u = 0; u < 2; ++u)                                                                     \
        _Pragma("unroll") for (int i = 0; i < 2; ++i) B[(r) & 1][2 * u + i] = tr_frag(sm + boff[i] + (((r) + (h ? 0 : 3)) & 3) * 16384 + u * 2048, 1024); \
      if ((r) == 0 && h == 0) {                                                                                         \
        _Pragma("unroll") for (int s = 5; s < 10; ++s)                                                                  \
          _Pragma("unroll") for (int i = 0; i < 2; ++i) W2[s][i] = tr_frag(sm + L_W2A + woff[i] + (s - 5) * 16384, 2048); \
      }                                                                                                                 \
      if ((r) == 1 && h == 0) { _Pragma("unroll") for (int i = 0; i < 2; ++i) W2[10][i] = tr_frag(sm + L_W2X + woff[i], 2048); } \
      if ((r) > 0) { step2(W2[(r) > 0 ? 2 * (r) - 2 : 0], B[((r) - 1) & 1], 0);                                         \
        _Pragma("unroll") for (int i = 0; i < 4; ++i) { __builtin_amdgcn_sched_group_barrier(0x8, 1, 0); __builtin_amdgcn_sched_group_barrier(0x100, 2, 0); } } \
      SCHED();                                                                                                          \
      if ((r) + 3 <= 5) load_h1(IC<h>{}, IC<(r) + 3>{});                                                                            \
      if ((r) == 1 && h == 0) {                                                                                         \
                          \
        const int rr_ = (w & 3) * 16 + (lane >> 2);                                                                     \
        dma16<AUX_SC1>(ws + WS_HT + ((size_t)(d0 + (w & 3) * 16) * S_LEN + (w >> 2) * 1024 + mt * 32) * 2,              \
                       (lane >> 2) * (S_LEN * 2) + (((lane & 3) ^ (2 * ((rr_ >> 2) & 1))) << 4), lb + L_RES + w * 1024); \
      }                                                                                                                 \
      if ((r) == 5 && h == 0) { if (w == 0) flc = ld32_sc1(ws + WS_FLC + 16384, flc_off); }                             \
      SCHED();                                                                                                          \
      if ((r) > 0) step2(W2[(r) > 0 ? 2 * (r) - 1 : 0], B[((r) - 1) & 1], 1);                                           \
      SCHED();
      if constexpr (h == 0) {
        G2_ITER(0, 6, 6) G2_ITER(1, 2, 2) G2_ITER(2, 63, 63) G2_ITER(3, 5, 6) G2_ITER(4, 3, 3) G2_ITER(5, 0, 0)
      } else {
        G2_ITER(0, 4, 4) G2_ITER(1, 4, 4) G2_ITER(2, 4, 4) G2_ITER(3, 4, 4) G2_ITER(4, 2, 2) G2_ITER(5, 0, 0)
      }
#undef G2_ITER
      if (kq < 3) step2(W2[10], B[1], 0);
      const int ds = dh * 2 + (kq >> 1), ms = kq & 1;
      const int rr = ds * 16 + rl;
      const u32x2 ru = __builtin_bit_cast(u32x2, tr_read(sm + L_RES + h * 4096 + rr * 64 + ((ms ^ ((rl >> 2) & 1)) << 5) + 8 * pp));
#pragma unroll
      for (int i = 0; i < 2; ++i)
#pragma unroll
        for (int jm = 0; jm < 2; ++jm) *(LAS f32x4*)(sm + L_EX2 + ((dh * 4 + i * 2 + jm) * 4 + kq) * 1024 + lane * 16) = acc[i][jm];
      if constexpr (h == 0) {
        if (w == 0) {
          WAIT_V(0);
          asm volatile("" : "+v"(flc));
          if (!__all(flc == epoch)) poll_flc(1);
        }
      }
      LBAR();
      if constexpr (h == 0) { load_h1(IC<1>{}, IC<0>{}); load_h1(IC<1>{}, IC<1>{}); load_h1(IC<1>{}, IC<2>{}); }
      const LAS char* e = sm + L_EX2 + ((dh * 4 + (kq >> 1) * 2 + ms) * 4) * 1024 + lane * 16;
      const f32x4 a = (*(const LAS f32x4*)(e) + *(const LAS f32x4*)(e + 1024)) + (*(const LAS f32x4*)(e + 2048) + *(const LAS f32x4*)(e + 3072));
      const u32x2 ov = {pk_bf16(bf_lo(ru[0]) + rbf(a[0]), bf_hi(ru[0]) + rbf(a[1])), pk_bf16(bf_lo(ru[1]) + rbf(a[2]), bf_hi(ru[1]) + rbf(a[3]))};
      *(u32x2*)(p.out + (size_t)(h * 1024 + mt * 32 + ms * 16 + fr) * D_DIM + d0 + ds * 16 + g * 4) = ov;
    });
  }
  asm volatile(".Lh3_code_end:");
}

extern "C" void kernel_launch(void* const* d_in, const int* in_sizes, int n_in, void* d_out, int out_size, void* d_ws, size_t ws_size,
                              hipStream_t stream) {
  Params p;
  p.q = (const u16*)d_in[0];
  p.k = (const u16*)d_in[1];
  p.v = (const u16*)d_in[2];
  p.shk = (const u16*)d_in[3];
  p.ssmk = (const u16*)d_in[4];
  p.ssmD = (const u16*)d_in[5];
  p.w1 = (const u16*)d_in[7];
  p.w2 = (const u16*)d_in[8];
  p.out = (u16*)d_out;
  p.ws = (char*)d_ws;
  (void)hipFuncSetAttribute((const void*)h3_fused, hipFuncAttributeMaxDynamicSharedMemorySize, LDS_BYTES);
  h3_fused<<<256, 512, LDS_BYTES, stream>>>(p);
}
```
